```python
import torch

def _softmax(x, axis=-1):
    return torch.softmax(x, dim=axis)
def _gelu(x):
    return torch.nn.functional.gelu(x, approximate="tanh")
def _silu(x):
    return torch.nn.functional.silu(x)
def _sigmoid(x):
    return torch.sigmoid(x)
def _layernorm(x, axis=-1, eps=1e-5):
    m = x.mean(dim=axis, keepdim=True)
    v = x.var(dim=axis, keepdim=True, unbiased=False)
    return (x - m) / torch.sqrt(v + eps)
def _scatter_add(tgt, idx, src, axis=0):
    return tgt.clone().index_add_(axis, idx, src)
def _segment_sum(src, seg, n):
    out = torch.zeros((n,) + src.shape[1:], dtype=src.dtype)
    return out.index_add_(0, seg, src)
def _pad(x, pw):
    flat = tuple(v for pair in reversed(pw) for v in pair)
    return torch.nn.functional.pad(x, flat)

kernel_name = 'parallel_sum__ssm_conv_s5__mixer_channel_mlp__prompt'

def setup_inputs():

    _w = (lambda a: (lambda t: t.to(torch.bfloat16) if t.is_floating_point() else t)(torch.as_tensor(a)))
    def _A_setup():
        B, S, D = 1, 2048, 512
        P = D
        Lambda_re = -0.5 * (lambda _x: torch.exp(torch.as_tensor(_x)))((lambda *s: torch.randn(*s, dtype=torch.bfloat16) * 0.2)(P))
        Lambda_im = (lambda *s: torch.randn(*s, dtype=torch.bfloat16) * 0.8)(P) * 1.0
        B_re = (lambda *s: torch.randn(*s, dtype=torch.bfloat16) * 0.2)(P, D) * (D ** -0.5)
        B_im = (lambda *s: torch.randn(*s, dtype=torch.bfloat16) * 0.2)(P, D) * (D ** -0.5)
        C_re = (lambda *s: torch.randn(*s, dtype=torch.bfloat16) * 0.2)(D, P) * (P ** -0.5)
        C_im = (lambda *s: torch.randn(*s, dtype=torch.bfloat16) * 0.2)(D, P) * (P ** -0.5)
        log_dt = (lambda *s: torch.randn(*s, dtype=torch.bfloat16) * 0.2)(P) * 0.3 - 2.0
        Dskip = (lambda *s: torch.randn(*s, dtype=torch.bfloat16) * 0.05)(D)
        return {
            "x": _w((lambda *s: torch.randn(*s, dtype=torch.bfloat16) * 0.05)(B, S, D)),
            "Lambda_re": _w(Lambda_re),
            "Lambda_im": _w(Lambda_im),
            "B_re": _w(B_re),
            "B_im": _w(B_im),
            "C_re": _w(C_re),
            "C_im": _w(C_im),
            "log_dt": _w(log_dt),
            "Dskip": _w(Dskip),
        }
    _A_in = _A_setup()

    def _B_setup():
        B, S, D, F = 1, 2048, 512, 1376
        return {
            "x": _w((lambda *s: torch.randn(*s, dtype=torch.bfloat16) * 0.05)(B, S, D)),
            "w1": _w((lambda *s: torch.randn(*s, dtype=torch.bfloat16) * 1.6)(D, F) * (D ** -0.5)),
            "b1": _w((lambda *s: torch.randn(*s, dtype=torch.bfloat16) * 0.8)(F)),
            "w2": _w((lambda *s: torch.randn(*s, dtype=torch.bfloat16) * 0.05)(F, D) * (F ** -0.5)),
            "b2": _w((lambda *s: torch.randn(*s, dtype=torch.bfloat16) * 0.05)(D)),
        }
    _B_in = _B_setup()

    return {
        "A_x": _A_in["x"],
        "A_Lambda_re": _A_in["Lambda_re"],
        "A_Lambda_im": _A_in["Lambda_im"],
        "A_B_re": _A_in["B_re"],
        "A_B_im": _A_in["B_im"],
        "A_C_re": _A_in["C_re"],
        "A_C_im": _A_in["C_im"],
        "A_log_dt": _A_in["log_dt"],
        "A_Dskip": _A_in["Dskip"],
        "B_x": _B_in["x"],
        "B_w1": _B_in["w1"],
        "B_b1": _B_in["b1"],
        "B_w2": _B_in["w2"],
        "B_b2": _B_in["b2"],
    }

def reference(A_x, A_Lambda_re, A_Lambda_im, A_B_re, A_B_im, A_C_re, A_C_im, A_log_dt, A_Dskip, B_x, B_w1, B_b1, B_w2, B_b2):
    def _A_ref(x, Lambda_re, Lambda_im, B_re, B_im, C_re, C_im, log_dt, Dskip):
        Bsz, L, H = x.shape
        Lam = Lambda_re + 1j * Lambda_im
        Bmat = B_re + 1j * B_im
        Cmat = C_re + 1j * C_im
        dt = torch.exp(log_dt)
        Lam_bar = torch.exp(dt * Lam)
        B_bar = ((Lam_bar - 1.0) / Lam)[:, None] * Bmat
        Bu = torch.einsum("bld,pd->blp", x + 0j, B_bar)
        h = torch.zeros_like(Bu[:, 0, :])
        hs = []
        for t in range(L):
            h = Lam_bar[None, :] * h + Bu[:, t, :]
            hs.append(h)
        hseq = torch.stack(hs, 1)
        y = 2.0 * torch.einsum("blp,dp->bld", hseq, Cmat).real + x * Dskip[None, None, :]
        return y

    def _B_ref(x, w1, b1, w2, b2):
        y = _layernorm(x, axis=-1)
        h = _gelu(torch.einsum("bsd,df->bsf", y, w1) + b1)
        return torch.einsum("bsf,fd->bsd", h, w2) + b2

    x = A_x
    xn = _layernorm(x, axis=-1)
    return x + _A_ref(xn, A_Lambda_re, A_Lambda_im, A_B_re, A_B_im, A_C_re, A_C_im, A_log_dt, A_Dskip) + _B_ref(xn, B_w1, B_b1, B_w2, B_b2)
```

```cpp
#include <hip/hip_runtime.h>
#include <hip/hip_bf16.h>
#include <stdint.h>

#define LAS __attribute__((address_space(3)))
#define GAS __attribute__((address_space(1)))

typedef __attribute__((ext_vector_type(8))) short s16x8;
typedef __attribute__((ext_vector_type(4))) short s16x4;
typedef __attribute__((ext_vector_type(8))) __bf16 bf16x8;
typedef __attribute__((ext_vector_type(2))) __bf16 bf16x2;
typedef __attribute__((ext_vector_type(4))) float f32x4;
typedef __attribute__((ext_vector_type(2))) float f32x2;
typedef __attribute__((ext_vector_type(4))) unsigned u32x4;
typedef __attribute__((ext_vector_type(2))) unsigned u32x2;

namespace {

constexpr int SEQ = 2048, DM = 512, NP = 512, FF = 1376;
constexpr int KACT = FF + 2 * NP;
constexpr int ACT_TILE = KACT * 128;
constexpr size_t WS_ACT = 0;
constexpr size_t WS_ROWSTAT = 10u << 20;
constexpr size_t WS_COLSUM = WS_ROWSTAT + (64u << 10);
constexpr size_t WS_EG = WS_ROWSTAT + (128u << 10);
constexpr size_t WS_STAMP = 200u << 20;
constexpr float LN_EPS = 1e-5f;

#define STAMP(k, s) do {} while (0)

template <int N> __device__ __forceinline__ void wait_vm() { asm volatile("s_waitcnt vmcnt(%0)" ::"i"(N) : "memory"); }
__device__ __forceinline__ void wait_lgkm0() { __builtin_amdgcn_s_waitcnt(0xC07F); asm volatile("" ::: "memory"); }
template <int NM, int ND, int DPM = 1, int I = 0> __device__ __forceinline__ void interleave() {
    if constexpr (I * DPM < ND && I < NM) {
        __builtin_amdgcn_sched_group_barrier(0x008, 1, 0);
        __builtin_amdgcn_sched_group_barrier(0x100, (ND - I * DPM) < DPM ? (ND - I * DPM) : DPM, 0);
        interleave<NM, ND, DPM, I + 1>();
    } else if constexpr (I < NM) {
        __builtin_amdgcn_sched_group_barrier(0x008, NM - I, 0);
    }
}
__device__ __forceinline__ void bar() {
    __builtin_amdgcn_sched_barrier(0);
    asm volatile("" ::: "memory");
    __builtin_amdgcn_s_barrier();
    asm volatile("" ::: "memory");
    __builtin_amdgcn_sched_barrier(0);
}
__device__ __forceinline__ float bf_lo(unsigned u) { return __uint_as_float(u << 16); }
__device__ __forceinline__ float bf_hi(unsigned u) { return __uint_as_float(u & 0xffff0000u); }
__device__ __forceinline__ float bf2f(unsigned short h) { return __uint_as_float((unsigned)h << 16); }
__device__ __forceinline__ unsigned pk_bf16(float a, float b) { bf16x2 v = {(__bf16)a, (__bf16)b}; return __builtin_bit_cast(unsigned, v); }
__device__ __forceinline__ float rbf(float a) { return bf_lo(pk_bf16(a, a)); }
__device__ __forceinline__ f32x2 pfma(f32x2 a, f32x2 b, f32x2 c) { return __builtin_elementwise_fma(a, b, c); }
__device__ __forceinline__ f32x2 splat(float a) { return f32x2{a, a}; }
__device__ __forceinline__ f32x4 mfma(s16x8 a, s16x8 b, f32x4 c) {
    return __builtin_amdgcn_mfma_f32_16x16x32_bf16(__builtin_bit_cast(bf16x8, a), __builtin_bit_cast(bf16x8, b), c, 0, 0, 0);
}
__device__ __forceinline__ s16x8 lds_row(LAS char* p) { return *(LAS s16x8*)p; }
__device__ __forceinline__ s16x8 lds_tr(LAS char* p0, LAS char* p1) {
    s16x4 a = __builtin_amdgcn_ds_read_tr16_b64_v4i16((LAS s16x4*)p0);
    s16x4 b = __builtin_amdgcn_ds_read_tr16_b64_v4i16((LAS s16x4*)p1);
    return __builtin_shufflevector(a, b, 0, 1, 2, 3, 4, 5, 6, 7);
}
__device__ __forceinline__ void dma16(const GAS char* g, LAS char* l) {
    __builtin_amdgcn_global_load_lds((const GAS unsigned*)g, (LAS unsigned*)l, 16, 0, 0);
}
__device__ __forceinline__ float wave_sum(float v) {
#pragma unroll
    for (int m = 32; m >= 1; m >>= 1) v += __shfl_xor(v, m);
    return v;
}
struct cf { float r, i; };
__device__ __forceinline__ cf cmul(cf a, cf b) { return cf{a.r * b.r - a.i * b.i, a.r * b.i + a.i * b.r}; }
__device__ __forceinline__ cf cfma(cf a, cf b, cf c) { return cf{fmaf(a.r, b.r, fmaf(-a.i, b.i, c.r)), fmaf(a.r, b.i, fmaf(a.i, b.r, c.i))}; }

struct RowScale { float mb, r1, r12; };
__device__ __forceinline__ RowScale row_scale(float m, float v) {
    const float s1 = rbf(sqrtf(rbf(rbf(v) + LN_EPS)));
    const float r1 = 1.0f / s1;
    const float v2 = rbf(v * r1 * r1);
    const float s2 = rbf(sqrtf(rbf(v2 + LN_EPS)));
    return RowScale{rbf(m), r1, r1 / s2};
}

__global__ __launch_bounds__(256) void k_stats(const unsigned short* __restrict__ x, const unsigned short* __restrict__ bre,
                                               const unsigned short* __restrict__ bim, const unsigned short* __restrict__ w1, char* __restrict__ ws) {
    __shared__ float red[4][8];
    const int b = blockIdx.x, wave = threadIdx.x >> 6, lane = threadIdx.x & 63;
    STAMP(0, 0);
    float2* rowstat = (float2*)(ws + WS_ROWSTAT);
    float* colsum = (float*)(ws + WS_COLSUM);
#pragma unroll
    for (int i = 0; i < 2; ++i) {
        const int row = 8 * b + 2 * wave + i;
        const u32x4 v = *(const u32x4*)(x + row * DM + lane * 8);
        float e[8];
#pragma unroll
        for (int j = 0; j < 4; ++j) { e[2 * j] = bf_lo(v[j]); e[2 * j + 1] = bf_hi(v[j]); }
        float s = 0;
#pragma unroll
        for (int j = 0; j < 8; ++j) s += e[j];
        const float m = wave_sum(s) * (1.0f / DM);
        float q = 0;
#pragma unroll
        for (int j = 0; j < 8; ++j) q += (e[j] - m) * (e[j] - m);
        const float var = wave_sum(q) * (1.0f / DM);
        if (lane == 0) rowstat[row] = make_float2(m, var);
    }
    {
        const int r = 4 * b + wave;
        const unsigned short* src = r < NP ? bre + r * DM : bim + (r - NP) * DM;
        const u32x4 v = *(const u32x4*)(src + lane * 8);
        float s = 0;
#pragma unroll
        for (int j = 0; j < 4; ++j) s += bf_lo(v[j]) + bf_hi(v[j]);
        s = wave_sum(s);
        if (lane == 0) colsum[r] = s;
    }
    if (b < FF / 8) {
        float a[8] = {};
#pragma unroll
        for (int i = 0; i < 2; ++i) {
            const u32x4 v = *(const u32x4*)(w1 + (threadIdx.x + 256 * i) * FF + b * 8);
#pragma unroll
            for (int j = 0; j < 4; ++j) { a[2 * j] += bf_lo(v[j]); a[2 * j + 1] += bf_hi(v[j]); }
        }
#pragma unroll
        for (int j = 0; j < 8; ++j) { a[j] = wave_sum(a[j]); if (lane == 0) red[wave][j] = a[j]; }
        __syncthreads();
        if (threadIdx.x < 8) colsum[1024 + b * 8 + threadIdx.x] = red[0][threadIdx.x] + red[1][threadIdx.x] + red[2][threadIdx.x] + red[3][threadIdx.x];
    }
    if (threadIdx.x < 32) ((u32x4*)(ws + WS_EG))[b * 32 + threadIdx.x] = u32x4{0, 0, 0, 0};
    STAMP(0, 1);
}

constexpr int P1_SLOT = 36 * 1024, P1_SLOTS = 4, P1_X = 0, P1_B = 16384, P1_W = 24576;
constexpr int P1_TAB = P1_SLOT * P1_SLOTS;
constexpr int P1_LDS = P1_TAB + 4096;

__global__ __launch_bounds__(512) void k_p1(const unsigned short* __restrict__ x, const unsigned short* __restrict__ lam_re,
                                            const unsigned short* __restrict__ lam_im, const unsigned short* __restrict__ bre,
                                            const unsigned short* __restrict__ bim, const unsigned short* __restrict__ log_dt,
                                            const unsigned short* __restrict__ w1, const unsigned short* __restrict__ b1, char* __restrict__ ws) {
    extern __shared__ __attribute__((aligned(16))) char smem_[];
    LAS char* smem = (LAS char*)smem_;
    const int wave = __builtin_amdgcn_readfirstlane(threadIdx.x >> 6), lane = threadIdx.x & 63;
    const int loc = blockIdx.x >> 3, ci = 2 * (blockIdx.x & 7) + (loc >> 4), cj = loc & 15;
    const int t0 = 128 * ci, p0 = 32 * cj, f0 = cj < 6 ? 96 * cj : 576 + 80 * (cj - 6), nfu = cj < 6 ? 6 : 5;

    if (wave >= 4) {
        const int lw = wave - 4;
        STAMP(1, 0 + 8);
        unsigned xo[4], bo[2], wo[3];
#pragma unroll
        for (int i = 0; i < 4; ++i) {
            const int R = 8 * (4 * lw + i) + (lane >> 3), ii = R & 15, bb = (R >> 4) & 3;
            const int tok = t0 + (R & 64) + 16 * (ii >> 2) + 8 * (ii & 1) + 2 * bb + ((ii >> 1) & 1);
            xo[i] = tok * 1024 + 16 * ((lane & 7) ^ (lane >> 3));
        }
#pragma unroll
        for (int i = 0; i < 2; ++i) {
            const int R = 8 * (2 * lw + i) + (lane >> 3);
            bo[i] = (p0 + (R & 31)) * 1024 + 16 * ((lane & 7) ^ (lane >> 3));
        }
#pragma unroll
        for (int i = 0; i < 3; ++i) {
            const int st = 2 * (3 * lw + i) + (lane >> 5), kg = st / 3, ns = st - 3 * kg, kr = (lane & 31) >> 2;
            const int ch = 4 * ns + ((lane & 3) ^ ((2 * kg + (kr >> 2)) & 3));
            const int f = min(f0 + 8 * ch, FF - 8);
            wo[i] = (8 * kg + kr) * (FF * 2) + f * 2;
        }
        const GAS char* xg = (const GAS char*)x;
        const GAS char* bg = (const GAS char*)(lw < 2 ? bre : bim);
        const GAS char* wg = (const GAS char*)w1;
        auto issue = [&](int t) {
            LAS char* s = smem + (t & 3) * P1_SLOT;
#pragma unroll
            for (int i = 0; i < 4; ++i) dma16(xg + t * 128 + xo[i], s + P1_X + (4 * lw + i) * 1024);
#pragma unroll
            for (int i = 0; i < 2; ++i) dma16(bg + t * 128 + bo[i], s + P1_B + (2 * lw + i) * 1024);
#pragma unroll
            for (int i = 0; i < 3; ++i) dma16(wg + (size_t)t * (64 * FF * 2) + wo[i], s + P1_W + (3 * lw + i) * 1024);
        };
        issue(0); issue(1); issue(2); issue(3);
        wait_vm<27>(); bar();
        STAMP(1, 1 + 8);
        wait_vm<18>(); bar(); issue(4);
        wait_vm<18>(); bar(); issue(5);
        wait_vm<18>(); bar(); issue(6);
        wait_vm<18>(); bar(); issue(7);
        wait_vm<18>(); bar();
        wait_vm<9>(); bar();
        wait_vm<0>(); bar();
        STAMP(1, 2 + 8);
        return;
    }

    const int wm = wave >> 1, wn = wave & 1, g = lane >> 4, li = lane & 15;
    STAMP(1, 0);
    const int p = p0 + 16 * wn + li;
    cf lam, l8, l16, l128, lsig, coef2;
    {
        const float lr = bf2f(lam_re[p]), lim = bf2f(lam_im[p]), dt = rbf(expf(bf2f(log_dt[p])));
        const float mag = expf(dt * lr);
        float sn, cs;
        sincosf(dt * lim, &sn, &cs);
        lam = cf{mag * cs, mag * sn};
        const float nr = lam.r - 1.0f, ni = lam.i, den = 1.0f / (lr * lr + lim * lim);
        coef2 = cf{2.0f * (nr * lr + ni * lim) * den, 2.0f * (ni * lr - nr * lim) * den};
        const cf l2 = cmul(lam, lam), l4 = cmul(l2, l2);
        l8 = cmul(l4, l4); l16 = cmul(l8, l8);
        const cf l32 = cmul(l16, l16), l64 = cmul(l32, l32);
        l128 = cmul(l64, l64);
        lsig = (g & 1) ? l16 : cf{1.0f, 0.0f};
        if (g & 2) lsig = cmul(lsig, l32);
        if (wm) lsig = cmul(lsig, l64);
    }
    const float* colsum = (const float*)(ws + WS_COLSUM);
    const float sbr = colsum[p], sbi = colsum[NP + p];
    float s1c[3], b1c[3];
#pragma unroll
    for (int c = 0; c < 3; ++c) {
        const int f = min(f0 + 16 * (3 * wn + c) + li, FF - 1);
        s1c[c] = colsum[1024 + f]; b1c[c] = bf2f(b1[f]);
    }
    if (threadIdx.x < 128) {
        const int tau = threadIdx.x;
        const float2 st = ((const float2*)(ws + WS_ROWSTAT))[t0 + tau];
        const RowScale rs = row_scale(st.x, st.y);
        const int idx = ((tau >> 4) * 8 + (tau & 7)) * 4 + ((tau >> 3) & 1);
        LAS float* ta = (LAS float*)(smem + P1_TAB);
        ta[idx] = rs.r1; ta[idx + 2] = -rs.r1 * rs.mb;
        ta[256 + idx] = rs.r12; ta[256 + idx + 2] = -rs.r12 * st.x;
    }

    const int sw = 16 * (g ^ (lane & 7));
    const int xa0 = P1_X + (64 * wm + li) * 128 + sw;
    const int ba0 = P1_B + (16 * wn + li) * 128 + sw;
    const int q = li >> 2, pp = lane & 3;
    int wa[2][2];
#pragma unroll
    for (int t = 0; t < 2; ++t)
#pragma unroll
        for (int cl = 0; cl < 2; ++cl) {
            const int par = (3 * wn + cl) & 1;
            wa[t][cl] = P1_W + 1536 * g + 64 * q + 256 * t + 32 * (par ^ (g & 1)) + 16 * ((pp >> 1) ^ t) + 8 * (pp & 1) + (cl == 0 ? 512 * wn : 1024 * wn);
        }

    f32x4 acc[4][5];
#pragma unroll
    for (int b = 0; b < 4; ++b)
#pragma unroll
        for (int c = 0; c < 5; ++c) acc[b][c] = f32x4{0, 0, 0, 0};
    s16x8 xa[2][4], wb[2][5];
    auto rd = [&](int slot, int ks, int buf) {
        LAS char* s = smem + slot * P1_SLOT;
        LAS char* xs = s + (xa0 ^ (64 * ks));
        LAS char* bs = s + (ba0 ^ (64 * ks));
#pragma unroll
        for (int b = 0; b < 4; ++b) xa[buf][b] = lds_row(xs + b * 2048);
        wb[buf][0] = lds_row(bs);
        wb[buf][1] = lds_row(bs + 4096);
        wb[buf][2] = lds_tr(s + wa[0][0] + 6144 * ks, s + wa[1][0] + 6144 * ks);
        wb[buf][3] = lds_tr(s + wa[0][1] + 6144 * ks, s + wa[1][1] + 6144 * ks);
        wb[buf][4] = lds_tr(s + wa[0][0] + 6144 * ks + 512, s + wa[1][0] + 6144 * ks + 512);
    };
    auto mm = [&](int buf) {
#pragma unroll
        for (int c = 0; c < 5; ++c)
#pragma unroll
            for (int b = 0; b < 4; ++b) acc[b][c] = mfma(xa[buf][b], wb[buf][c], acc[b][c]);
    };
    wait_lgkm0();
    bar();
    STAMP(1, 1);
    rd(0, 0, 0);
    wait_lgkm0();
#pragma unroll 1
    for (int t = 0; t < 7; ++t) {
        __builtin_amdgcn_sched_barrier(0);
        rd(t & 3, 1, 1);
        mm(0);
        interleave<20, 12>();
        __builtin_amdgcn_sched_barrier(0);
        wait_lgkm0();
        bar();
        rd((t + 1) & 3, 0, 0);
        mm(1);
        interleave<20, 12>();
        __builtin_amdgcn_sched_barrier(0);
        wait_lgkm0();
    }
    rd(3, 1, 1);
    mm(0);
    interleave<20, 12>();
    __builtin_amdgcn_sched_barrier(0);
    mm(1);
    STAMP(1, 2);

    LAS f32x4* tab1 = (LAS f32x4*)(smem + P1_TAB) + (wm * 4 + g) * 8;
    LAS f32x4* tab2 = tab1 + 64;
    f32x2 ure[8], uim[8];
#pragma unroll
    for (int j = 0; j < 8; ++j) {
        const f32x4 tb = tab1[j];
        const f32x2 a = {tb[0], tb[1]}, bb = {tb[2], tb[3]};
        const int b = j >> 1, r = 2 * (j & 1);
        ure[j] = pfma(a, f32x2{acc[b][0][r], acc[b][0][r + 1]}, bb * splat(sbr));
        uim[j] = pfma(a, f32x2{acc[b][1][r], acc[b][1][r + 1]}, bb * splat(sbi));
    }
    f32x2 hr = ure[0], hi = uim[0];
#pragma unroll
    for (int j = 1; j < 8; ++j) {
        const f32x2 tr = pfma(splat(lam.r), hr, ure[j]), ti = pfma(splat(lam.r), hi, uim[j]);
        const f32x2 nr = pfma(splat(-lam.i), hi, tr), ni = pfma(splat(lam.i), hr, ti);
        hr = nr; hi = ni;
    }
    const cf eA = cf{hr[0], hi[0]};
    const cf e = cfma(l8, eA, cf{hr[1], hi[1]});
    const int sig = 4 * wm + g;
    LAS f32x2* eL = (LAS f32x2*)(smem + P1_TAB + 2048);
    eL[sig * 32 + 16 * wn + li] = f32x2{e.r, e.i};
    wait_lgkm0();
    bar();
    cf Lp = cf{0.0f, 0.0f};
#pragma unroll
    for (int s = 0; s < 7; ++s) {
        const f32x2 v = eL[s * 32 + 16 * wn + li];
        const cf n = cfma(l16, Lp, cf{v[0], v[1]});
        if (s < sig) Lp = n;
    }
    GAS u32x4* eg = (GAS u32x4*)(ws + WS_EG);
    if (sig == 7) {
        const cf E = cfma(l16, Lp, e);
        const u32x4 gr = {__float_as_uint(E.r), 1u, __float_as_uint(E.i), 1u};
        asm volatile("global_store_dwordx4 %0, %1, off sc1\n\ts_nop 1" ::"v"(eg + ci * NP + p), "v"(gr) : "memory");
    }
    STAMP(1, 3);

    GAS char* act = (GAS char*)(ws + WS_ACT) + (size_t)(2 * ci + wm) * ACT_TILE;
    {
        f32x4 tb[8];
#pragma unroll
        for (int j = 0; j < 8; ++j) tb[j] = tab2[j];
        constexpr float C1 = -2.3022081986f;
        constexpr float C2 = C1 * 0.044715f;
#pragma unroll
        for (int c = 0; c < 3; ++c) {
            f32x2 y[8];
#pragma unroll
            for (int j = 0; j < 8; ++j) {
                const int b = j >> 1, r = 2 * (j & 1);
                const f32x2 a = {tb[j][0], tb[j][1]}, bb = {tb[j][2], tb[j][3]};
                const f32x2 z = pfma(a, f32x2{acc[b][2 + c][r], acc[b][2 + c][r + 1]}, pfma(bb, splat(s1c[c]), splat(b1c[c])));
                const f32x2 u = z * pfma(z * z, splat(C2), splat(C1));
                const f32x2 d = f32x2{__builtin_amdgcn_exp2f(u[0]), __builtin_amdgcn_exp2f(u[1])} + splat(1.0f);
                y[j] = z * f32x2{__builtin_amdgcn_rcpf(d[0]), __builtin_amdgcn_rcpf(d[1])};
            }
            if (3 * wn + c < nfu) {
                const int k = f0 + 16 * (3 * wn + c) + li;
                GAS char* dst = act + (k >> 3) * 1024 + (k & 7) * 64 + 16 * (g ^ ((k >> 2) & 3));
#pragma unroll
                for (int sgm = 0; sgm < 2; ++sgm)
                    *(GAS u32x4*)(dst + 512 * sgm) = u32x4{pk_bf16(y[0][sgm], y[1][sgm]), pk_bf16(y[2][sgm], y[3][sgm]), pk_bf16(y[4][sgm], y[5][sgm]), pk_bf16(y[6][sgm], y[7][sgm])};
            }
        }
    }
    STAMP(1, 4);

    cf Hin = cf{0.0f, 0.0f};
    if (ci > 0) {
        u32x4 gr[15];
        for (unsigned spins = 0; spins < (1u << 22); ++spins) {
#pragma unroll
            for (int j = 0; j < 15; ++j) {
                const int jj = j < ci ? j : ci - 1;
                asm volatile("global_load_dwordx4 %0, %1, off sc1" : "=v"(gr[j]) : "v"(eg + jj * NP + p) : "memory");
            }
            asm volatile("s_waitcnt vmcnt(0)"
                         : "+v"(gr[0]), "+v"(gr[1]), "+v"(gr[2]), "+v"(gr[3]), "+v"(gr[4]), "+v"(gr[5]), "+v"(gr[6]), "+v"(gr[7]), "+v"(gr[8]),
                           "+v"(gr[9]), "+v"(gr[10]), "+v"(gr[11]), "+v"(gr[12]), "+v"(gr[13]), "+v"(gr[14])::"memory");
            bool ok = true;
#pragma unroll
            for (int j = 0; j < 15; ++j) ok &= (gr[j][1] == 1u) & (gr[j][3] == 1u);
            if (__all(ok)) break;
            __builtin_amdgcn_s_sleep(2);
        }
#pragma unroll
        for (int j = 0; j < 15; ++j)
            if (j < ci) Hin = cfma(l128, Hin, cf{__uint_as_float(gr[j][0]), __uint_as_float(gr[j][2])});
    }
    STAMP(1, 5);
    {
        const cf cA = cfma(lsig, Hin, Lp);
        const cf cB = cfma(l8, cA, eA);
        f32x2 sr = {cA.r, cB.r}, si = {cA.i, cB.i};
        f32x2 ore[8], oim[8];
#pragma unroll
        for (int j = 0; j < 8; ++j) {
            const f32x2 tr = pfma(splat(lam.r), sr, ure[j]), ti = pfma(splat(lam.r), si, uim[j]);
            const f32x2 nr = pfma(splat(-lam.i), si, tr), ni = pfma(splat(lam.i), sr, ti);
            sr = nr; si = ni;
            ore[j] = pfma(splat(coef2.r), sr, splat(-coef2.i) * si);
            oim[j] = pfma(splat(-coef2.r), si, splat(-coef2.i) * sr);
        }
        const int kr = FF + p, kim = FF + NP + p;
        GAS char* dr = act + (kr >> 3) * 1024 + (kr & 7) * 64 + 16 * (g ^ ((kr >> 2) & 3));
        GAS char* di = act + (kim >> 3) * 1024 + (kim & 7) * 64 + 16 * (g ^ ((kim >> 2) & 3));
#pragma unroll
        for (int sgm = 0; sgm < 2; ++sgm) {
            *(GAS u32x4*)(dr + 512 * sgm) = u32x4{pk_bf16(ore[0][sgm], ore[1][sgm]), pk_bf16(ore[2][sgm], ore[3][sgm]), pk_bf16(ore[4][sgm], ore[5][sgm]), pk_bf16(ore[6][sgm], ore[7][sgm])};
            *(GAS u32x4*)(di + 512 * sgm) = u32x4{pk_bf16(oim[0][sgm], oim[1][sgm]), pk_bf16(oim[2][sgm], oim[3][sgm]), pk_bf16(oim[4][sgm], oim[5][sgm]), pk_bf16(oim[6][sgm], oim[7][sgm])};
        }
    }
    STAMP(1, 6);
}

constexpr int P2_SLOT = 16384, P2_SLOTS = 8, P2_NST = 38;
constexpr int P2_LDS = P2_SLOT * P2_SLOTS;

__global__ __launch_bounds__(512) void k_p2(const unsigned short* __restrict__ x, const unsigned short* __restrict__ cre,
                                            const unsigned short* __restrict__ cim, const unsigned short* __restrict__ dskip,
                                            const unsigned short* __restrict__ w2, const unsigned short* __restrict__ b2,
                                            char* __restrict__ ws, float* __restrict__ out) {
    extern __shared__ __attribute__((aligned(16))) char smem_[];
    LAS char* smem = (LAS char*)smem_;
    const int wave = __builtin_amdgcn_readfirstlane(threadIdx.x >> 6), lane = threadIdx.x & 63;
    const int loc = blockIdx.x >> 3, T = 4 * (blockIdx.x & 7) + (loc >> 3), d0 = 64 * (loc & 7);

    if (wave >= 4) {
        const int lw = wave - 4;
        STAMP(2, 0 + 8);
        const GAS char* ag = (const GAS char*)(ws + WS_ACT) + (size_t)T * ACT_TILE + lw * 2048 + lane * 16;
        unsigned wo[2], co[2];
#pragma unroll
        for (int i = 0; i < 2; ++i) {
            const int kg = 2 * lw + i, kr = (lane & 31) >> 2;
            const int ch = 4 * (lane >> 5) + ((lane & 3) ^ ((2 * kg + (kr >> 2)) & 3));
            wo[i] = (8 * kg + kr) * (DM * 2) + (d0 + 8 * ch) * 2;
            const int R = 8 * kg + (lane >> 3);
            co[i] = (d0 + R) * (NP * 2) + 16 * ((lane & 7) ^ (lane >> 3));
        }
        auto issue = [&](int s) {
            LAS char* sl = smem + (s & 7) * P2_SLOT;
            if (s < 22) {
                const int k0 = s < 21 ? 64 * s : FF - 64;
#pragma unroll
                for (int i = 0; i < 2; ++i) dma16(ag + k0 * 128 + i * 1024, sl + (2 * lw + i) * 1024);
#pragma unroll
                for (int i = 0; i < 2; ++i) dma16((const GAS char*)w2 + k0 * (DM * 2) + wo[i], sl + 8192 + (2 * lw + i) * 1024);
            } else {
                const int kk = 64 * (s - 22);
#pragma unroll
                for (int i = 0; i < 2; ++i) dma16(ag + (FF + kk) * 128 + i * 1024, sl + (2 * lw + i) * 1024);
                const GAS char* cg = (const GAS char*)(kk < NP ? cre : cim) + (kk & (NP - 1)) * 2;
#pragma unroll
                for (int i = 0; i < 2; ++i) dma16(cg + co[i], sl + 8192 + (2 * lw + i) * 1024);
            }
        };
        for (int s = 0; s < 6; ++s) issue(s);
        for (int s = 0; s < P2_NST - 5; ++s) {
            wait_vm<20>(); bar();
            if (s == 0) STAMP(2, 1 + 8);
            if (s + 6 < P2_NST) issue(s + 6);
        }
        wait_vm<16>(); bar();
        wait_vm<12>(); bar();
        wait_vm<8>(); bar();
        wait_vm<4>(); bar();
        wait_vm<0>(); bar();
        STAMP(2, 2 + 8);
        return;
    }

    const int wd = wave >> 1, wt = wave & 1, g = lane >> 4, li = lane & 15;
    STAMP(2, 0);
    const int q = li >> 2, pp = lane & 3;
    int tr[2][2];
#pragma unroll
    for (int t = 0; t < 2; ++t)
#pragma unroll
        for (int par = 0; par < 2; ++par) tr[t][par] = 1024 * g + 256 * t + 64 * q + 32 * (par ^ (g & 1)) + 16 * ((pp >> 1) ^ t) + 8 * (pp & 1);
    const int ca0 = 8192 + (32 * wd + li) * 128 + 16 * (g ^ (lane & 7));
    f32x4 acc[2][2];
#pragma unroll
    for (int i = 0; i < 2; ++i)
#pragma unroll
        for (int j = 0; j < 2; ++j) acc[i][j] = f32x4{0, 0, 0, 0};
    s16x8 wf[2][2][2], af[2][2][2];
    auto rd_act = [&](LAS char* sl, int buf) {
#pragma unroll
        for (int ks = 0; ks < 2; ++ks)
#pragma unroll
            for (int i = 0; i < 2; ++i) af[buf][ks][i] = lds_tr(sl + 512 * wt + 4096 * ks + tr[0][i], sl + 512 * wt + 4096 * ks + tr[1][i]);
    };
    auto rd4 = [&](int s, int buf) {
        LAS char* sl = smem + (s & 7) * P2_SLOT;
        rd_act(sl, buf);
#pragma unroll
        for (int ks = 0; ks < 2; ++ks)
#pragma unroll
            for (int i = 0; i < 2; ++i) wf[buf][ks][i] = lds_tr(sl + 8192 + 512 * wd + 4096 * ks + tr[0][i], sl + 8192 + 512 * wd + 4096 * ks + tr[1][i]);
    };
    auto rd3 = [&](int s, int buf) {
        LAS char* sl = smem + (s & 7) * P2_SLOT;
        rd_act(sl, buf);
#pragma unroll
        for (int ks = 0; ks < 2; ++ks)
#pragma unroll
            for (int i = 0; i < 2; ++i) wf[buf][ks][i] = lds_row(sl + ((ca0 ^ (64 * ks)) + 2048 * i));
    };
    auto mmk = [&](int buf, int ks) {
#pragma unroll
        for (int i = 0; i < 2; ++i)
#pragma unroll
            for (int j = 0; j < 2; ++j) acc[i][j] = mfma(wf[buf][ks][i], af[buf][ks][j], acc[i][j]);
    };
    auto mm = [&](int buf) { mmk(buf, 0); mmk(buf, 1); };
#define SB0 __builtin_amdgcn_sched_barrier(0)
    bar(); rd4(0, 0); SB0;
    STAMP(2, 1);
#pragma unroll 1
    for (int s = 1; s < 21; s += 2) {
        bar(); rd4(s, 1); mm(0); interleave<8, 16, 3>(); SB0;
        bar(); rd4(s + 1, 0); mm(1); interleave<8, 16, 3>(); SB0;
    }
    bar(); rd4(21, 1); mm(0); interleave<8, 16, 3>(); SB0;
    bar(); rd3(22, 0); mmk(1, 1); interleave<4, 12, 3>(); SB0;
#pragma unroll 1
    for (int s = 23; s < P2_NST - 1; s += 2) {
        bar(); rd3(s, 1); mm(0); interleave<8, 12, 2>(); SB0;
        bar(); rd3(s + 1, 0); mm(1); interleave<8, 12, 2>(); SB0;
    }
    bar(); rd3(P2_NST - 1, 1); mm(0); interleave<8, 12, 2>(); SB0;
    mm(1);
    STAMP(2, 2);

#pragma unroll
    for (int j = 0; j < 2; ++j) {
        const int tok = 64 * T + 32 * j + 16 * ((lane >> 3) & 1) + 8 * wt + (lane & 7);
        const float2 st = ((const float2*)(ws + WS_ROWSTAT))[tok];
        const RowScale rs = row_scale(st.x, st.y);
#pragma unroll
        for (int i = 0; i < 2; ++i) {
            const int d = d0 + 32 * wd + 16 * i + 4 * g;
            const u32x2 xv = *(const u32x2*)(x + tok * DM + d), dv = *(const u32x2*)(dskip + d), bv = *(const u32x2*)(b2 + d);
            const float xe[4] = {bf_lo(xv[0]), bf_hi(xv[0]), bf_lo(xv[1]), bf_hi(xv[1])};
            const float de[4] = {bf_lo(dv[0]), bf_hi(dv[0]), bf_lo(dv[1]), bf_hi(dv[1])};
            const float be[4] = {bf_lo(bv[0]), bf_hi(bv[0]), bf_lo(bv[1]), bf_hi(bv[1])};
            f32x4 o;
#pragma unroll
            for (int r = 0; r < 4; ++r) {
                const float xn = rbf(rbf(xe[r] - rs.mb) * rs.r1);
                o[r] = xe[r] + rbf(xn * de[r]) + be[r] + acc[i][j][r];
            }
            *(f32x4*)(out + tok * DM + d) = o;
        }
    }
    STAMP(2, 3);
}

}

extern "C" void kernel_launch(void* const* d_in, const int* in_sizes, int n_in, void* d_out, int out_size, void* d_ws, size_t ws_size,
                              hipStream_t stream) {
    typedef const unsigned short* P;
    P x = (P)d_in[0], lre = (P)d_in[1], lim = (P)d_in[2], bre = (P)d_in[3], bim = (P)d_in[4], cre = (P)d_in[5], cim = (P)d_in[6],
      ldt = (P)d_in[7], dsk = (P)d_in[8], w1 = (P)d_in[10], b1 = (P)d_in[11], w2 = (P)d_in[12], b2 = (P)d_in[13];
    char* ws = (char*)d_ws;
    (void)hipFuncSetAttribute((const void*)k_p1, hipFuncAttributeMaxDynamicSharedMemorySize, P1_LDS);
    (void)hipFuncSetAttribute((const void*)k_p2, hipFuncAttributeMaxDynamicSharedMemorySize, P2_LDS);
    k_stats<<<256, 256, 0, stream>>>(x, bre, bim, w1, ws);
    k_p1<<<256, 512, P1_LDS, stream>>>(x, lre, lim, bre, bim, ldt, w1, b1, ws);
    k_p2<<<256, 512, P2_LDS, stream>>>(x, cre, cim, dsk, w2, b2, ws, (float*)d_out);
}
```

```cpp
#include <hip/hip_runtime.h>
#include <hip/hip_bf16.h>
#include <stdint.h>
#include <type_traits>

#define LAS __attribute__((address_space(3)))
#define GAS __attribute__((address_space(1)))

typedef __attribute__((ext_vector_type(8))) short s16x8;
typedef __attribute__((ext_vector_type(4))) short s16x4;
typedef __attribute__((ext_vector_type(8))) __bf16 bf16x8;
typedef __attribute__((ext_vector_type(2))) __bf16 bf16x2;
typedef __attribute__((ext_vector_type(4))) float f32x4;
typedef __attribute__((ext_vector_type(2))) float f32x2;
typedef __attribute__((ext_vector_type(4))) unsigned u32x4;
typedef __attribute__((ext_vector_type(2))) unsigned u32x2;

#define ACT_AUX 0
#define ST_AUX 16
#define PREFETCH 1
#define WARM1 0
#define PRIO 0
#define WARMA 1
#define D4 4
#define D3 3
#define PREF2 0
#define DEFER 0
#define LPRIO2 0
#define CPRIO2 0
#define ACQ 0

namespace {

constexpr int SEQ = 2048, DM = 512, NP = 512, FF = 1376;
constexpr int KACT = FF + 2 * NP;
constexpr int ACT_TILE = KACT * 128;
constexpr unsigned WS_ACT = 0;
constexpr unsigned WS_RS = 10u << 20;
constexpr unsigned WS_EG = WS_RS + (64u << 10);
constexpr unsigned WS_FH = WS_EG + (256u << 10);
constexpr unsigned WS_FS = WS_FH + (8u << 10);
constexpr unsigned WS_CS = WS_FS + (8u << 10);
constexpr unsigned WS_EP = WS_CS + (256u << 10);
constexpr size_t WS_STAMP = 200u << 20;
constexpr float LN_EPS = 1e-5f;
constexpr unsigned SPIN_CAP = 1u << 18;

constexpr int P1_SLOT = 36 * 1024, P1_X = 0, P1_B = 16384, P1_W = 24576;
constexpr int P2_IMG = 4 * 2080, P2_SLOT = 2 * P2_IMG, P2_RING = 9 * P2_SLOT, P2_NST = 38;
constexpr int L_TAB1 = P2_RING;
constexpr int L_TAB2 = L_TAB1 + 1024;
constexpr int L_EL = L_TAB2 + 1024;
constexpr int L_CS = L_EL + 2048;
constexpr int L_TOK = L_CS + 1280;
constexpr int L_DUMMY = L_TOK + 1024;
constexpr int L_MAIL = L_DUMMY + 1024;
constexpr int LDS_BYTES = L_MAIL + 64 + 704;

#define STAMP(s) do {} while (0)

#define SB0 __builtin_amdgcn_sched_barrier(0)
template <int N> __device__ __forceinline__ void wait_vm() { asm volatile("s_waitcnt vmcnt(%0)" ::"i"(N) : "memory"); }
__device__ __forceinline__ void wait_lgkm0() { __builtin_amdgcn_s_waitcnt(0xC07F); asm volatile("" ::: "memory"); }
template <int NM, int ND, int DPM = 1, int I = 0> __device__ __forceinline__ void interleave() {
    if constexpr (I * DPM < ND && I < NM) {
        __builtin_amdgcn_sched_group_barrier(0x008, 1, 0);
        __builtin_amdgcn_sched_group_barrier(0x100, (ND - I * DPM) < DPM ? (ND - I * DPM) : DPM, 0);
        interleave<NM, ND, DPM, I + 1>();
    } else if constexpr (I < NM) {
        __builtin_amdgcn_sched_group_barrier(0x008, NM - I, 0);
    }
}
__device__ __forceinline__ void bar() {
    SB0;
    asm volatile("" ::: "memory");
    __builtin_amdgcn_s_barrier();
    asm volatile("" ::: "memory");
    SB0;
}
__device__ __forceinline__ float bf_lo(unsigned u) { return __uint_as_float(u << 16); }
__device__ __forceinline__ float bf_hi(unsigned u) { return __uint_as_float(u & 0xffff0000u); }
__device__ __forceinline__ float bf2f(unsigned short h) { return __uint_as_float((unsigned)h << 16); }
__device__ __forceinline__ unsigned pk_bf16(float a, float b) { bf16x2 v = {(__bf16)a, (__bf16)b}; return __builtin_bit_cast(unsigned, v); }
__device__ __forceinline__ float rbf(float a) { return bf_lo(pk_bf16(a, a)); }
__device__ __forceinline__ f32x2 pfma(f32x2 a, f32x2 b, f32x2 c) { return __builtin_elementwise_fma(a, b, c); }
__device__ __forceinline__ f32x2 splat(float a) { return f32x2{a, a}; }
__device__ __forceinline__ f32x4 mfma(s16x8 a, s16x8 b, f32x4 c) {
    return __builtin_amdgcn_mfma_f32_16x16x32_bf16(__builtin_bit_cast(bf16x8, a), __builtin_bit_cast(bf16x8, b), c, 0, 0, 0);
}
__device__ __forceinline__ void mfma_acc(f32x4& c, s16x8 a, s16x8 b) {
    asm volatile("v_mfma_f32_16x16x32_bf16 %0, %1, %2, %0" : "+v"(c) : "v"(a), "v"(b));
}
__device__ __forceinline__ s16x4 lds_tr1(LAS char* p) { return __builtin_amdgcn_ds_read_tr16_b64_v4i16((LAS s16x4*)p); }
__device__ __forceinline__ s16x8 cat(s16x4 a, s16x4 b) { return __builtin_shufflevector(a, b, 0, 1, 2, 3, 4, 5, 6, 7); }
__device__ __forceinline__ s16x8 lds_row(LAS char* p) { return *(LAS s16x8*)p; }
__device__ __forceinline__ s16x8 lds_tr(LAS char* p0, LAS char* p1) {
    s16x4 a = __builtin_amdgcn_ds_read_tr16_b64_v4i16((LAS s16x4*)p0);
    s16x4 b = __builtin_amdgcn_ds_read_tr16_b64_v4i16((LAS s16x4*)p1);
    return __builtin_shufflevector(a, b, 0, 1, 2, 3, 4, 5, 6, 7);
}
template <int OFF> __device__ __forceinline__ void dma1(unsigned lds, unsigned voff, const GAS char* base) {
    asm volatile("s_mov_b32 m0, %0\n\ts_nop 0\n\tglobal_load_lds_dwordx4 %1, %2 offset:%3" ::"s"(lds), "v"(voff), "s"(base), "i"(OFF) : "memory");
}
template <int OFF> __device__ __forceinline__ void dma2(unsigned lds, unsigned voff0, unsigned voff1, const GAS char* base) {
    asm volatile("s_mov_b32 m0, %0\n\ts_nop 0\n\tglobal_load_lds_dwordx4 %1, %3\n\tglobal_load_lds_dwordx4 %2, %3 offset:%4"
                 ::"s"(lds), "v"(voff0), "v"(voff1), "s"(base), "i"(OFF) : "memory");
}
__device__ __forceinline__ void touch(unsigned lds, const GAS char* a) {
    asm volatile("s_mov_b32 m0, %0\n\ts_nop 0\n\tglobal_load_lds_dword %1, off" ::"s"(lds), "v"(a) : "memory");
}
__device__ __forceinline__ void lds_st32(unsigned a, float v) { asm volatile("ds_write_b32 %0, %1" ::"v"(a), "v"(v) : "memory"); }
__device__ __forceinline__ void lds_st64(unsigned a, f32x2 v) { asm volatile("ds_write_b64 %0, %1" ::"v"(a), "v"(v) : "memory"); }
template <int CTRL, int RM> __device__ __forceinline__ float dpp_add(float v) {
    return v + __uint_as_float(__builtin_amdgcn_update_dpp(0u, __float_as_uint(v), CTRL, RM, 0xf, true));
}
__device__ __forceinline__ float wave_sum(float v) {
    v = dpp_add<0x111, 0xf>(v); v = dpp_add<0x112, 0xf>(v); v = dpp_add<0x114, 0xf>(v); v = dpp_add<0x118, 0xf>(v);
    v = dpp_add<0x142, 0xa>(v); v = dpp_add<0x143, 0xc>(v);
    return __uint_as_float(__builtin_amdgcn_readlane(__float_as_uint(v), 63));
}
__device__ __forceinline__ float sum_groups(float v) {
    const auto a = __builtin_amdgcn_permlane16_swap(__float_as_uint(v), __float_as_uint(v), false, false);
    v = __uint_as_float(a[0]) + __uint_as_float(a[1]);
    const auto b = __builtin_amdgcn_permlane32_swap(__float_as_uint(v), __float_as_uint(v), false, false);
    return __uint_as_float(b[0]) + __uint_as_float(b[1]);
}
__device__ __forceinline__ u32x4 load16_sc1(const GAS void* p) {
    u32x4 v;
    asm volatile("global_load_dwordx4 %0, %1, off sc1\n\ts_waitcnt vmcnt(0)" : "=&v"(v) : "v"(p) : "memory");
    return v;
}
__device__ __forceinline__ void store16_sc1(GAS void* p, u32x4 v) {
    asm volatile("global_store_dwordx4 %0, %1, off sc1\n\ts_nop 1" ::"v"(p), "v"(v) : "memory");
}
__device__ __forceinline__ void wait_flags(const GAS unsigned* f, unsigned tag) {
#pragma unroll 1
    for (unsigned spins = 0; spins < SPIN_CAP; ++spins) {
        const unsigned v = __hip_atomic_load(f + (threadIdx.x & 31), __ATOMIC_RELAXED, __HIP_MEMORY_SCOPE_AGENT);
        if (__all(v == tag)) break;
        __builtin_amdgcn_s_sleep(1);
    }
    asm volatile("" ::: "memory");
}
struct cf { float r, i; };
__device__ __forceinline__ cf cmul(cf a, cf b) { return cf{a.r * b.r - a.i * b.i, a.r * b.i + a.i * b.r}; }
__device__ __forceinline__ cf cfma(cf a, cf b, cf c) { return cf{fmaf(a.r, b.r, fmaf(-a.i, b.i, c.r)), fmaf(a.r, b.i, fmaf(a.i, b.r, c.i))}; }
__device__ __forceinline__ float exp_f(float x) {
    const float hi = x * 1.44269504f, lo = fmaf(x, 1.44269504f, -hi) + x * 1.92596299e-8f;
    return __builtin_amdgcn_exp2f(hi) * fmaf(lo, 0.693147181f, 1.0f);
}
__device__ __forceinline__ void sincos_f(float x, float& s, float& c) {
    const float k = rintf(x * 0.636619772f);
    float r = fmaf(k, -1.57079601e+00f, x);
    r = fmaf(k, -3.13916473e-07f, r);
    r = fmaf(k, -5.39030253e-15f, r);
    const float z = r * r;
    const float sp = fmaf(r * z, fmaf(z, fmaf(z, fmaf(z, 2.75573137e-06f, -1.98412698e-04f), 8.33333333e-03f), -1.66666667e-01f), r);
    const float cp = fmaf(z, fmaf(z, fmaf(z, fmaf(z, fmaf(z, -2.75573144e-07f, 2.48015873e-05f), -1.38888889e-03f), 4.16666667e-02f), -0.5f), 1.0f);
    const int q = (int)k;
    const float a = (q & 1) ? cp : sp, b = (q & 1) ? sp : cp;
    s = (q & 2) ? -a : a;
    c = ((q + 1) & 2) ? -b : b;
}
__device__ __forceinline__ unsigned row_std(float v) {
    const float s1 = rbf(__builtin_amdgcn_sqrtf(rbf(rbf(v) + LN_EPS)));
    const float r1 = __builtin_amdgcn_rcpf(s1);
    const float s2 = __builtin_amdgcn_sqrtf(rbf(rbf(v * r1 * r1) + LN_EPS));
    return pk_bf16(s1, s2);
}

typedef const unsigned short* __restrict__ bfp;

__global__ __launch_bounds__(512) void k_fused(bfp x, bfp lam_re, bfp lam_im, bfp bre, bfp bim, bfp cre, bfp cim, bfp log_dt, bfp dskip,
                                               bfp w1, bfp b1, bfp w2, bfp b2, char* ws_, float* __restrict__ out) {
    extern __shared__ __attribute__((aligned(16))) char smem_[];
    LAS char* smem = (LAS char*)smem_;
    GAS char* ws = (GAS char*)ws_;
    const int wave = __builtin_amdgcn_readfirstlane(threadIdx.x >> 6), lane = threadIdx.x & 63;
    const int g = lane >> 4, li = lane & 15;
    const int xg8 = blockIdx.x & 7, loc = blockIdx.x >> 3, ci = 4 * (xg8 >> 1) + (loc >> 3), cj = 8 * (xg8 & 1) + (loc & 7);
    const int t0 = 128 * ci, p0 = 32 * cj, f0 = cj < 11 ? 96 * cj : 1056 + 64 * (cj - 11), nfu = cj < 11 ? 6 : 4;
    const int T = 2 * ci + (cj >> 3), d0 = 64 * (cj & 7);
    const int rot = ci >> 1;
    STAMP(0);
    unsigned epoch;
    asm volatile("global_load_dword %0, %1, off sc1" : "=v"(epoch) : "v"(ws + WS_EP) : "memory");
    const int q = li >> 2, pp = lane & 3;

    if (__builtin_expect(wave >= 4, 1)) {
        const int lw = wave - 4;
        __builtin_amdgcn_s_setprio(PRIO);
        const unsigned lds0 = (unsigned)(size_t)smem;
        unsigned xo[4], bo[2], wo[3];
#pragma unroll
        for (int i = 0; i < 4; ++i) {
            const int R = 8 * (4 * lw + i) + (lane >> 3), ii = R & 15, bb = (R >> 4) & 3;
            const int tok = t0 + (R & 64) + 16 * (ii >> 2) + 8 * (ii & 1) + 2 * bb + ((ii >> 1) & 1);
            xo[i] = tok * 1024 + 16 * ((lane & 7) ^ (lane >> 3)) + 3072 - 1024 * i;
        }
#pragma unroll
        for (int i = 0; i < 2; ++i) {
            const int R = 8 * (2 * lw + i) + (lane >> 3);
            bo[i] = (p0 + (R & 31)) * 1024 + 16 * ((lane & 7) ^ (lane >> 3)) + 3072 - 1024 * i;
        }
#pragma unroll
        for (int i = 0; i < 3; ++i) {
            const int st = 2 * (3 * lw + i) + (lane >> 5), kg = st / 3, ns = st - 3 * kg, kr = (lane & 31) >> 2;
            const int ch = 4 * ns + ((lane & 3) ^ ((2 * kg + (kr >> 2)) & 3));
            const int f = min(f0 + 8 * ch, FF - 8);
            wo[i] = (8 * kg + kr) * (FF * 2) + f * 2 + 3072 - 1024 * i;
        }
        const GAS char* xg = (const GAS char*)x - 3072;
        const GAS char* bg = (const GAS char*)(lw < 2 ? bre : bim) - 3072;
        const GAS char* wg = (const GAS char*)w1 - 3072;
        const unsigned lx = lds0 + P1_X + lw * 4096, lb = lds0 + P1_B + lw * 2048, lwb = lds0 + P1_W + lw * 3072;
        auto piece = [&](int t, auto I) {
            constexpr int i = decltype(I)::value;
            const unsigned so = (t & 3) * P1_SLOT;
            const int kt = (t + rot) & 7;
            if constexpr (i < 4) dma1<1024 * i>(lx + so, xo[i], xg + kt * 128);
            else if constexpr (i < 6) dma1<1024 * (i - 4)>(lb + so, bo[i - 4], bg + kt * 128);
            else dma1<1024 * (i - 6)>(lwb + so, wo[i - 6], wg + (size_t)kt * (64 * FF * 2));
        };
#define PC(t, i) piece(t, std::integral_constant<int, i>{})
        auto issue = [&](int t) { PC(t, 0); PC(t, 1); PC(t, 2); PC(t, 3); PC(t, 4); PC(t, 5); PC(t, 6); PC(t, 7); PC(t, 8); };
        const GAS char* tbase;
        unsigned tstep;
        if (lw == 0) { tbase = (const GAS char*)x + (t0 + 16 * (cj & 7) + li) * 1024; tstep = 128; }
        else if (lw == 1) { const int r = 16 * (ci & 3) + li; tbase = (const GAS char*)(r < 32 ? bre : bim) + (p0 + (r & 31)) * 1024; tstep = 128; }
        else { tbase = (const GAS char*)w1 + (16 * (ci & 3) + li) * (FF * 2) + min(f0 * 2 + (lw == 2 ? 0 : 128), FF * 2 - 4); tstep = 64 * FF * 2; }
        auto warm = [&](int t) { if (WARM1) touch(lds0 + L_DUMMY + 256 * lw, tbase + ((t + rot) & 7) * tstep); };
        STAMP(6);
        issue(0); issue(1);
        warm(2 + (g & 1));
        STAMP(7);

        unsigned w2o[2], co[2], ao[2];
#pragma unroll
        for (int i = 0; i < 2; ++i) {
            const int kg = 2 * lw + i;
            w2o[i] = (8 * kg + ((lane & 31) >> 2)) * (DM * 2) + (d0 + 32 * (lane >> 5) + 8 * (lane & 3)) * 2 + 3072 - 1056 * i;
            co[i] = (d0 + 8 * kg + (lane >> 3)) * (NP * 2) + 16 * ((lane & 7) ^ (lane >> 3)) + 3072 - 1024 * i;
            ao[i] = kg * 1024 + lane * 16 + 3072 - 1056 * i;
        }
        const GAS char* ag = ws + WS_ACT + (size_t)T * ACT_TILE;
        const GAS char* w2g = (const GAS char*)w2 - 3072;
        const unsigned l2 = lds0 + lw * 2080, l2c = lds0 + P2_IMG + lw * 2048;
        auto dmaA = [&](const GAS char* src, unsigned slot) { dma2<1056>(l2 + slot, ao[0], ao[1], src - 3072); };
        auto dmaW = [&](const GAS char* src, unsigned slot) { dma2<1056>(l2 + slot + P2_IMG, w2o[0], w2o[1], src); };
        auto dmaC = [&](const GAS char* src, unsigned slot) { dma2<1024>(l2c + slot, co[0], co[1], src); };

        const GAS char* l2a[2];
#pragma unroll
        for (int i = 0; i < 2; ++i) {
            const int n = 456 * (ci & 3) + 114 * lw + min(64 * i + lane, 113);
            const int m = n - 800;
            l2a[i] = n < 800 ? (const GAS char*)w2 + (576 + n) * 1024 + d0 * 2
                             : (const GAS char*)(m < 512 ? cre : cim) + (d0 + ((m >> 3) & 63)) * 1024 + (m & 7) * 128;
        }
        const int n1 = 75 * blockIdx.x + 19 * lw + min(lane, 18);
        const GAS char* l1a = n1 < 11008 ? (const GAS char*)w2 + n1 * 128
                                         : (n1 < 15104 ? (const GAS char*)cre + (n1 - 11008) * 128 : (const GAS char*)cim + (min(n1, 19199) - 15104) * 128);
        const GAS u32x4* rsp = (const GAS u32x4*)(ws + WS_RS) + t0 + 64 * (lw & 1) + lane;
        const GAS u32x4* csp = (const GAS u32x4*)(ws + WS_CS) + ((cj * 16 + 8 * (lw >> 1) + 2 * g) * 2 + (lw & 1)) * 32 + li;
        asm volatile("" : "+v"(l2a[0]), "+v"(l2a[1]), "+v"(l1a), "+v"(rsp), "+v"(csp));
        asm volatile("s_waitcnt vmcnt(%1)" : "+v"(epoch) : "i"(9 + WARM1) : "memory");
        unsigned tag = __builtin_amdgcn_readfirstlane(epoch) + 1u;
        asm volatile("" : "+s"(tag));
        bar();
        STAMP(1);
        issue(2); warm(4);
        if (PREFETCH) touch(lds0 + L_DUMMY + 256 * lw, l1a);
        wait_vm<9 + 2 * WARM1 + PREFETCH>(); bar();
        issue(3); warm(5);
        {
            f32x2 v;
            asm volatile("ds_read_b64 %0, %1\n\ts_waitcnt lgkmcnt(0)" : "=&v"(v) : "v"(lds0 + L_MAIL + 16 * lw + 8 * (lane & 1)) : "memory");
            if (lane < 2) store16_sc1((GAS u32x4*)(ws + WS_RS) + t0 + 8 * cj + 2 * lw + lane, u32x4{__float_as_uint(v[0]), tag, __float_as_uint(v[1]), tag});
        }
        wait_vm<10 + 2 * WARM1 + PREFETCH>(); bar();
        issue(4); warm(6);
        {
            float a, b, c;
            const unsigned ma = lds0 + L_MAIL + 64 + 4 * (((lw >> 1) * 5 + 3 * (lw & 1)) * 16 + li);
            asm volatile("ds_read_b32 %0, %3\n\tds_read_b32 %1, %3 offset:64\n\tds_read_b32 %2, %3 offset:128\n\ts_waitcnt lgkmcnt(0)"
                         : "=&v"(a), "=&v"(b), "=&v"(c) : "v"(ma) : "memory");
            if (g == 0) store16_sc1((GAS u32x4*)(ws + WS_CS) + ((cj * 16 + ci) * 2 + (lw >> 1)) * 32 + 16 * (lw & 1) + li,
                                    u32x4{__float_as_uint(a), __float_as_uint(b), __float_as_uint(c), tag});
        }
        wait_vm<11 + 2 * WARM1>(); bar();
        issue(5); warm(7); wait_vm<10 + 2 * WARM1>(); bar();
        issue(6); wait_vm<9 + WARM1>(); bar();
        issue(7);
        u32x4 rs, cs[4];
        auto gather = [&]() {
            asm volatile("global_load_dwordx4 %0, %5, off sc1\n\t"
                         "global_load_dwordx4 %1, %6, off sc1\n\tglobal_load_dwordx4 %2, %6, off offset:256 sc1\n\t"
                         "global_load_dwordx4 %3, %6, off offset:1024 sc1\n\tglobal_load_dwordx4 %4, %6, off offset:1280 sc1"
                         : "=&v"(rs), "=&v"(cs[0]), "=&v"(cs[1]), "=&v"(cs[2]), "=&v"(cs[3]) : "v"(rsp), "v"(csp) : "memory");
        };
        gather();
        wait_vm<14>(); bar();
        dmaW(w2g, 0); dmaW(w2g + 65536, P2_SLOT); dmaW(w2g + 2 * 65536, 2 * P2_SLOT); dmaW(w2g + 3 * 65536, 3 * P2_SLOT);
        if (PREF2 == 1) { touch(lds0 + L_DUMMY + 256 * lw, l2a[0]); touch(lds0 + L_DUMMY + 256 * lw, l2a[1]); }
        asm volatile("s_waitcnt vmcnt(%5)" : "+v"(rs), "+v"(cs[0]), "+v"(cs[1]), "+v"(cs[2]), "+v"(cs[3]) : "i"(PREF2 == 1 ? 10 : 8) : "memory");
        bar();
        dmaW(w2g + 4 * 65536, 4 * P2_SLOT); dmaW(w2g + 5 * 65536, 5 * P2_SLOT);
        auto fresh = [&]() {
            return __all(rs[1] == tag && rs[3] == tag && cs[0][3] == tag && cs[1][3] == tag && cs[2][3] == tag && cs[3][3] == tag);
        };
#pragma unroll 1
        for (unsigned spins = 0; spins < SPIN_CAP && !fresh(); ++spins) {
            gather();
            asm volatile("s_waitcnt vmcnt(0)" : "+v"(rs), "+v"(cs[0]), "+v"(cs[1]), "+v"(cs[2]), "+v"(cs[3])::"memory");
        }
        if (lw < 2) {
            const float m = __uint_as_float(rs[0]), mb = rbf(m);
            const float r1 = __builtin_amdgcn_rcpf(bf_lo(rs[2])), r12 = r1 * __builtin_amdgcn_rcpf(bf_hi(rs[2]));
            const int tau = 64 * lw + lane;
            const unsigned ta = lds0 + L_TAB1 + 4 * (((tau >> 4) * 8 + (tau & 7)) * 4 + ((tau >> 3) & 1));
            lds_st32(ta, r1); lds_st32(ta + 8, -r1 * mb);
            lds_st32(ta + 1024, r12); lds_st32(ta + 1032, -r12 * m);
            lds_st64(lds0 + L_TOK + 8 * tau, f32x2{mb, r1});
        }
        bar();
        dmaW(w2g + 6 * 65536, 6 * P2_SLOT); dmaW(w2g + 7 * 65536, 7 * P2_SLOT); dmaW(w2g + 8 * 65536, 8 * P2_SLOT);
        {
            float v5[5];
#pragma unroll
            for (int k = 0; k < 3; ++k) v5[k] = __uint_as_float(cs[0][k]) + __uint_as_float(cs[2][k]);
#pragma unroll
            for (int k = 0; k < 2; ++k) v5[3 + k] = __uint_as_float(cs[1][k]) + __uint_as_float(cs[3][k]);
#pragma unroll
            for (int k = 0; k < 5; ++k) v5[k] = sum_groups(v5[k]);
            if (g == 0) {
#pragma unroll
                for (int k = 0; k < 5; ++k) lds_st32(lds0 + L_CS + 4 * ((((lw >> 1) * 2 + (lw & 1)) * 5 + k) * 16 + li), v5[k]);
            }
        }
        asm volatile("s_waitcnt lgkmcnt(0)" ::: "memory");
        bar();
        STAMP(2);
        if (PREF2 == 2) { touch(lds0 + L_DUMMY + 256 * lw, l2a[0]); touch(lds0 + L_DUMMY + 256 * lw, l2a[1]); }
        bar();
        bar();
        if (PREF2 == 3) { touch(lds0 + L_DUMMY + 256 * lw, l2a[0]); touch(lds0 + L_DUMMY + 256 * lw, l2a[1]); }
        __builtin_amdgcn_s_setprio(LPRIO2);
        wait_flags((const GAS unsigned*)(ws + WS_FH) + T * 32, tag);
        STAMP(3);
        dmaA(ag, 0); dmaA(ag + 8192, P2_SLOT);
        if (WARMA == 1)
            touch(lds0 + L_DUMMY + 256 * lw, ag + (128 + 156 * (cj & 7) + 39 * lw + min(lane, 38)) * 128);
        dmaA(ag + 2 * 8192, 2 * P2_SLOT); dmaA(ag + 3 * 8192, 3 * P2_SLOT);
        wait_vm<4 + (WARMA == 1)>(); bar();
        STAMP(4);
        dmaA(ag + 4 * 8192, 4 * P2_SLOT); dmaA(ag + 5 * 8192, 5 * P2_SLOT); dmaA(ag + 6 * 8192, 6 * P2_SLOT); dmaA(ag + 7 * 8192, 7 * P2_SLOT);
        wait_vm<8>(); bar();
        const GAS char* ap = ag + 9 * 8192;
        const GAS char* wp = w2g + 9 * 65536;
        unsigned slot = 0;
        auto adv = [&]() { ap += 8192; slot = slot == P2_RING - P2_SLOT ? 0 : slot + P2_SLOT; };
        auto stW = [&]() { dmaA(ap, slot); dmaW(wp, slot); wp += 65536; adv(); };
        auto stC = [&]() { dmaA(ap, slot); dmaC(wp, slot); wp += 128; adv(); };
        dmaA(ag + 8 * 8192, 8 * P2_SLOT); stW();
        wait_vm<10>(); bar(); stW(); stW();
        wait_vm<14>(); bar(); stW(); stW();
#pragma unroll 1
        for (int j = 4; j < 7; ++j) { wait_vm<16>(); bar(); stW(); stW(); }
        wait_vm<16>(); bar(); stW();
        ap -= 4096; wp -= 32768;
        stW();
        wp = (const GAS char*)cre - 3072;
        wait_vm<16>(); bar();
        if (WARMA) touch(lds0 + L_DUMMY + 256 * lw, ag + (FF + 128 * (cj & 7) + 32 * lw + (lane & 31)) * 128);
        stC(); stC();
#pragma unroll 1
        for (int j = 9; j < 12; ++j) { wait_vm<16>(); bar(); stC(); stC(); }
        wp = (const GAS char*)cim - 3072;
#pragma unroll 1
        for (int j = 12; j < 16; ++j) { wait_vm<16>(); bar(); stC(); stC(); }
        wait_vm<16>(); bar();
        wait_vm<8>(); bar();
        wait_vm<0>(); bar();
        STAMP(5);
        return;
    }

    const int wm = wave >> 1, wn = wave & 1;
    const int p = p0 + 16 * wn + li;
    cf lam, l8, l16, l128, lsig, coef2;
    float b1c[3];
    unsigned tag;
    {
        u32x4 xr[2];
        unsigned short pr[6];
#pragma unroll
        for (int i = 0; i < 2; ++i) xr[i] = *(const u32x4*)(x + (t0 + 8 * cj + 2 * wave + i) * DM + lane * 8);
        pr[0] = lam_re[p]; pr[1] = lam_im[p]; pr[2] = log_dt[p];
#pragma unroll
        for (int c = 0; c < 3; ++c) pr[3 + c] = b1[min(f0 + 16 * (3 * wn + c) + li, FF - 1)];
        SB0;
        const float lr = bf2f(pr[0]), lim = bf2f(pr[1]), dt = rbf(exp_f(bf2f(pr[2])));
#pragma unroll
        for (int c = 0; c < 3; ++c) b1c[c] = bf2f(pr[3 + c]);
        float mean[2], var[2];
#pragma unroll
        for (int i = 0; i < 2; ++i) {
            float e[8], s = 0.0f;
#pragma unroll
            for (int j = 0; j < 4; ++j) { e[2 * j] = bf_lo(xr[i][j]); e[2 * j + 1] = bf_hi(xr[i][j]); s += e[2 * j] + e[2 * j + 1]; }
            mean[i] = wave_sum(s) * (1.0f / DM);
            float qq = 0.0f;
#pragma unroll
            for (int j = 0; j < 8; ++j) qq = fmaf(e[j] - mean[i], e[j] - mean[i], qq);
            var[i] = wave_sum(qq) * (1.0f / DM);
        }
        if (lane < 2) ((LAS f32x2*)(smem + L_MAIL))[2 * wave + lane] = f32x2{lane ? mean[1] : mean[0], __uint_as_float(row_std(lane ? var[1] : var[0]))};
        const float mag = exp_f(dt * lr);
        float sn, cs;
        sincos_f(dt * lim, sn, cs);
        lam = cf{mag * cs, mag * sn};
        const float nr = lam.r - 1.0f, ni = lam.i, den = 1.0f / (lr * lr + lim * lim);
        coef2 = cf{2.0f * (nr * lr + ni * lim) * den, 2.0f * (ni * lr - nr * lim) * den};
        const cf l2 = cmul(lam, lam), l4 = cmul(l2, l2);
        l8 = cmul(l4, l4); l16 = cmul(l8, l8);
        const cf l32 = cmul(l16, l16), l64 = cmul(l32, l32);
        l128 = cmul(l64, l64);
        lsig = (g & 1) ? l16 : cf{1.0f, 0.0f};
        if (g & 2) lsig = cmul(lsig, l32);
        if (wm) lsig = cmul(lsig, l64);
        asm volatile("" : "+v"(lam.r), "+v"(lam.i), "+v"(l8.r), "+v"(l8.i), "+v"(l16.r), "+v"(l16.i), "+v"(l128.r), "+v"(l128.i), "+v"(lsig.r), "+v"(lsig.i),
                          "+v"(coef2.r), "+v"(coef2.i), "+v"(b1c[0]), "+v"(b1c[1]), "+v"(b1c[2]));
    }
    f32x4 acc[4][5];
    {
        const int sw = 16 * (g ^ (lane & 7));
        const int xa0 = P1_X + (64 * wm + li) * 128 + sw;
        const int ba0 = P1_B + (16 * wn + li) * 128 + sw;
        int wa[2][2];
#pragma unroll
        for (int t = 0; t < 2; ++t)
#pragma unroll
            for (int cl = 0; cl < 2; ++cl) {
                const int par = (3 * wn + cl) & 1;
                wa[t][cl] = P1_W + 1536 * g + 64 * q + 256 * t + 32 * (par ^ (g & 1)) + 16 * ((pp >> 1) ^ t) + 8 * (pp & 1) + (cl == 0 ? 512 * wn : 1024 * wn);
            }
#pragma unroll
        for (int b = 0; b < 4; ++b)
#pragma unroll
            for (int c = 0; c < 5; ++c) acc[b][c] = f32x4{0, 0, 0, 0};
        s16x8 xa[2][4], wb[2][5];
        auto rd = [&](int slot, int ks, int buf) {
            LAS char* s = smem + slot * P1_SLOT;
            LAS char* xs = s + (xa0 ^ (64 * ks));
            LAS char* bs = s + (ba0 ^ (64 * ks));
#pragma unroll
            for (int b = 0; b < 4; ++b) xa[buf][b] = lds_row(xs + b * 2048);
            wb[buf][0] = lds_row(bs);
            wb[buf][1] = lds_row(bs + 4096);
            wb[buf][2] = lds_tr(s + wa[0][0] + 6144 * ks, s + wa[1][0] + 6144 * ks);
            wb[buf][3] = lds_tr(s + wa[0][1] + 6144 * ks, s + wa[1][1] + 6144 * ks);
            wb[buf][4] = lds_tr(s + wa[0][0] + 6144 * ks + 512, s + wa[1][0] + 6144 * ks + 512);
        };
        auto mm = [&](int buf) {
#pragma unroll
            for (int c = 0; c < 5; ++c)
#pragma unroll
                for (int b = 0; b < 4; ++b) acc[b][c] = mfma(xa[buf][b], wb[buf][c], acc[b][c]);
        };
        const int colsum_ks = wm == 0 ? (ci & 1) : -1;
        auto colsum = [&](int buf) {
            const s16x8 ones = {0x3f80, 0x3f80, 0x3f80, 0x3f80, 0x3f80, 0x3f80, 0x3f80, 0x3f80};
            float v5[5];
#pragma unroll
            for (int c = 0; c < 5; ++c) v5[c] = mfma(ones, wb[buf][c], f32x4{0, 0, 0, 0})[0];
            if (g == 0) {
#pragma unroll
                for (int c = 0; c < 5; ++c) ((LAS float*)(smem + L_MAIL + 64))[(wn * 5 + c) * 16 + li] = v5[c];
            }
        };
        asm volatile("s_waitcnt vmcnt(0) lgkmcnt(0)" : "+v"(epoch)::"memory");
        tag = __builtin_amdgcn_readfirstlane(epoch) + 1u;
        asm volatile("" : "+s"(tag));
        bar();
        STAMP(1);
        rd(0, 0, 0);
        wait_lgkm0();
#pragma unroll 1
        for (int t = 0; t < 7; ++t) {
            SB0;
            rd(t & 3, 1, 1); mm(0); interleave<20, 12>();
            SB0;
            if (t == 0 && colsum_ks == 0) colsum(0);
            SB0; wait_lgkm0(); bar();
            rd((t + 1) & 3, 0, 0); mm(1); interleave<20, 12>();
            SB0;
            if (t == 0 && colsum_ks == 1) colsum(1);
            SB0; wait_lgkm0();
        }
        rd(3, 1, 1); mm(0); interleave<20, 12>();
        SB0; wait_lgkm0(); bar();
        mm(1);
        bar();
    }
    STAMP(2);

    LAS f32x4* tab1 = (LAS f32x4*)(smem + L_TAB1) + (wm * 4 + g) * 8;
    LAS f32x4* tab2 = (LAS f32x4*)(smem + L_TAB2) + (wm * 4 + g) * 8;
    auto csum = [&](int k) { return ((LAS float*)(smem + L_CS))[(wn * 5 + k) * 16 + li] + ((LAS float*)(smem + L_CS))[((2 + wn) * 5 + k) * 16 + li]; };
    u32x2 xv[2][2], dv[2], bv[2];
#pragma unroll
    for (int i = 0; i < 2; ++i) {
        const int d = d0 + 32 * (wave >> 1) + 16 * i + 4 * g;
        dv[i] = *(const u32x2*)(dskip + d); bv[i] = *(const u32x2*)(b2 + d);
#pragma unroll
        for (int j = 0; j < 2; ++j) xv[i][j] = *(const u32x2*)(x + (64 * T + 32 * j + 16 * ((lane >> 3) & 1) + 8 * (wave & 1) + (lane & 7)) * DM + d);
    }
    const int Tw = 2 * ci + wm;
    const __amdgpu_buffer_rsrc_t act = __builtin_amdgcn_make_buffer_rsrc((void*)(ws_ + WS_ACT), 0, 32 * ACT_TILE, 0x00020000);
    const unsigned act0 = Tw * ACT_TILE;
    {
        f32x4 tb[8];
#pragma unroll
        for (int j = 0; j < 8; ++j) tb[j] = tab2[j];
        constexpr float C1 = -2.30220819f;
        constexpr float C2 = C1 * 0.044715f;
#pragma unroll
        for (int c = 0; c < 3; ++c) {
            const int col = 16 * (3 * wn + c) + li;
            const float s1c = csum(2 + c);
            f32x2 y[8];
#pragma unroll
            for (int j = 0; j < 8; ++j) {
                const int b = j >> 1, r = 2 * (j & 1);
                const f32x2 a = {tb[j][0], tb[j][1]}, bb = {tb[j][2], tb[j][3]};
                const f32x2 z = pfma(a, f32x2{acc[b][2 + c][r], acc[b][2 + c][r + 1]}, pfma(bb, splat(s1c), splat(b1c[c])));
                const f32x2 u = z * pfma(z * z, splat(C2), splat(C1));
                const f32x2 d = f32x2{__builtin_amdgcn_exp2f(u[0]), __builtin_amdgcn_exp2f(u[1])} + splat(1.0f);
                y[j] = z * f32x2{__builtin_amdgcn_rcpf(d[0]), __builtin_amdgcn_rcpf(d[1])};
            }
            if (3 * wn + c < nfu) {
                const int k = f0 + col;
                const unsigned dst = act0 + (k >> 3) * 1024 + (k & 7) * 64 + 16 * g;
#pragma unroll
                for (int sgm = 0; sgm < 2; ++sgm)
                    __builtin_amdgcn_raw_buffer_store_b128(u32x4{pk_bf16(y[0][sgm], y[1][sgm]), pk_bf16(y[2][sgm], y[3][sgm]), pk_bf16(y[4][sgm], y[5][sgm]), pk_bf16(y[6][sgm], y[7][sgm])},
                                                           act, dst + 512 * sgm, 0, ST_AUX);
            }
        }
    }
    STAMP(3);

    f32x2 ure[8], uim[8];
    {
        const float sbr = csum(0), sbi = csum(1);
#pragma unroll
        for (int j = 0; j < 8; ++j) {
            const f32x4 tb = tab1[j];
            const f32x2 a = {tb[0], tb[1]}, bb = {tb[2], tb[3]};
            const int b = j >> 1, r = 2 * (j & 1);
            ure[j] = pfma(a, f32x2{acc[b][0][r], acc[b][0][r + 1]}, bb * splat(sbr));
            uim[j] = pfma(a, f32x2{acc[b][1][r], acc[b][1][r + 1]}, bb * splat(sbi));
        }
    }
    f32x2 hr = ure[0], hi = uim[0];
#pragma unroll
    for (int j = 1; j < 8; ++j) {
        const f32x2 tr = pfma(splat(lam.r), hr, ure[j]), ti = pfma(splat(lam.r), hi, uim[j]);
        const f32x2 nr = pfma(splat(-lam.i), hi, tr), ni = pfma(splat(lam.i), hr, ti);
        hr = nr; hi = ni;
    }
    const cf eA = cf{hr[0], hi[0]};
    const cf e = cfma(l8, eA, cf{hr[1], hi[1]});
    const int sig = 4 * wm + g;
    LAS f32x2* eL = (LAS f32x2*)(smem + L_EL);
    eL[sig * 32 + 16 * wn + li] = f32x2{e.r, e.i};
    wait_lgkm0();
    bar();
    cf Lp = cf{0.0f, 0.0f};
#pragma unroll
    for (int s = 0; s < 7; ++s) {
        const f32x2 v = eL[s * 32 + 16 * wn + li];
        const cf n = cfma(l16, Lp, cf{v[0], v[1]});
        if (s < sig) Lp = n;
    }
    wait_vm<0>();
    if (lane == 0) __hip_atomic_store((GAS unsigned*)(ws + WS_FH) + Tw * 32 + 2 * cj + wn, tag, __ATOMIC_RELAXED, __HIP_MEMORY_SCOPE_AGENT);
    GAS u32x4* eg = (GAS u32x4*)(ws + WS_EG);
    if (sig == 7) {
        const cf E = cfma(l16, Lp, e);
        store16_sc1(eg + ci * NP + p, u32x4{__float_as_uint(E.r), tag, __float_as_uint(E.i), tag});
    }
    bar();
    STAMP(4);

    const int wd = wave >> 1, wt = wave & 1;
    f32x4 o[2][2];
    cf Hin = cf{0.0f, 0.0f};
    {
#pragma unroll
        for (int j = 0; j < 2; ++j) {
            const f32x2 st = ((LAS f32x2*)(smem + L_TOK))[64 * (T & 1) + 32 * j + 16 * ((lane >> 3) & 1) + 8 * wt + (lane & 7)];
#pragma unroll
            for (int i = 0; i < 2; ++i) {
                const float xe[4] = {bf_lo(xv[i][j][0]), bf_hi(xv[i][j][0]), bf_lo(xv[i][j][1]), bf_hi(xv[i][j][1])};
                const float de[4] = {bf_lo(dv[i][0]), bf_hi(dv[i][0]), bf_lo(dv[i][1]), bf_hi(dv[i][1])};
                const float be[4] = {bf_lo(bv[i][0]), bf_hi(bv[i][0]), bf_lo(bv[i][1]), bf_hi(bv[i][1])};
#pragma unroll
                for (int k = 0; k < 4; ++k) {
                    const float xn = rbf(rbf(xe[k] - st[0]) * st[1]);
                    o[i][j][k] = xe[k] + rbf(xn * de[k]) + be[k];
                }
            }
        }
        asm volatile("" : "+v"(o[0][0]), "+v"(o[0][1]), "+v"(o[1][0]), "+v"(o[1][1]));
        const GAS u32x4* ep = eg + g * NP + p;
        u32x4 gr[4];
        auto fetch = [&]() {
            asm volatile("global_load_dwordx4 %0, %4, off sc1\n\tglobal_load_dwordx4 %1, %5, off sc1\n\t"
                         "global_load_dwordx4 %2, %6, off sc1\n\tglobal_load_dwordx4 %3, %7, off sc1"
                         : "=&v"(gr[0]), "=&v"(gr[1]), "=&v"(gr[2]), "=&v"(gr[3])
                         : "v"(ep), "v"(ep + 4 * NP), "v"(ep + 8 * NP), "v"(ep + 12 * NP) : "memory");
        };
        auto landed = [&]() { asm volatile("s_waitcnt vmcnt(0)" : "+v"(gr[0]), "+v"(gr[1]), "+v"(gr[2]), "+v"(gr[3])::"memory"); };
        auto complete = [&]() {
            bool ok = true;
#pragma unroll
            for (int i = 0; i < 4; ++i) ok &= (4 * i + g >= ci) | ((gr[i][1] == tag) & (gr[i][3] == tag));
            return __all(ok);
        };
        fetch();
        const cf l256 = cmul(l128, l128), l512 = cmul(l256, l256);
        landed();
#pragma unroll 1
        for (unsigned spins = 0; spins < SPIN_CAP && !complete(); ++spins) { __builtin_amdgcn_s_sleep(2); fetch(); landed(); }
        cf hp = cf{0.0f, 0.0f};
#pragma unroll
        for (int i = 0; i < 4; ++i) {
            const cf n = cfma(l512, hp, cf{__uint_as_float(gr[i][0]), __uint_as_float(gr[i][2])});
            if (4 * i + g < ci) hp = n;
        }
        const int rr = (ci - 1 - g) & 3;
        if (rr & 1) hp = cmul(hp, l128);
        if (rr & 2) hp = cmul(hp, l256);
        Hin = cf{sum_groups(hp.r), sum_groups(hp.i)};
    }
    STAMP(5);
    u32x4 hq[2][2];
    unsigned hdr, hdi;
    auto store_h = [&]() {
#pragma unroll
        for (int sgm = 0; sgm < 2; ++sgm) {
            __builtin_amdgcn_raw_buffer_store_b128(hq[0][sgm], act, hdr + 512 * sgm, 0, ST_AUX);
            __builtin_amdgcn_raw_buffer_store_b128(hq[1][sgm], act, hdi + 512 * sgm, 0, ST_AUX);
        }
    };
    {
        const cf cA = cfma(lsig, Hin, Lp);
        const cf cB = cfma(l8, cA, eA);
        f32x2 sr = {cA.r, cB.r}, si = {cA.i, cB.i};
        f32x2 ore[8], oim[8];
#pragma unroll
        for (int j = 0; j < 8; ++j) {
            const f32x2 tr = pfma(splat(lam.r), sr, ure[j]), ti = pfma(splat(lam.r), si, uim[j]);
            const f32x2 nr = pfma(splat(-lam.i), si, tr), ni = pfma(splat(lam.i), sr, ti);
            sr = nr; si = ni;
            ore[j] = pfma(splat(coef2.r), sr, splat(-coef2.i) * si);
            oim[j] = pfma(splat(-coef2.r), si, splat(-coef2.i) * sr);
        }
        const int kr = FF + p, kim = FF + NP + p;
        hdr = act0 + (kr >> 3) * 1024 + (kr & 7) * 64 + 16 * g;
        hdi = act0 + (kim >> 3) * 1024 + (kim & 7) * 64 + 16 * g;
#pragma unroll
        for (int sgm = 0; sgm < 2; ++sgm) {
            hq[0][sgm] = u32x4{pk_bf16(ore[0][sgm], ore[1][sgm]), pk_bf16(ore[2][sgm], ore[3][sgm]), pk_bf16(ore[4][sgm], ore[5][sgm]), pk_bf16(ore[6][sgm], ore[7][sgm])};
            hq[1][sgm] = u32x4{pk_bf16(oim[0][sgm], oim[1][sgm]), pk_bf16(oim[2][sgm], oim[3][sgm]), pk_bf16(oim[4][sgm], oim[5][sgm]), pk_bf16(oim[6][sgm], oim[7][sgm])};
        }
        if (!DEFER) store_h();
    }
    STAMP(6);

    {
        const int trb = (g >> 1) * 2080 + (g & 1) * 1056 + 64 * q + 16 * (pp >> 1) + 8 * (pp & 1);
        const int tra = trb + 512 * wt, trw = P2_IMG + trb + 512 * wd;
        const int ca0 = P2_IMG + (32 * wd + li) * 128 + 16 * (g ^ (lane & 7));
        s16x8 wf[2][2][2], af[2][2][2];
        int slot = 0;
        auto next = [&]() { slot = slot == P2_RING - P2_SLOT ? 0 : slot + P2_SLOT; };
        auto frag_a = [&](LAS char* sl, int buf, int ks, int i) { LAS char* a = sl + tra + 4160 * ks + 32 * i; af[buf][ks][i] = lds_tr(a, a + 256); };
        auto frag_w = [&](LAS char* sl, int buf, int ks, int i, bool g4) {
            if (g4) { LAS char* a = sl + trw + 4160 * ks + 32 * i; wf[buf][ks][i] = lds_tr(a, a + 256); }
            else wf[buf][ks][i] = lds_row(sl + ((ca0 ^ (64 * ks)) + 2048 * i));
        };
        auto half = [&](int rb, bool rd, bool g4, int k0, bool mm_) {
            LAS char* sl = smem + slot;
            const int ub = rb ^ 1;
#pragma unroll
            for (int ks = 0; ks < 2; ++ks) {
#pragma unroll
                for (int m = 0; m < 4; ++m) {
                    if (mm_ && ks >= k0) mfma_acc(o[m >> 1][m & 1], wf[ub][ks][m >> 1], af[ub][ks][m & 1]);
                    SB0;
                    if (rd) { if (m < 2) frag_a(sl, rb, ks, m); else frag_w(sl, rb, ks, m - 2, g4); }
                    SB0;
                }
            }
            if (rd) next();
        };
        const GAS unsigned* fsp = (const GAS unsigned*)(ws + WS_FS) + T * 32 + (lane & 31);
        const unsigned fsl = (unsigned)(size_t)smem + L_EL + 256 * wave;
        __builtin_amdgcn_s_setprio(CPRIO2);
        bar(); half(0, true, true, 0, false);
        STAMP(7);
        if (DEFER) store_h();
        half(1, true, true, 0, true);
#pragma unroll 1
        for (int j = 1; j < 10; ++j) {
            bar(); half(0, true, true, 0, true);
            half(1, true, true, 0, true);
            if (j == 1 + DEFER) {
                wait_vm<0>();
                if (lane == 0) __hip_atomic_store((GAS unsigned*)(ws + WS_FS) + Tw * 32 + 2 * cj + wn, tag, __ATOMIC_RELAXED, __HIP_MEMORY_SCOPE_AGENT);
            }
            if (j == 6) asm volatile("s_mov_b32 m0, %0\n\ts_nop 0\n\tglobal_load_lds_dword %1, off sc1" ::"s"(fsl), "v"(fsp) : "memory");
            if (j == 7) {
                unsigned fsv;
                asm volatile("s_waitcnt vmcnt(0)\n\tds_read_b32 %0, %1\n\ts_waitcnt lgkmcnt(0)" : "=&v"(fsv) : "v"(fsl + 4 * lane) : "memory");
                if (!__all(fsv == tag)) wait_flags((const GAS unsigned*)(ws + WS_FS) + T * 32, tag);
            }
            SB0;
        }
        bar(); half(0, true, true, 0, true);
        half(1, true, true, 0, true);
        bar(); half(0, true, false, 1, true);
        half(1, true, false, 0, true);
#pragma unroll 1
        for (int j = 12; j < 19; ++j) {
            bar(); half(0, true, false, 0, true);
            half(1, true, false, 0, true);
        }
        half(0, false, false, 0, true);
        asm volatile("s_nop 7\n\ts_nop 7" : "+v"(o[0][0]), "+v"(o[0][1]), "+v"(o[1][0]), "+v"(o[1][1]));
        STAMP(8);

#pragma unroll
        for (int j = 0; j < 2; ++j)
#pragma unroll
            for (int i = 0; i < 2; ++i)
                *(f32x4*)(out + (64 * T + 32 * j + 16 * ((lane >> 3) & 1) + 8 * wt + (lane & 7)) * DM + d0 + 32 * wd + 16 * i + 4 * g) = o[i][j];
        if (ci == 15 && cj == 0 && threadIdx.x == 0) __hip_atomic_store((GAS unsigned*)(ws + WS_EP), tag, __ATOMIC_RELAXED, __HIP_MEMORY_SCOPE_AGENT);
        STAMP(9);
    }
}

}

extern "C" void kernel_launch(void* const* d_in, const int* in_sizes, int n_in, void* d_out, int out_size, void* d_ws, size_t ws_size,
                              hipStream_t stream) {
    typedef const unsigned short* P;
    (void)hipFuncSetAttribute((const void*)k_fused, hipFuncAttributeMaxDynamicSharedMemorySize, LDS_BYTES);
    k_fused<<<256, 512, LDS_BYTES, stream>>>((P)d_in[0], (P)d_in[1], (P)d_in[2], (P)d_in[3], (P)d_in[4], (P)d_in[5], (P)d_in[6], (P)d_in[7],
                                             (P)d_in[8], (P)d_in[10], (P)d_in[11], (P)d_in[12], (P)d_in[13], (char*)d_ws, (float*)d_out);
}
```

```cpp
#include <hip/hip_runtime.h>
#include <hip/hip_bf16.h>
#include <stdint.h>

#define LAS __attribute__((address_space(3)))
#define GAS __attribute__((address_space(1)))

typedef __attribute__((ext_vector_type(8))) short s16x8;
typedef __attribute__((ext_vector_type(4))) short s16x4;
typedef __attribute__((ext_vector_type(8))) __bf16 bf16x8;
typedef __attribute__((ext_vector_type(2))) __bf16 bf16x2;
typedef __attribute__((ext_vector_type(4))) float f32x4;
typedef __attribute__((ext_vector_type(2))) float f32x2;
typedef __attribute__((ext_vector_type(4))) unsigned u32x4;
typedef __attribute__((ext_vector_type(2))) unsigned u32x2;

namespace {

constexpr int SEQ = 2048, DM = 512, NP = 512, FF = 1376;
constexpr int KACT = FF + 2 * NP;
constexpr int ACT_TILE = KACT * 128;
constexpr size_t WS_ACT = 0;
constexpr size_t WS_ROWSTAT = 10u << 20;
constexpr size_t WS_COLSUM = WS_ROWSTAT + (64u << 10);
constexpr size_t WS_EG = WS_ROWSTAT + (128u << 10);
constexpr size_t WS_STAMP = 200u << 20;
constexpr float LN_EPS = 1e-5f;

#define STAMP(k, s) do {} while (0)

template <int N> __device__ __forceinline__ void wait_vm() { asm volatile("s_waitcnt vmcnt(%0)" ::"i"(N) : "memory"); }
__device__ __forceinline__ void wait_lgkm0() { __builtin_amdgcn_s_waitcnt(0xC07F); asm volatile("" ::: "memory"); }
template <int NM, int ND, int DPM = 1, int I = 0> __device__ __forceinline__ void interleave() {
    if constexpr (I * DPM < ND && I < NM) {
        __builtin_amdgcn_sched_group_barrier(0x008, 1, 0);
        __builtin_amdgcn_sched_group_barrier(0x100, (ND - I * DPM) < DPM ? (ND - I * DPM) : DPM, 0);
        interleave<NM, ND, DPM, I + 1>();
    } else if constexpr (I < NM) {
        __builtin_amdgcn_sched_group_barrier(0x008, NM - I, 0);
    }
}
__device__ __forceinline__ void bar() {
    __builtin_amdgcn_sched_barrier(0);
    asm volatile("" ::: "memory");
    __builtin_amdgcn_s_barrier();
    asm volatile("" ::: "memory");
    __builtin_amdgcn_sched_barrier(0);
}
__device__ __forceinline__ float bf_lo(unsigned u) { return __uint_as_float(u << 16); }
__device__ __forceinline__ float bf_hi(unsigned u) { return __uint_as_float(u & 0xffff0000u); }
__device__ __forceinline__ float bf2f(unsigned short h) { return __uint_as_float((unsigned)h << 16); }
__device__ __forceinline__ unsigned pk_bf16(float a, float b) { bf16x2 v = {(__bf16)a, (__bf16)b}; return __builtin_bit_cast(unsigned, v); }
__device__ __forceinline__ float rbf(float a) { return bf_lo(pk_bf16(a, a)); }
__device__ __forceinline__ f32x2 pfma(f32x2 a, f32x2 b, f32x2 c) { return __builtin_elementwise_fma(a, b, c); }
__device__ __forceinline__ f32x2 splat(float a) { return f32x2{a, a}; }
__device__ __forceinline__ f32x4 mfma(s16x8 a, s16x8 b, f32x4 c) {
    return __builtin_amdgcn_mfma_f32_16x16x32_bf16(__builtin_bit_cast(bf16x8, a), __builtin_bit_cast(bf16x8, b), c, 0, 0, 0);
}
__device__ __forceinline__ s16x8 lds_row(LAS char* p) { return *(LAS s16x8*)p; }
__device__ __forceinline__ s16x8 lds_tr(LAS char* p0, LAS char* p1) {
    s16x4 a = __builtin_amdgcn_ds_read_tr16_b64_v4i16((LAS s16x4*)p0);
    s16x4 b = __builtin_amdgcn_ds_read_tr16_b64_v4i16((LAS s16x4*)p1);
    return __builtin_shufflevector(a, b, 0, 1, 2, 3, 4, 5, 6, 7);
}
__device__ __forceinline__ void dma16(const GAS char* g, LAS char* l) {
    __builtin_amdgcn_global_load_lds((const GAS unsigned*)g, (LAS unsigned*)l, 16, 0, 0);
}
__device__ __forceinline__ float wave_sum(float v) {
#pragma unroll
    for (int m = 32; m >= 1; m >>= 1) v += __shfl_xor(v, m);
    return v;
}
struct cf { float r, i; };
__device__ __forceinline__ cf cmul(cf a, cf b) { return cf{a.r * b.r - a.i * b.i, a.r * b.i + a.i * b.r}; }
__device__ __forceinline__ cf cfma(cf a, cf b, cf c) { return cf{fmaf(a.r, b.r, fmaf(-a.i, b.i, c.r)), fmaf(a.r, b.i, fmaf(a.i, b.r, c.i))}; }

struct RowScale { float mb, r1, r12; };
__device__ __forceinline__ RowScale row_scale(float m, float v) {
    const float s1 = rbf(sqrtf(rbf(rbf(v) + LN_EPS)));
    const float r1 = 1.0f / s1;
    const float v2 = rbf(v * r1 * r1);
    const float s2 = rbf(sqrtf(rbf(v2 + LN_EPS)));
    return RowScale{rbf(m), r1, r1 / s2};
}

__global__ __launch_bounds__(256) void k_stats(const unsigned short* __restrict__ x, const unsigned short* __restrict__ bre,
                                               const unsigned short* __restrict__ bim, const unsigned short* __restrict__ w1, char* __restrict__ ws) {
    __shared__ float red[4][8];
    const int b = blockIdx.x, wave = threadIdx.x >> 6, lane = threadIdx.x & 63;
    if (b == 0 && threadIdx.x == 0) *(unsigned long long*)(ws + (120u << 20)) = __builtin_amdgcn_s_memrealtime();
    STAMP(0, 0);
    float2* rowstat = (float2*)(ws + WS_ROWSTAT);
    float* colsum = (float*)(ws + WS_COLSUM);
#pragma unroll
    for (int i = 0; i < 2; ++i) {
        const int row = 8 * b + 2 * wave + i;
        const u32x4 v = *(const u32x4*)(x + row * DM + lane * 8);
        float e[8];
#pragma unroll
        for (int j = 0; j < 4; ++j) { e[2 * j] = bf_lo(v[j]); e[2 * j + 1] = bf_hi(v[j]); }
        float s = 0;
#pragma unroll
        for (int j = 0; j < 8; ++j) s += e[j];
        const float m = wave_sum(s) * (1.0f / DM);
        float q = 0;
#pragma unroll
        for (int j = 0; j < 8; ++j) q += (e[j] - m) * (e[j] - m);
        const float var = wave_sum(q) * (1.0f / DM);
        if (lane == 0) rowstat[row] = make_float2(m, var);
    }
    {
        const int r = 4 * b + wave;
        const unsigned short* src = r < NP ? bre + r * DM : bim + (r - NP) * DM;
        const u32x4 v = *(const u32x4*)(src + lane * 8);
        float s = 0;
#pragma unroll
        for (int j = 0; j < 4; ++j) s += bf_lo(v[j]) + bf_hi(v[j]);
        s = wave_sum(s);
        if (lane == 0) colsum[r] = s;
    }
    if (b < FF / 8) {
        float a[8] = {};
#pragma unroll
        for (int i = 0; i < 2; ++i) {
            const u32x4 v = *(const u32x4*)(w1 + (threadIdx.x + 256 * i) * FF + b * 8);
#pragma unroll
            for (int j = 0; j < 4; ++j) { a[2 * j] += bf_lo(v[j]); a[2 * j + 1] += bf_hi(v[j]); }
        }
#pragma unroll
        for (int j = 0; j < 8; ++j) { a[j] = wave_sum(a[j]); if (lane == 0) red[wave][j] = a[j]; }
        __syncthreads();
        if (threadIdx.x < 8) colsum[1024 + b * 8 + threadIdx.x] = red[0][threadIdx.x] + red[1][threadIdx.x] + red[2][threadIdx.x] + red[3][threadIdx.x];
    }
    if (threadIdx.x < 32) ((u32x4*)(ws + WS_EG))[b * 32 + threadIdx.x] = u32x4{0, 0, 0, 0};
    STAMP(0, 1);
}

constexpr int P1_SLOT = 36 * 1024, P1_SLOTS = 4, P1_X = 0, P1_B = 16384, P1_W = 24576;
constexpr int P1_TAB = P1_SLOT * P1_SLOTS;
constexpr int P1_LDS = P1_TAB + 4096;

__global__ __launch_bounds__(512) void k_p1(const unsigned short* __restrict__ x, const unsigned short* __restrict__ lam_re,
                                            const unsigned short* __restrict__ lam_im, const unsigned short* __restrict__ bre,
                                            const unsigned short* __restrict__ bim, const unsigned short* __restrict__ log_dt,
                                            const unsigned short* __restrict__ w1, const unsigned short* __restrict__ b1, char* __restrict__ ws) {
    extern __shared__ __attribute__((aligned(16))) char smem_[];
    LAS char* smem = (LAS char*)smem_;
    const int wave = __builtin_amdgcn_readfirstlane(threadIdx.x >> 6), lane = threadIdx.x & 63;
    const int loc = blockIdx.x >> 3, ci = 2 * (blockIdx.x & 7) + (loc >> 4), cj = loc & 15;
    const int t0 = 128 * ci, p0 = 32 * cj, f0 = cj < 6 ? 96 * cj : 576 + 80 * (cj - 6), nfu = cj < 6 ? 6 : 5;

    if (wave >= 4) {
        const int lw = wave - 4;
        STAMP(1, 0 + 8);
        unsigned xo[4], bo[2], wo[3];
#pragma unroll
        for (int i = 0; i < 4; ++i) {
            const int R = 8 * (4 * lw + i) + (lane >> 3), ii = R & 15, bb = (R >> 4) & 3;
            const int tok = t0 + (R & 64) + 16 * (ii >> 2) + 8 * (ii & 1) + 2 * bb + ((ii >> 1) & 1);
            xo[i] = tok * 1024 + 16 * ((lane & 7) ^ (lane >> 3));
        }
#pragma unroll
        for (int i = 0; i < 2; ++i) {
            const int R = 8 * (2 * lw + i) + (lane >> 3);
            bo[i] = (p0 + (R & 31)) * 1024 + 16 * ((lane & 7) ^ (lane >> 3));
        }
#pragma unroll
        for (int i = 0; i < 3; ++i) {
            const int st = 2 * (3 * lw + i) + (lane >> 5), kg = st / 3, ns = st - 3 * kg, kr = (lane & 31) >> 2;
            const int ch = 4 * ns + ((lane & 3) ^ ((2 * kg + (kr >> 2)) & 3));
            const int f = min(f0 + 8 * ch, FF - 8);
            wo[i] = (8 * kg + kr) * (FF * 2) + f * 2;
        }
        const GAS char* xg = (const GAS char*)x;
        const GAS char* bg = (const GAS char*)(lw < 2 ? bre : bim);
        const GAS char* wg = (const GAS char*)w1;
        auto issue = [&](int t) {
            LAS char* s = smem + (t & 3) * P1_SLOT;
#pragma unroll
            for (int i = 0; i < 4; ++i) dma16(xg + t * 128 + xo[i], s + P1_X + (4 * lw + i) * 1024);
#pragma unroll
            for (int i = 0; i < 2; ++i) dma16(bg + t * 128 + bo[i], s + P1_B + (2 * lw + i) * 1024);
#pragma unroll
            for (int i = 0; i < 3; ++i) dma16(wg + (size_t)t * (64 * FF * 2) + wo[i], s + P1_W + (3 * lw + i) * 1024);
        };
        issue(0); issue(1); issue(2); issue(3);
        wait_vm<27>(); bar();
        STAMP(1, 1 + 8);
        wait_vm<18>(); bar(); issue(4);
        wait_vm<18>(); bar(); issue(5);
        wait_vm<18>(); bar(); issue(6);
        wait_vm<18>(); bar(); issue(7);
        wait_vm<18>(); bar();
        wait_vm<9>(); bar();
        wait_vm<0>(); bar();
        STAMP(1, 2 + 8);
        return;
    }

    const int wm = wave >> 1, wn = wave & 1, g = lane >> 4, li = lane & 15;
    STAMP(1, 0);
    const int p = p0 + 16 * wn + li;
    cf lam, l8, l16, l128, lsig, coef2;
    {
        const float lr = bf2f(lam_re[p]), lim = bf2f(lam_im[p]), dt = rbf(expf(bf2f(log_dt[p])));
        const float mag = expf(dt * lr);
        float sn, cs;
        sincosf(dt * lim, &sn, &cs);
        lam = cf{mag * cs, mag * sn};
        const float nr = lam.r - 1.0f, ni = lam.i, den = 1.0f / (lr * lr + lim * lim);
        coef2 = cf{2.0f * (nr * lr + ni * lim) * den, 2.0f * (ni * lr - nr * lim) * den};
        const cf l2 = cmul(lam, lam), l4 = cmul(l2, l2);
        l8 = cmul(l4, l4); l16 = cmul(l8, l8);
        const cf l32 = cmul(l16, l16), l64 = cmul(l32, l32);
        l128 = cmul(l64, l64);
        lsig = (g & 1) ? l16 : cf{1.0f, 0.0f};
        if (g & 2) lsig = cmul(lsig, l32);
        if (wm) lsig = cmul(lsig, l64);
    }
    const float* colsum = (const float*)(ws + WS_COLSUM);
    const float sbr = colsum[p], sbi = colsum[NP + p];
    float s1c[3], b1c[3];
#pragma unroll
    for (int c = 0; c < 3; ++c) {
        const int f = min(f0 + 16 * (3 * wn + c) + li, FF - 1);
        s1c[c] = colsum[1024 + f]; b1c[c] = bf2f(b1[f]);
    }
    if (threadIdx.x < 128) {
        const int tau = threadIdx.x;
        const float2 st = ((const float2*)(ws + WS_ROWSTAT))[t0 + tau];
        const RowScale rs = row_scale(st.x, st.y);
        const int idx = ((tau >> 4) * 8 + (tau & 7)) * 4 + ((tau >> 3) & 1);
        LAS float* ta = (LAS float*)(smem + P1_TAB);
        ta[idx] = rs.r1; ta[idx + 2] = -rs.r1 * rs.mb;
        ta[256 + idx] = rs.r12; ta[256 + idx + 2] = -rs.r12 * st.x;
    }

    const int sw = 16 * (g ^ (lane & 7));
    const int xa0 = P1_X + (64 * wm + li) * 128 + sw;
    const int ba0 = P1_B + (16 * wn + li) * 128 + sw;
    const int q = li >> 2, pp = lane & 3;
    int wa[2][2];
#pragma unroll
    for (int t = 0; t < 2; ++t)
#pragma unroll
        for (int cl = 0; cl < 2; ++cl) {
            const int par = (3 * wn + cl) & 1;
            wa[t][cl] = P1_W + 1536 * g + 64 * q + 256 * t + 32 * (par ^ (g & 1)) + 16 * ((pp >> 1) ^ t) + 8 * (pp & 1) + (cl == 0 ? 512 * wn : 1024 * wn);
        }

    f32x4 acc[4][5];
#pragma unroll
    for (int b = 0; b < 4; ++b)
#pragma unroll
        for (int c = 0; c < 5; ++c) acc[b][c] = f32x4{0, 0, 0, 0};
    s16x8 xa[2][4], wb[2][5];
    auto rd = [&](int slot, int ks, int buf) {
        LAS char* s = smem + slot * P1_SLOT;
        LAS char* xs = s + (xa0 ^ (64 * ks));
        LAS char* bs = s + (ba0 ^ (64 * ks));
#pragma unroll
        for (int b = 0; b < 4; ++b) xa[buf][b] = lds_row(xs + b * 2048);
        wb[buf][0] = lds_row(bs);
        wb[buf][1] = lds_row(bs + 4096);
        wb[buf][2] = lds_tr(s + wa[0][0] + 6144 * ks, s + wa[1][0] + 6144 * ks);
        wb[buf][3] = lds_tr(s + wa[0][1] + 6144 * ks, s + wa[1][1] + 6144 * ks);
        wb[buf][4] = lds_tr(s + wa[0][0] + 6144 * ks + 512, s + wa[1][0] + 6144 * ks + 512);
    };
    auto mm = [&](int buf) {
#pragma unroll
        for (int c = 0; c < 5; ++c)
#pragma unroll
            for (int b = 0; b < 4; ++b) acc[b][c] = mfma(xa[buf][b], wb[buf][c], acc[b][c]);
    };
    wait_lgkm0();
    bar();
    STAMP(1, 1);
    rd(0, 0, 0);
    wait_lgkm0();
#pragma unroll 1
    for (int t = 0; t < 7; ++t) {
        __builtin_amdgcn_sched_barrier(0);
        rd(t & 3, 1, 1);
        mm(0);
        interleave<20, 12>();
        __builtin_amdgcn_sched_barrier(0);
        wait_lgkm0();
        bar();
        rd((t + 1) & 3, 0, 0);
        mm(1);
        interleave<20, 12>();
        __builtin_amdgcn_sched_barrier(0);
        wait_lgkm0();
    }
    rd(3, 1, 1);
    mm(0);
    interleave<20, 12>();
    __builtin_amdgcn_sched_barrier(0);
    mm(1);
    STAMP(1, 2);

    LAS f32x4* tab1 = (LAS f32x4*)(smem + P1_TAB) + (wm * 4 + g) * 8;
    LAS f32x4* tab2 = tab1 + 64;
    f32x2 ure[8], uim[8];
#pragma unroll
    for (int j = 0; j < 8; ++j) {
        const f32x4 tb = tab1[j];
        const f32x2 a = {tb[0], tb[1]}, bb = {tb[2], tb[3]};
        const int b = j >> 1, r = 2 * (j & 1);
        ure[j] = pfma(a, f32x2{acc[b][0][r], acc[b][0][r + 1]}, bb * splat(sbr));
        uim[j] = pfma(a, f32x2{acc[b][1][r], acc[b][1][r + 1]}, bb * splat(sbi));
    }
    f32x2 hr = ure[0], hi = uim[0];
#pragma unroll
    for (int j = 1; j < 8; ++j) {
        const f32x2 tr = pfma(splat(lam.r), hr, ure[j]), ti = pfma(splat(lam.r), hi, uim[j]);
        const f32x2 nr = pfma(splat(-lam.i), hi, tr), ni = pfma(splat(lam.i), hr, ti);
        hr = nr; hi = ni;
    }
    const cf eA = cf{hr[0], hi[0]};
    const cf e = cfma(l8, eA, cf{hr[1], hi[1]});
    const int sig = 4 * wm + g;
    LAS f32x2* eL = (LAS f32x2*)(smem + P1_TAB + 2048);
    eL[sig * 32 + 16 * wn + li] = f32x2{e.r, e.i};
    wait_lgkm0();
    bar();
    cf Lp = cf{0.0f, 0.0f};
#pragma unroll
    for (int s = 0; s < 7; ++s) {
        const f32x2 v = eL[s * 32 + 16 * wn + li];
        const cf n = cfma(l16, Lp, cf{v[0], v[1]});
        if (s < sig) Lp = n;
    }
    GAS u32x4* eg = (GAS u32x4*)(ws + WS_EG);
    if (sig == 7) {
        const cf E = cfma(l16, Lp, e);
        const u32x4 gr = {__float_as_uint(E.r), 1u, __float_as_uint(E.i), 1u};
        asm volatile("global_store_dwordx4 %0, %1, off sc1\n\ts_nop 1" ::"v"(eg + ci * NP + p), "v"(gr) : "memory");
    }
    STAMP(1, 3);

    GAS char* act = (GAS char*)(ws + WS_ACT) + (size_t)(2 * ci + wm) * ACT_TILE;
    {
        f32x4 tb[8];
#pragma unroll
        for (int j = 0; j < 8; ++j) tb[j] = tab2[j];
        constexpr float C1 = -2.3022081986f;
        constexpr float C2 = C1 * 0.044715f;
#pragma unroll
        for (int c = 0; c < 3; ++c) {
            f32x2 y[8];
#pragma unroll
            for (int j = 0; j < 8; ++j) {
                const int b = j >> 1, r = 2 * (j & 1);
                const f32x2 a = {tb[j][0], tb[j][1]}, bb = {tb[j][2], tb[j][3]};
                const f32x2 z = pfma(a, f32x2{acc[b][2 + c][r], acc[b][2 + c][r + 1]}, pfma(bb, splat(s1c[c]), splat(b1c[c])));
                const f32x2 u = z * pfma(z * z, splat(C2), splat(C1));
                const f32x2 d = f32x2{__builtin_amdgcn_exp2f(u[0]), __builtin_amdgcn_exp2f(u[1])} + splat(1.0f);
                y[j] = z * f32x2{__builtin_amdgcn_rcpf(d[0]), __builtin_amdgcn_rcpf(d[1])};
            }
            if (3 * wn + c < nfu) {
                const int k = f0 + 16 * (3 * wn + c) + li;
                GAS char* dst = act + (k >> 3) * 1024 + (k & 7) * 64 + 16 * (g ^ ((k >> 2) & 3));
#pragma unroll
                for (int sgm = 0; sgm < 2; ++sgm)
                    *(GAS u32x4*)(dst + 512 * sgm) = u32x4{pk_bf16(y[0][sgm], y[1][sgm]), pk_bf16(y[2][sgm], y[3][sgm]), pk_bf16(y[4][sgm], y[5][sgm]), pk_bf16(y[6][sgm], y[7][sgm])};
            }
        }
    }
    STAMP(1, 4);

    cf Hin = cf{0.0f, 0.0f};
    if (ci > 0) {
        u32x4 gr[15];
        for (unsigned spins = 0; spins < (1u << 22); ++spins) {
#pragma unroll
            for (int j = 0; j < 15; ++j) {
                const int jj = j < ci ? j : ci - 1;
                asm volatile("global_load_dwordx4 %0, %1, off sc1" : "=v"(gr[j]) : "v"(eg + jj * NP + p) : "memory");
            }
            asm volatile("s_waitcnt vmcnt(0)"
                         : "+v"(gr[0]), "+v"(gr[1]), "+v"(gr[2]), "+v"(gr[3]), "+v"(gr[4]), "+v"(gr[5]), "+v"(gr[6]), "+v"(gr[7]), "+v"(gr[8]),
                           "+v"(gr[9]), "+v"(gr[10]), "+v"(gr[11]), "+v"(gr[12]), "+v"(gr[13]), "+v"(gr[14])::"memory");
            bool ok = true;
#pragma unroll
            for (int j = 0; j < 15; ++j) ok &= (gr[j][1] == 1u) & (gr[j][3] == 1u);
            if (__all(ok)) break;
            __builtin_amdgcn_s_sleep(2);
        }
#pragma unroll
        for (int j = 0; j < 15; ++j)
            if (j < ci) Hin = cfma(l128, Hin, cf{__uint_as_float(gr[j][0]), __uint_as_float(gr[j][2])});
    }
    STAMP(1, 5);
    {
        const cf cA = cfma(lsig, Hin, Lp);
        const cf cB = cfma(l8, cA, eA);
        f32x2 sr = {cA.r, cB.r}, si = {cA.i, cB.i};
        f32x2 ore[8], oim[8];
#pragma unroll
        for (int j = 0; j < 8; ++j) {
            const f32x2 tr = pfma(splat(lam.r), sr, ure[j]), ti = pfma(splat(lam.r), si, uim[j]);
            const f32x2 nr = pfma(splat(-lam.i), si, tr), ni = pfma(splat(lam.i), sr, ti);
            sr = nr; si = ni;
            ore[j] = pfma(splat(coef2.r), sr, splat(-coef2.i) * si);
            oim[j] = pfma(splat(-coef2.r), si, splat(-coef2.i) * sr);
        }
        const int kr = FF + p, kim = FF + NP + p;
        GAS char* dr = act + (kr >> 3) * 1024 + (kr & 7) * 64 + 16 * (g ^ ((kr >> 2) & 3));
        GAS char* di = act + (kim >> 3) * 1024 + (kim & 7) * 64 + 16 * (g ^ ((kim >> 2) & 3));
#pragma unroll
        for (int sgm = 0; sgm < 2; ++sgm) {
            *(GAS u32x4*)(dr + 512 * sgm) = u32x4{pk_bf16(ore[0][sgm], ore[1][sgm]), pk_bf16(ore[2][sgm], ore[3][sgm]), pk_bf16(ore[4][sgm], ore[5][sgm]), pk_bf16(ore[6][sgm], ore[7][sgm])};
            *(GAS u32x4*)(di + 512 * sgm) = u32x4{pk_bf16(oim[0][sgm], oim[1][sgm]), pk_bf16(oim[2][sgm], oim[3][sgm]), pk_bf16(oim[4][sgm], oim[5][sgm]), pk_bf16(oim[6][sgm], oim[7][sgm])};
        }
    }
    STAMP(1, 6);
}

constexpr int P2_SLOT = 16384, P2_SLOTS = 8, P2_NST = 38;
constexpr int P2_LDS = P2_SLOT * P2_SLOTS;

__global__ __launch_bounds__(512) void k_p2(const unsigned short* __restrict__ x, const unsigned short* __restrict__ cre,
                                            const unsigned short* __restrict__ cim, const unsigned short* __restrict__ dskip,
                                            const unsigned short* __restrict__ w2, const unsigned short* __restrict__ b2,
                                            char* __restrict__ ws, float* __restrict__ out) {
    extern __shared__ __attribute__((aligned(16))) char smem_[];
    LAS char* smem = (LAS char*)smem_;
    const int wave = __builtin_amdgcn_readfirstlane(threadIdx.x >> 6), lane = threadIdx.x & 63;
    const int loc = blockIdx.x >> 3, T = 4 * (blockIdx.x & 7) + (loc >> 3), d0 = 64 * (loc & 7);

    if (wave >= 4) {
        const int lw = wave - 4;
        STAMP(2, 0 + 8);
        const GAS char* ag = (const GAS char*)(ws + WS_ACT) + (size_t)T * ACT_TILE + lw * 2048 + lane * 16;
        unsigned wo[2], co[2];
#pragma unroll
        for (int i = 0; i < 2; ++i) {
            const int kg = 2 * lw + i, kr = (lane & 31) >> 2;
            const int ch = 4 * (lane >> 5) + ((lane & 3) ^ ((2 * kg + (kr >> 2)) & 3));
            wo[i] = (8 * kg + kr) * (DM * 2) + (d0 + 8 * ch) * 2;
            const int R = 8 * kg + (lane >> 3);
            co[i] = (d0 + R) * (NP * 2) + 16 * ((lane & 7) ^ (lane >> 3));
        }
        auto issue = [&](int s) {
            LAS char* sl = smem + (s & 7) * P2_SLOT;
            if (s < 22) {
                const int k0 = s < 21 ? 64 * s : FF - 64;
#pragma unroll
                for (int i = 0; i < 2; ++i) dma16(ag + k0 * 128 + i * 1024, sl + (2 * lw + i) * 1024);
#pragma unroll
                for (int i = 0; i < 2; ++i) dma16((const GAS char*)w2 + k0 * (DM * 2) + wo[i], sl + 8192 + (2 * lw + i) * 1024);
            } else {
                const int kk = 64 * (s - 22);
#pragma unroll
                for (int i = 0; i < 2; ++i) dma16(ag + (FF + kk) * 128 + i * 1024, sl + (2 * lw + i) * 1024);
                const GAS char* cg = (const GAS char*)(kk < NP ? cre : cim) + (kk & (NP - 1)) * 2;
#pragma unroll
                for (int i = 0; i < 2; ++i) dma16(cg + co[i], sl + 8192 + (2 * lw + i) * 1024);
            }
        };
        for (int s = 0; s < 6; ++s) issue(s);
        for (int s = 0; s < P2_NST - 5; ++s) {
            wait_vm<20>(); bar();
            if (s == 0) STAMP(2, 1 + 8);
            if (s + 6 < P2_NST) issue(s + 6);
        }
        wait_vm<16>(); bar();
        wait_vm<12>(); bar();
        wait_vm<8>(); bar();
        wait_vm<4>(); bar();
        wait_vm<0>(); bar();
        STAMP(2, 2 + 8);
        return;
    }

    const int wd = wave >> 1, wt = wave & 1, g = lane >> 4, li = lane & 15;
    STAMP(2, 0);
    const int q = li >> 2, pp = lane & 3;
    int tr[2][2];
#pragma unroll
    for (int t = 0; t < 2; ++t)
#pragma unroll
        for (int par = 0; par < 2; ++par) tr[t][par] = 1024 * g + 256 * t + 64 * q + 32 * (par ^ (g & 1)) + 16 * ((pp >> 1) ^ t) + 8 * (pp & 1);
    const int ca0 = 8192 + (32 * wd + li) * 128 + 16 * (g ^ (lane & 7));
    f32x4 acc[2][2];
#pragma unroll
    for (int i = 0; i < 2; ++i)
#pragma unroll
        for (int j = 0; j < 2; ++j) acc[i][j] = f32x4{0, 0, 0, 0};
    s16x8 wf[2][2][2], af[2][2][2];
    auto rd_act = [&](LAS char* sl, int buf) {
#pragma unroll
        for (int ks = 0; ks < 2; ++ks)
#pragma unroll
            for (int i = 0; i < 2; ++i) af[buf][ks][i] = lds_tr(sl + 512 * wt + 4096 * ks + tr[0][i], sl + 512 * wt + 4096 * ks + tr[1][i]);
    };
    auto rd4 = [&](int s, int buf) {
        LAS char* sl = smem + (s & 7) * P2_SLOT;
        rd_act(sl, buf);
#pragma unroll
        for (int ks = 0; ks < 2; ++ks)
#pragma unroll
            for (int i = 0; i < 2; ++i) wf[buf][ks][i] = lds_tr(sl + 8192 + 512 * wd + 4096 * ks + tr[0][i], sl + 8192 + 512 * wd + 4096 * ks + tr[1][i]);
    };
    auto rd3 = [&](int s, int buf) {
        LAS char* sl = smem + (s & 7) * P2_SLOT;
        rd_act(sl, buf);
#pragma unroll
        for (int ks = 0; ks < 2; ++ks)
#pragma unroll
            for (int i = 0; i < 2; ++i) wf[buf][ks][i] = lds_row(sl + ((ca0 ^ (64 * ks)) + 2048 * i));
    };
    auto mmk = [&](int buf, int ks) {
#pragma unroll
        for (int i = 0; i < 2; ++i)
#pragma unroll
            for (int j = 0; j < 2; ++j) acc[i][j] = mfma(wf[buf][ks][i], af[buf][ks][j], acc[i][j]);
    };
    auto mm = [&](int buf) { mmk(buf, 0); mmk(buf, 1); };
#define SB0 __builtin_amdgcn_sched_barrier(0)
    bar(); rd4(0, 0); SB0;
    STAMP(2, 1);
#pragma unroll 1
    for (int s = 1; s < 21; s += 2) {
        bar(); rd4(s, 1); mm(0); interleave<8, 16, 3>(); SB0;
        bar(); rd4(s + 1, 0); mm(1); interleave<8, 16, 3>(); SB0;
    }
    bar(); rd4(21, 1); mm(0); interleave<8, 16, 3>(); SB0;
    bar(); rd3(22, 0); mmk(1, 1); interleave<4, 12, 3>(); SB0;
#pragma unroll 1
    for (int s = 23; s < P2_NST - 1; s += 2) {
        bar(); rd3(s, 1); mm(0); interleave<8, 12, 2>(); SB0;
        bar(); rd3(s + 1, 0); mm(1); interleave<8, 12, 2>(); SB0;
    }
    bar(); rd3(P2_NST - 1, 1); mm(0); interleave<8, 12, 2>(); SB0;
    mm(1);
    STAMP(2, 2);

#pragma unroll
    for (int j = 0; j < 2; ++j) {
        const int tok = 64 * T + 32 * j + 16 * ((lane >> 3) & 1) + 8 * wt + (lane & 7);
        const float2 st = ((const float2*)(ws + WS_ROWSTAT))[tok];
        const RowScale rs = row_scale(st.x, st.y);
#pragma unroll
        for (int i = 0; i < 2; ++i) {
            const int d = d0 + 32 * wd + 16 * i + 4 * g;
            const u32x2 xv = *(const u32x2*)(x + tok * DM + d), dv = *(const u32x2*)(dskip + d), bv = *(const u32x2*)(b2 + d);
            const float xe[4] = {bf_lo(xv[0]), bf_hi(xv[0]), bf_lo(xv[1]), bf_hi(xv[1])};
            const float de[4] = {bf_lo(dv[0]), bf_hi(dv[0]), bf_lo(dv[1]), bf_hi(dv[1])};
            const float be[4] = {bf_lo(bv[0]), bf_hi(bv[0]), bf_lo(bv[1]), bf_hi(bv[1])};
            f32x4 o;
#pragma unroll
            for (int r = 0; r < 4; ++r) {
                const float xn = rbf(rbf(xe[r] - rs.mb) * rs.r1);
                o[r] = xe[r] + rbf(xn * de[r]) + be[r] + acc[i][j][r];
            }
            *(f32x4*)(out + tok * DM + d) = o;
        }
    }
    STAMP(2, 3);
    { const unsigned long long e0 = *(volatile unsigned long long*)(ws + (120u << 20)); while (__builtin_amdgcn_s_memrealtime() - e0 < 6000) {} }
}

}

extern "C" void kernel_launch(void* const* d_in, const int* in_sizes, int n_in, void* d_out, int out_size, void* d_ws, size_t ws_size,
                              hipStream_t stream) {
    typedef const unsigned short* P;
    P x = (P)d_in[0], lre = (P)d_in[1], lim = (P)d_in[2], bre = (P)d_in[3], bim = (P)d_in[4], cre = (P)d_in[5], cim = (P)d_in[6],
      ldt = (P)d_in[7], dsk = (P)d_in[8], w1 = (P)d_in[10], b1 = (P)d_in[11], w2 = (P)d_in[12], b2 = (P)d_in[13];
    char* ws = (char*)d_ws;
    (void)hipFuncSetAttribute((const void*)k_p1, hipFuncAttributeMaxDynamicSharedMemorySize, P1_LDS);
    (void)hipFuncSetAttribute((const void*)k_p2, hipFuncAttributeMaxDynamicSharedMemorySize, P2_LDS);
    k_stats<<<256, 256, 0, stream>>>(x, bre, bim, w1, ws);
    k_p1<<<256, 512, P1_LDS, stream>>>(x, lre, lim, bre, bim, ldt, w1, b1, ws);
    k_p2<<<256, 512, P2_LDS, stream>>>(x, cre, cim, dsk, w2, b2, ws, (float*)d_out);
}
```

```cpp
#include <hip/hip_runtime.h>
#include <hip/hip_bf16.h>
#include <stdint.h>
#include <type_traits>

#define LAS __attribute__((address_space(3)))
#define GAS __attribute__((address_space(1)))

typedef __attribute__((ext_vector_type(8))) short s16x8;
typedef __attribute__((ext_vector_type(4))) short s16x4;
typedef __attribute__((ext_vector_type(8))) __bf16 bf16x8;
typedef __attribute__((ext_vector_type(2))) __bf16 bf16x2;
typedef __attribute__((ext_vector_type(4))) float f32x4;
typedef __attribute__((ext_vector_type(2))) float f32x2;
typedef __attribute__((ext_vector_type(4))) unsigned u32x4;
typedef __attribute__((ext_vector_type(2))) unsigned u32x2;

#define ACT_AUX 0
#define ST_AUX 16
#define PREFETCH 1
#define WARM1 0
#define PRIO 0
#define WARMA 1
#define D4 4
#define D3 3
#define PREF2 0
#define DEFER 0
#define LPRIO2 0
#define CPRIO2 0
#define CODEW 2
#define CODE_LINES 200
#define ACQ 0

namespace {

constexpr int SEQ = 2048, DM = 512, NP = 512, FF = 1376;
constexpr int KACT = FF + 2 * NP;
constexpr int ACT_TILE = KACT * 128;
constexpr unsigned WS_ACT = 0;
constexpr unsigned WS_RS = 10u << 20;
constexpr unsigned WS_EG = WS_RS + (64u << 10);
constexpr unsigned WS_FH = WS_EG + (256u << 10);
constexpr unsigned WS_FS = WS_FH + (8u << 10);
constexpr unsigned WS_CS = WS_FS + (8u << 10);
constexpr unsigned WS_EP = WS_CS + (256u << 10);
constexpr size_t WS_STAMP = 200u << 20;
constexpr float LN_EPS = 1e-5f;
constexpr unsigned SPIN_CAP = 1u << 18;

constexpr int P1_SLOT = 36 * 1024, P1_X = 0, P1_B = 16384, P1_W = 24576;
constexpr int P2_IMG = 4 * 2080, P2_SLOT = 2 * P2_IMG, P2_RING = 9 * P2_SLOT, P2_NST = 38;
constexpr int L_TAB1 = P2_RING;
constexpr int L_TAB2 = L_TAB1 + 1024;
constexpr int L_EL = L_TAB2 + 1024;
constexpr int L_CS = L_EL + 2048;
constexpr int L_TOK = L_CS + 1280;
constexpr int L_DUMMY = L_TOK + 1024;
constexpr int L_MAIL = L_DUMMY + 1024;
constexpr int LDS_BYTES = L_MAIL + 64 + 704;

#define STAMP(s) do {} while (0)

#define SB0 __builtin_amdgcn_sched_barrier(0)
template <int N> __device__ __forceinline__ void wait_vm() { asm volatile("s_waitcnt vmcnt(%0)" ::"i"(N) : "memory"); }
__device__ __forceinline__ void wait_lgkm0() { __builtin_amdgcn_s_waitcnt(0xC07F); asm volatile("" ::: "memory"); }
template <int NM, int ND, int DPM = 1, int I = 0> __device__ __forceinline__ void interleave() {
    if constexpr (I * DPM < ND && I < NM) {
        __builtin_amdgcn_sched_group_barrier(0x008, 1, 0);
        __builtin_amdgcn_sched_group_barrier(0x100, (ND - I * DPM) < DPM ? (ND - I * DPM) : DPM, 0);
        interleave<NM, ND, DPM, I + 1>();
    } else if constexpr (I < NM) {
        __builtin_amdgcn_sched_group_barrier(0x008, NM - I, 0);
    }
}
__device__ __forceinline__ void bar() {
    SB0;
    asm volatile("" ::: "memory");
    __builtin_amdgcn_s_barrier();
    asm volatile("" ::: "memory");
    SB0;
}
__device__ __forceinline__ float bf_lo(unsigned u) { return __uint_as_float(u << 16); }
__device__ __forceinline__ float bf_hi(unsigned u) { return __uint_as_float(u & 0xffff0000u); }
__device__ __forceinline__ float bf2f(unsigned short h) { return __uint_as_float((unsigned)h << 16); }
__device__ __forceinline__ unsigned pk_bf16(float a, float b) { bf16x2 v = {(__bf16)a, (__bf16)b}; return __builtin_bit_cast(unsigned, v); }
__device__ __forceinline__ float rbf(float a) { return bf_lo(pk_bf16(a, a)); }
__device__ __forceinline__ f32x2 pfma(f32x2 a, f32x2 b, f32x2 c) { return __builtin_elementwise_fma(a, b, c); }
__device__ __forceinline__ f32x2 splat(float a) { return f32x2{a, a}; }
__device__ __forceinline__ f32x4 mfma(s16x8 a, s16x8 b, f32x4 c) {
    return __builtin_amdgcn_mfma_f32_16x16x32_bf16(__builtin_bit_cast(bf16x8, a), __builtin_bit_cast(bf16x8, b), c, 0, 0, 0);
}
__device__ __forceinline__ void mfma_acc(f32x4& c, s16x8 a, s16x8 b) {
    asm volatile("v_mfma_f32_16x16x32_bf16 %0, %1, %2, %0" : "+v"(c) : "v"(a), "v"(b));
}
__device__ __forceinline__ s16x4 lds_tr1(LAS char* p) { return __builtin_amdgcn_ds_read_tr16_b64_v4i16((LAS s16x4*)p); }
__device__ __forceinline__ s16x8 cat(s16x4 a, s16x4 b) { return __builtin_shufflevector(a, b, 0, 1, 2, 3, 4, 5, 6, 7); }
__device__ __forceinline__ s16x8 lds_row(LAS char* p) { return *(LAS s16x8*)p; }
__device__ __forceinline__ s16x8 lds_tr(LAS char* p0, LAS char* p1) {
    s16x4 a = __builtin_amdgcn_ds_read_tr16_b64_v4i16((LAS s16x4*)p0);
    s16x4 b = __builtin_amdgcn_ds_read_tr16_b64_v4i16((LAS s16x4*)p1);
    return __builtin_shufflevector(a, b, 0, 1, 2, 3, 4, 5, 6, 7);
}
template <int OFF> __device__ __forceinline__ void dma1(unsigned lds, unsigned voff, const GAS char* base) {
    asm volatile("s_mov_b32 m0, %0\n\ts_nop 0\n\tglobal_load_lds_dwordx4 %1, %2 offset:%3" ::"s"(lds), "v"(voff), "s"(base), "i"(OFF) : "memory");
}
template <int OFF> __device__ __forceinline__ void dma2(unsigned lds, unsigned voff0, unsigned voff1, const GAS char* base) {
    asm volatile("s_mov_b32 m0, %0\n\ts_nop 0\n\tglobal_load_lds_dwordx4 %1, %3\n\tglobal_load_lds_dwordx4 %2, %3 offset:%4"
                 ::"s"(lds), "v"(voff0), "v"(voff1), "s"(base), "i"(OFF) : "memory");
}
__device__ __forceinline__ void touch(unsigned lds, const GAS char* a) {
    asm volatile("s_mov_b32 m0, %0\n\ts_nop 0\n\tglobal_load_lds_dword %1, off" ::"s"(lds), "v"(a) : "memory");
}
__device__ __forceinline__ void lds_st32(unsigned a, float v) { asm volatile("ds_write_b32 %0, %1" ::"v"(a), "v"(v) : "memory"); }
__device__ __forceinline__ void lds_st64(unsigned a, f32x2 v) { asm volatile("ds_write_b64 %0, %1" ::"v"(a), "v"(v) : "memory"); }
template <int CTRL, int RM> __device__ __forceinline__ float dpp_add(float v) {
    return v + __uint_as_float(__builtin_amdgcn_update_dpp(0u, __float_as_uint(v), CTRL, RM, 0xf, true));
}
__device__ __forceinline__ float wave_sum(float v) {
    v = dpp_add<0x111, 0xf>(v); v = dpp_add<0x112, 0xf>(v); v = dpp_add<0x114, 0xf>(v); v = dpp_add<0x118, 0xf>(v);
    v = dpp_add<0x142, 0xa>(v); v = dpp_add<0x143, 0xc>(v);
    return __uint_as_float(__builtin_amdgcn_readlane(__float_as_uint(v), 63));
}
__device__ __forceinline__ float sum_groups(float v) {
    const auto a = __builtin_amdgcn_permlane16_swap(__float_as_uint(v), __float_as_uint(v), false, false);
    v = __uint_as_float(a[0]) + __uint_as_float(a[1]);
    const auto b = __builtin_amdgcn_permlane32_swap(__float_as_uint(v), __float_as_uint(v), false, false);
    return __uint_as_float(b[0]) + __uint_as_float(b[1]);
}
__device__ __forceinline__ u32x4 load16_sc1(const GAS void* p) {
    u32x4 v;
    asm volatile("global_load_dwordx4 %0, %1, off sc1\n\ts_waitcnt vmcnt(0)" : "=&v"(v) : "v"(p) : "memory");
    return v;
}
__device__ __forceinline__ void store16_sc1(GAS void* p, u32x4 v) {
    asm volatile("global_store_dwordx4 %0, %1, off sc1\n\ts_nop 1" ::"v"(p), "v"(v) : "memory");
}
__device__ __forceinline__ void wait_flags(const GAS unsigned* f, unsigned tag) {
#pragma unroll 1
    for (unsigned spins = 0; spins < SPIN_CAP; ++spins) {
        const unsigned v = __hip_atomic_load(f + (threadIdx.x & 31), __ATOMIC_RELAXED, __HIP_MEMORY_SCOPE_AGENT);
        if (__all(v == tag)) break;
        __builtin_amdgcn_s_sleep(1);
    }
    asm volatile("" ::: "memory");
}
struct cf { float r, i; };
__device__ __forceinline__ cf cmul(cf a, cf b) { return cf{a.r * b.r - a.i * b.i, a.r * b.i + a.i * b.r}; }
__device__ __forceinline__ cf cfma(cf a, cf b, cf c) { return cf{fmaf(a.r, b.r, fmaf(-a.i, b.i, c.r)), fmaf(a.r, b.i, fmaf(a.i, b.r, c.i))}; }
__device__ __forceinline__ float exp_f(float x) {
    const float hi = x * 1.44269504f, lo = fmaf(x, 1.44269504f, -hi) + x * 1.92596299e-8f;
    return __builtin_amdgcn_exp2f(hi) * fmaf(lo, 0.693147181f, 1.0f);
}
__device__ __forceinline__ void sincos_f(float x, float& s, float& c) {
    const float k = rintf(x * 0.636619772f);
    float r = fmaf(k, -1.57079601e+00f, x);
    r = fmaf(k, -3.13916473e-07f, r);
    r = fmaf(k, -5.39030253e-15f, r);
    const float z = r * r;
    const float sp = fmaf(r * z, fmaf(z, fmaf(z, fmaf(z, 2.75573137e-06f, -1.98412698e-04f), 8.33333333e-03f), -1.66666667e-01f), r);
    const float cp = fmaf(z, fmaf(z, fmaf(z, fmaf(z, fmaf(z, -2.75573144e-07f, 2.48015873e-05f), -1.38888889e-03f), 4.16666667e-02f), -0.5f), 1.0f);
    const int q = (int)k;
    const float a = (q & 1) ? cp : sp, b = (q & 1) ? sp : cp;
    s = (q & 2) ? -a : a;
    c = ((q + 1) & 2) ? -b : b;
}
__device__ __forceinline__ unsigned row_std(float v) {
    const float s1 = rbf(__builtin_amdgcn_sqrtf(rbf(rbf(v) + LN_EPS)));
    const float r1 = __builtin_amdgcn_rcpf(s1);
    const float s2 = __builtin_amdgcn_sqrtf(rbf(rbf(v * r1 * r1) + LN_EPS));
    return pk_bf16(s1, s2);
}

typedef const unsigned short* __restrict__ bfp;

__global__ __launch_bounds__(512) void k_fused(bfp x, bfp lam_re, bfp lam_im, bfp bre, bfp bim, bfp cre, bfp cim, bfp log_dt, bfp dskip,
                                               bfp w1, bfp b1, bfp w2, bfp b2, char* ws_, float* __restrict__ out) {
    extern __shared__ __attribute__((aligned(16))) char smem_[];
    LAS char* smem = (LAS char*)smem_;
    GAS char* ws = (GAS char*)ws_;
    const int wave = __builtin_amdgcn_readfirstlane(threadIdx.x >> 6), lane = threadIdx.x & 63;
    const int g = lane >> 4, li = lane & 15;
    const int xg8 = blockIdx.x & 7, loc = blockIdx.x >> 3, ci = 4 * (xg8 >> 1) + (loc >> 3), cj = 8 * (xg8 & 1) + (loc & 7);
    const int t0 = 128 * ci, p0 = 32 * cj, f0 = cj < 11 ? 96 * cj : 1056 + 64 * (cj - 11), nfu = cj < 11 ? 6 : 4;
    const int T = 2 * ci + (cj >> 3), d0 = 64 * (cj & 7);
    const int rot = ci >> 1;
    STAMP(0);
    unsigned long long entry_pc;
    asm volatile("s_getpc_b64 %0" : "=s"(entry_pc));
    unsigned epoch;
    asm volatile("global_load_dword %0, %1, off sc1" : "=v"(epoch) : "v"(ws + WS_EP) : "memory");
    const int q = li >> 2, pp = lane & 3;

    if (__builtin_expect(wave >= 4, 1)) {
        const int lw = wave - 4;
        __builtin_amdgcn_s_setprio(PRIO);
        const unsigned lds0 = (unsigned)(size_t)smem;
        unsigned xo[4], bo[2], wo[3];
#pragma unroll
        for (int i = 0; i < 4; ++i) {
            const int R = 8 * (4 * lw + i) + (lane >> 3), ii = R & 15, bb = (R >> 4) & 3;
            const int tok = t0 + (R & 64) + 16 * (ii >> 2) + 8 * (ii & 1) + 2 * bb + ((ii >> 1) & 1);
            xo[i] = tok * 1024 + 16 * ((lane & 7) ^ (lane >> 3)) + 3072 - 1024 * i;
        }
#pragma unroll
        for (int i = 0; i < 2; ++i) {
            const int R = 8 * (2 * lw + i) + (lane >> 3);
            bo[i] = (p0 + (R & 31)) * 1024 + 16 * ((lane & 7) ^ (lane >> 3)) + 3072 - 1024 * i;
        }
#pragma unroll
        for (int i = 0; i < 3; ++i) {
            const int st = 2 * (3 * lw + i) + (lane >> 5), kg = st / 3, ns = st - 3 * kg, kr = (lane & 31) >> 2;
            const int ch = 4 * ns + ((lane & 3) ^ ((2 * kg + (kr >> 2)) & 3));
            const int f = min(f0 + 8 * ch, FF - 8);
            wo[i] = (8 * kg + kr) * (FF * 2) + f * 2 + 3072 - 1024 * i;
        }
        const GAS char* xg = (const GAS char*)x - 3072;
        const GAS char* bg = (const GAS char*)(lw < 2 ? bre : bim) - 3072;
        const GAS char* wg = (const GAS char*)w1 - 3072;
        const unsigned lx = lds0 + P1_X + lw * 4096, lb = lds0 + P1_B + lw * 2048, lwb = lds0 + P1_W + lw * 3072;
        auto piece = [&](int t, auto I) {
            constexpr int i = decltype(I)::value;
            const unsigned so = (t & 3) * P1_SLOT;
            const int kt = (t + rot) & 7;
            if constexpr (i < 4) dma1<1024 * i>(lx + so, xo[i], xg + kt * 128);
            else if constexpr (i < 6) dma1<1024 * (i - 4)>(lb + so, bo[i - 4], bg + kt * 128);
            else dma1<1024 * (i - 6)>(lwb + so, wo[i - 6], wg + (size_t)kt * (64 * FF * 2));
        };
#define PC(t, i) piece(t, std::integral_constant<int, i>{})
        auto issue = [&](int t) { PC(t, 0); PC(t, 1); PC(t, 2); PC(t, 3); PC(t, 4); PC(t, 5); PC(t, 6); PC(t, 7); PC(t, 8); };
        const GAS char* tbase;
        unsigned tstep;
        if (lw == 0) { tbase = (const GAS char*)x + (t0 + 16 * (cj & 7) + li) * 1024; tstep = 128; }
        else if (lw == 1) { const int r = 16 * (ci & 3) + li; tbase = (const GAS char*)(r < 32 ? bre : bim) + (p0 + (r & 31)) * 1024; tstep = 128; }
        else { tbase = (const GAS char*)w1 + (16 * (ci & 3) + li) * (FF * 2) + min(f0 * 2 + (lw == 2 ? 0 : 128), FF * 2 - 4); tstep = 64 * FF * 2; }
        auto warm = [&](int t) { if (WARM1) touch(lds0 + L_DUMMY + 256 * lw, tbase + ((t + rot) & 7) * tstep); };
        STAMP(6);
        if (CODEW == 1) {
            touch(lds0 + L_DUMMY + 256 * lw, (const GAS char*)(entry_pc & ~255ull) + min(((loc & 3) * 4 + lw) * 13 + min(lane, 12), CODE_LINES - 1) * 128);
        }
        issue(0); issue(1);
        warm(2 + (g & 1));
        if (CODEW == 2) {
            touch(lds0 + L_DUMMY + 256 * lw, (const GAS char*)(entry_pc & ~255ull) + min(((loc & 3) * 4 + lw) * 13 + min(lane, 12), CODE_LINES - 1) * 128);
        }
        STAMP(7);

        unsigned w2o[2], co[2], ao[2];
#pragma unroll
        for (int i = 0; i < 2; ++i) {
            const int kg = 2 * lw + i;
            w2o[i] = (8 * kg + ((lane & 31) >> 2)) * (DM * 2) + (d0 + 32 * (lane >> 5) + 8 * (lane & 3)) * 2 + 3072 - 1056 * i;
            co[i] = (d0 + 8 * kg + (lane >> 3)) * (NP * 2) + 16 * ((lane & 7) ^ (lane >> 3)) + 3072 - 1024 * i;
            ao[i] = kg * 1024 + lane * 16 + 3072 - 1056 * i;
        }
        const GAS char* ag = ws + WS_ACT + (size_t)T * ACT_TILE;
        const GAS char* w2g = (const GAS char*)w2 - 3072;
        const unsigned l2 = lds0 + lw * 2080, l2c = lds0 + P2_IMG + lw * 2048;
        auto dmaA = [&](const GAS char* src, unsigned slot) { dma2<1056>(l2 + slot, ao[0], ao[1], src - 3072); };
        auto dmaW = [&](const GAS char* src, unsigned slot) { dma2<1056>(l2 + slot + P2_IMG, w2o[0], w2o[1], src); };
        auto dmaC = [&](const GAS char* src, unsigned slot) { dma2<1024>(l2c + slot, co[0], co[1], src); };

        const GAS char* l2a[2];
#pragma unroll
        for (int i = 0; i < 2; ++i) {
            const int n = 456 * (ci & 3) + 114 * lw + min(64 * i + lane, 113);
            const int m = n - 800;
            l2a[i] = n < 800 ? (const GAS char*)w2 + (576 + n) * 1024 + d0 * 2
                             : (const GAS char*)(m < 512 ? cre : cim) + (d0 + ((m >> 3) & 63)) * 1024 + (m & 7) * 128;
        }
        const int n1 = 75 * blockIdx.x + 19 * lw + min(lane, 18);
        const GAS char* l1a = n1 < 11008 ? (const GAS char*)w2 + n1 * 128
                                         : (n1 < 15104 ? (const GAS char*)cre + (n1 - 11008) * 128 : (const GAS char*)cim + (min(n1, 19199) - 15104) * 128);
        const GAS u32x4* rsp = (const GAS u32x4*)(ws + WS_RS) + t0 + 64 * (lw & 1) + lane;
        const GAS u32x4* csp = (const GAS u32x4*)(ws + WS_CS) + ((cj * 16 + 8 * (lw >> 1) + 2 * g) * 2 + (lw & 1)) * 32 + li;
        asm volatile("" : "+v"(l2a[0]), "+v"(l2a[1]), "+v"(l1a), "+v"(rsp), "+v"(csp));
        asm volatile("s_waitcnt vmcnt(%1)" : "+v"(epoch) : "i"(9 + WARM1 + (CODEW == 2)) : "memory");
        unsigned tag = __builtin_amdgcn_readfirstlane(epoch) + 1u;
        asm volatile("" : "+s"(tag));
        bar();
        STAMP(1);
        issue(2); warm(4);
        if (PREFETCH) touch(lds0 + L_DUMMY + 256 * lw, l1a);
        wait_vm<9 + 2 * WARM1 + PREFETCH>(); bar();
        issue(3); warm(5);
        {
            f32x2 v;
            asm volatile("ds_read_b64 %0, %1\n\ts_waitcnt lgkmcnt(0)" : "=&v"(v) : "v"(lds0 + L_MAIL + 16 * lw + 8 * (lane & 1)) : "memory");
            if (lane < 2) store16_sc1((GAS u32x4*)(ws + WS_RS) + t0 + 8 * cj + 2 * lw + lane, u32x4{__float_as_uint(v[0]), tag, __float_as_uint(v[1]), tag});
        }
        wait_vm<10 + 2 * WARM1 + PREFETCH>(); bar();
        issue(4); warm(6);
        {
            float a, b, c;
            const unsigned ma = lds0 + L_MAIL + 64 + 4 * (((lw >> 1) * 5 + 3 * (lw & 1)) * 16 + li);
            asm volatile("ds_read_b32 %0, %3\n\tds_read_b32 %1, %3 offset:64\n\tds_read_b32 %2, %3 offset:128\n\ts_waitcnt lgkmcnt(0)"
                         : "=&v"(a), "=&v"(b), "=&v"(c) : "v"(ma) : "memory");
            if (g == 0) store16_sc1((GAS u32x4*)(ws + WS_CS) + ((cj * 16 + ci) * 2 + (lw >> 1)) * 32 + 16 * (lw & 1) + li,
                                    u32x4{__float_as_uint(a), __float_as_uint(b), __float_as_uint(c), tag});
        }
        wait_vm<11 + 2 * WARM1>(); bar();
        issue(5); warm(7); wait_vm<10 + 2 * WARM1>(); bar();
        issue(6); wait_vm<9 + WARM1>(); bar();
        issue(7);
        u32x4 rs, cs[4];
        auto gather = [&]() {
            asm volatile("global_load_dwordx4 %0, %5, off sc1\n\t"
                         "global_load_dwordx4 %1, %6, off sc1\n\tglobal_load_dwordx4 %2, %6, off offset:256 sc1\n\t"
                         "global_load_dwordx4 %3, %6, off offset:1024 sc1\n\tglobal_load_dwordx4 %4, %6, off offset:1280 sc1"
                         : "=&v"(rs), "=&v"(cs[0]), "=&v"(cs[1]), "=&v"(cs[2]), "=&v"(cs[3]) : "v"(rsp), "v"(csp) : "memory");
        };
        gather();
        wait_vm<14>(); bar();
        dmaW(w2g, 0); dmaW(w2g + 65536, P2_SLOT); dmaW(w2g + 2 * 65536, 2 * P2_SLOT); dmaW(w2g + 3 * 65536, 3 * P2_SLOT);
        if (PREF2 == 1) { touch(lds0 + L_DUMMY + 256 * lw, l2a[0]); touch(lds0 + L_DUMMY + 256 * lw, l2a[1]); }
        asm volatile("s_waitcnt vmcnt(%5)" : "+v"(rs), "+v"(cs[0]), "+v"(cs[1]), "+v"(cs[2]), "+v"(cs[3]) : "i"(PREF2 == 1 ? 10 : 8) : "memory");
        bar();
        dmaW(w2g + 4 * 65536, 4 * P2_SLOT); dmaW(w2g + 5 * 65536, 5 * P2_SLOT);
        auto fresh = [&]() {
            return __all(rs[1] == tag && rs[3] == tag && cs[0][3] == tag && cs[1][3] == tag && cs[2][3] == tag && cs[3][3] == tag);
        };
#pragma unroll 1
        for (unsigned spins = 0; spins < SPIN_CAP && !fresh(); ++spins) {
            gather();
            asm volatile("s_waitcnt vmcnt(0)" : "+v"(rs), "+v"(cs[0]), "+v"(cs[1]), "+v"(cs[2]), "+v"(cs[3])::"memory");
        }
        if (lw < 2) {
            const float m = __uint_as_float(rs[0]), mb = rbf(m);
            const float r1 = __builtin_amdgcn_rcpf(bf_lo(rs[2])), r12 = r1 * __builtin_amdgcn_rcpf(bf_hi(rs[2]));
            const int tau = 64 * lw + lane;
            const unsigned ta = lds0 + L_TAB1 + 4 * (((tau >> 4) * 8 + (tau & 7)) * 4 + ((tau >> 3) & 1));
            lds_st32(ta, r1); lds_st32(ta + 8, -r1 * mb);
            lds_st32(ta + 1024, r12); lds_st32(ta + 1032, -r12 * m);
            lds_st64(lds0 + L_TOK + 8 * tau, f32x2{mb, r1});
        }
        bar();
        dmaW(w2g + 6 * 65536, 6 * P2_SLOT); dmaW(w2g + 7 * 65536, 7 * P2_SLOT); dmaW(w2g + 8 * 65536, 8 * P2_SLOT);
        {
            float v5[5];
#pragma unroll
            for (int k = 0; k < 3; ++k) v5[k] = __uint_as_float(cs[0][k]) + __uint_as_float(cs[2][k]);
#pragma unroll
            for (int k = 0; k < 2; ++k) v5[3 + k] = __uint_as_float(cs[1][k]) + __uint_as_float(cs[3][k]);
#pragma unroll
            for (int k = 0; k < 5; ++k) v5[k] = sum_groups(v5[k]);
            if (g == 0) {
#pragma unroll
                for (int k = 0; k < 5; ++k) lds_st32(lds0 + L_CS + 4 * ((((lw >> 1) * 2 + (lw & 1)) * 5 + k) * 16 + li), v5[k]);
            }
        }
        asm volatile("s_waitcnt lgkmcnt(0)" ::: "memory");
        bar();
        STAMP(2);
        if (PREF2 == 2) { touch(lds0 + L_DUMMY + 256 * lw, l2a[0]); touch(lds0 + L_DUMMY + 256 * lw, l2a[1]); }
        bar();
        bar();
        if (PREF2 == 3) { touch(lds0 + L_DUMMY + 256 * lw, l2a[0]); touch(lds0 + L_DUMMY + 256 * lw, l2a[1]); }
        __builtin_amdgcn_s_setprio(LPRIO2);
        wait_flags((const GAS unsigned*)(ws + WS_FH) + T * 32, tag);
        STAMP(3);
        dmaA(ag, 0); dmaA(ag + 8192, P2_SLOT);
        if (WARMA == 1)
            touch(lds0 + L_DUMMY + 256 * lw, ag + (128 + 156 * (cj & 7) + 39 * lw + min(lane, 38)) * 128);
        dmaA(ag + 2 * 8192, 2 * P2_SLOT); dmaA(ag + 3 * 8192, 3 * P2_SLOT);
        wait_vm<4 + (WARMA == 1)>(); bar();
        STAMP(4);
        dmaA(ag + 4 * 8192, 4 * P2_SLOT); dmaA(ag + 5 * 8192, 5 * P2_SLOT); dmaA(ag + 6 * 8192, 6 * P2_SLOT); dmaA(ag + 7 * 8192, 7 * P2_SLOT);
        wait_vm<8>(); bar();
        const GAS char* ap = ag + 9 * 8192;
        const GAS char* wp = w2g + 9 * 65536;
        unsigned slot = 0;
        auto adv = [&]() { ap += 8192; slot = slot == P2_RING - P2_SLOT ? 0 : slot + P2_SLOT; };
        auto stW = [&]() { dmaA(ap, slot); dmaW(wp, slot); wp += 65536; adv(); };
        auto stC = [&]() { dmaA(ap, slot); dmaC(wp, slot); wp += 128; adv(); };
        dmaA(ag + 8 * 8192, 8 * P2_SLOT); stW();
        wait_vm<10>(); bar(); stW(); stW();
        wait_vm<14>(); bar(); stW(); stW();
#pragma unroll 1
        for (int j = 4; j < 7; ++j) { wait_vm<16>(); bar(); stW(); stW(); }
        wait_vm<16>(); bar(); stW();
        ap -= 4096; wp -= 32768;
        stW();
        wp = (const GAS char*)cre - 3072;
        wait_vm<16>(); bar();
        if (WARMA) touch(lds0 + L_DUMMY + 256 * lw, ag + (FF + 128 * (cj & 7) + 32 * lw + (lane & 31)) * 128);
        stC(); stC();
#pragma unroll 1
        for (int j = 9; j < 12; ++j) { wait_vm<16>(); bar(); stC(); stC(); }
        wp = (const GAS char*)cim - 3072;
#pragma unroll 1
        for (int j = 12; j < 16; ++j) { wait_vm<16>(); bar(); stC(); stC(); }
        wait_vm<16>(); bar();
        wait_vm<8>(); bar();
        wait_vm<0>(); bar();
        STAMP(5);
        return;
    }

    const int wm = wave >> 1, wn = wave & 1;
    const int p = p0 + 16 * wn + li;
    cf lam, l8, l16, l128, lsig, coef2;
    float b1c[3];
    unsigned tag;
    {
        u32x4 xr[2];
        unsigned short pr[6];
#pragma unroll
        for (int i = 0; i < 2; ++i) xr[i] = *(const u32x4*)(x + (t0 + 8 * cj + 2 * wave + i) * DM + lane * 8);
        pr[0] = lam_re[p]; pr[1] = lam_im[p]; pr[2] = log_dt[p];
#pragma unroll
        for (int c = 0; c < 3; ++c) pr[3 + c] = b1[min(f0 + 16 * (3 * wn + c) + li, FF - 1)];
        SB0;
        const float lr = bf2f(pr[0]), lim = bf2f(pr[1]), dt = rbf(exp_f(bf2f(pr[2])));
#pragma unroll
        for (int c = 0; c < 3; ++c) b1c[c] = bf2f(pr[3 + c]);
        float mean[2], var[2];
#pragma unroll
        for (int i = 0; i < 2; ++i) {
            float e[8], s = 0.0f;
#pragma unroll
            for (int j = 0; j < 4; ++j) { e[2 * j] = bf_lo(xr[i][j]); e[2 * j + 1] = bf_hi(xr[i][j]); s += e[2 * j] + e[2 * j + 1]; }
            mean[i] = wave_sum(s) * (1.0f / DM);
            float qq = 0.0f;
#pragma unroll
            for (int j = 0; j < 8; ++j) qq = fmaf(e[j] - mean[i], e[j] - mean[i], qq);
            var[i] = wave_sum(qq) * (1.0f / DM);
        }
        if (lane < 2) ((LAS f32x2*)(smem + L_MAIL))[2 * wave + lane] = f32x2{lane ? mean[1] : mean[0], __uint_as_float(row_std(lane ? var[1] : var[0]))};
        const float mag = exp_f(dt * lr);
        float sn, cs;
        sincos_f(dt * lim, sn, cs);
        lam = cf{mag * cs, mag * sn};
        const float nr = lam.r - 1.0f, ni = lam.i, den = 1.0f / (lr * lr + lim * lim);
        coef2 = cf{2.0f * (nr * lr + ni * lim) * den, 2.0f * (ni * lr - nr * lim) * den};
        const cf l2 = cmul(lam, lam), l4 = cmul(l2, l2);
        l8 = cmul(l4, l4); l16 = cmul(l8, l8);
        const cf l32 = cmul(l16, l16), l64 = cmul(l32, l32);
        l128 = cmul(l64, l64);
        lsig = (g & 1) ? l16 : cf{1.0f, 0.0f};
        if (g & 2) lsig = cmul(lsig, l32);
        if (wm) lsig = cmul(lsig, l64);
        asm volatile("" : "+v"(lam.r), "+v"(lam.i), "+v"(l8.r), "+v"(l8.i), "+v"(l16.r), "+v"(l16.i), "+v"(l128.r), "+v"(l128.i), "+v"(lsig.r), "+v"(lsig.i),
                          "+v"(coef2.r), "+v"(coef2.i), "+v"(b1c[0]), "+v"(b1c[1]), "+v"(b1c[2]));
    }
    f32x4 acc[4][5];
    {
        const int sw = 16 * (g ^ (lane & 7));
        const int xa0 = P1_X + (64 * wm + li) * 128 + sw;
        const int ba0 = P1_B + (16 * wn + li) * 128 + sw;
        int wa[2][2];
#pragma unroll
        for (int t = 0; t < 2; ++t)
#pragma unroll
            for (int cl = 0; cl < 2; ++cl) {
                const int par = (3 * wn + cl) & 1;
                wa[t][cl] = P1_W + 1536 * g + 64 * q + 256 * t + 32 * (par ^ (g & 1)) + 16 * ((pp >> 1) ^ t) + 8 * (pp & 1) + (cl == 0 ? 512 * wn : 1024 * wn);
            }
#pragma unroll
        for (int b = 0; b < 4; ++b)
#pragma unroll
            for (int c = 0; c < 5; ++c) acc[b][c] = f32x4{0, 0, 0, 0};
        s16x8 xa[2][4], wb[2][5];
        auto rd = [&](int slot, int ks, int buf) {
            LAS char* s = smem + slot * P1_SLOT;
            LAS char* xs = s + (xa0 ^ (64 * ks));
            LAS char* bs = s + (ba0 ^ (64 * ks));
#pragma unroll
            for (int b = 0; b < 4; ++b) xa[buf][b] = lds_row(xs + b * 2048);
            wb[buf][0] = lds_row(bs);
            wb[buf][1] = lds_row(bs + 4096);
            wb[buf][2] = lds_tr(s + wa[0][0] + 6144 * ks, s + wa[1][0] + 6144 * ks);
            wb[buf][3] = lds_tr(s + wa[0][1] + 6144 * ks, s + wa[1][1] + 6144 * ks);
            wb[buf][4] = lds_tr(s + wa[0][0] + 6144 * ks + 512, s + wa[1][0] + 6144 * ks + 512);
        };
        auto mm = [&](int buf) {
#pragma unroll
            for (int c = 0; c < 5; ++c)
#pragma unroll
                for (int b = 0; b < 4; ++b) acc[b][c] = mfma(xa[buf][b], wb[buf][c], acc[b][c]);
        };
        const int colsum_ks = wm == 0 ? (ci & 1) : -1;
        auto colsum = [&](int buf) {
            const s16x8 ones = {0x3f80, 0x3f80, 0x3f80, 0x3f80, 0x3f80, 0x3f80, 0x3f80, 0x3f80};
            float v5[5];
#pragma unroll
            for (int c = 0; c < 5; ++c) v5[c] = mfma(ones, wb[buf][c], f32x4{0, 0, 0, 0})[0];
            if (g == 0) {
#pragma unroll
                for (int c = 0; c < 5; ++c) ((LAS float*)(smem + L_MAIL + 64))[(wn * 5 + c) * 16 + li] = v5[c];
            }
        };
        asm volatile("s_waitcnt vmcnt(0) lgkmcnt(0)" : "+v"(epoch)::"memory");
        tag = __builtin_amdgcn_readfirstlane(epoch) + 1u;
        asm volatile("" : "+s"(tag));
        bar();
        STAMP(1);
        rd(0, 0, 0);
        wait_lgkm0();
#pragma unroll 1
        for (int t = 0; t < 7; ++t) {
            SB0;
            rd(t & 3, 1, 1); mm(0); interleave<20, 12>();
            SB0;
            if (t == 0 && colsum_ks == 0) colsum(0);
            SB0; wait_lgkm0(); bar();
            rd((t + 1) & 3, 0, 0); mm(1); interleave<20, 12>();
            SB0;
            if (t == 0 && colsum_ks == 1) colsum(1);
            SB0; wait_lgkm0();
        }
        rd(3, 1, 1); mm(0); interleave<20, 12>();
        SB0; wait_lgkm0(); bar();
        mm(1);
        bar();
    }
    STAMP(2);

    LAS f32x4* tab1 = (LAS f32x4*)(smem + L_TAB1) + (wm * 4 + g) * 8;
    LAS f32x4* tab2 = (LAS f32x4*)(smem + L_TAB2) + (wm * 4 + g) * 8;
    auto csum = [&](int k) { return ((LAS float*)(smem + L_CS))[(wn * 5 + k) * 16 + li] + ((LAS float*)(smem + L_CS))[((2 + wn) * 5 + k) * 16 + li]; };
    u32x2 xv[2][2], dv[2], bv[2];
#pragma unroll
    for (int i = 0; i < 2; ++i) {
        const int d = d0 + 32 * (wave >> 1) + 16 * i + 4 * g;
        dv[i] = *(const u32x2*)(dskip + d); bv[i] = *(const u32x2*)(b2 + d);
#pragma unroll
        for (int j = 0; j < 2; ++j) xv[i][j] = *(const u32x2*)(x + (64 * T + 32 * j + 16 * ((lane >> 3) & 1) + 8 * (wave & 1) + (lane & 7)) * DM + d);
    }
    const int Tw = 2 * ci + wm;
    const __amdgpu_buffer_rsrc_t act = __builtin_amdgcn_make_buffer_rsrc((void*)(ws_ + WS_ACT), 0, 32 * ACT_TILE, 0x00020000);
    const unsigned act0 = Tw * ACT_TILE;
    {
        f32x4 tb[8];
#pragma unroll
        for (int j = 0; j < 8; ++j) tb[j] = tab2[j];
        constexpr float C1 = -2.30220819f;
        constexpr float C2 = C1 * 0.044715f;
#pragma unroll
        for (int c = 0; c < 3; ++c) {
            const int col = 16 * (3 * wn + c) + li;
            const float s1c = csum(2 + c);
            f32x2 y[8];
#pragma unroll
            for (int j = 0; j < 8; ++j) {
                const int b = j >> 1, r = 2 * (j & 1);
                const f32x2 a = {tb[j][0], tb[j][1]}, bb = {tb[j][2], tb[j][3]};
                const f32x2 z = pfma(a, f32x2{acc[b][2 + c][r], acc[b][2 + c][r + 1]}, pfma(bb, splat(s1c), splat(b1c[c])));
                const f32x2 u = z * pfma(z * z, splat(C2), splat(C1));
                const f32x2 d = f32x2{__builtin_amdgcn_exp2f(u[0]), __builtin_amdgcn_exp2f(u[1])} + splat(1.0f);
                y[j] = z * f32x2{__builtin_amdgcn_rcpf(d[0]), __builtin_amdgcn_rcpf(d[1])};
            }
            if (3 * wn + c < nfu) {
                const int k = f0 + col;
                const unsigned dst = act0 + (k >> 3) * 1024 + (k & 7) * 64 + 16 * g;
#pragma unroll
                for (int sgm = 0; sgm < 2; ++sgm)
                    __builtin_amdgcn_raw_buffer_store_b128(u32x4{pk_bf16(y[0][sgm], y[1][sgm]), pk_bf16(y[2][sgm], y[3][sgm]), pk_bf16(y[4][sgm], y[5][sgm]), pk_bf16(y[6][sgm], y[7][sgm])},
                                                           act, dst + 512 * sgm, 0, ST_AUX);
            }
        }
    }
    STAMP(3);

    f32x2 ure[8], uim[8];
    {
        const float sbr = csum(0), sbi = csum(1);
#pragma unroll
        for (int j = 0; j < 8; ++j) {
            const f32x4 tb = tab1[j];
            const f32x2 a = {tb[0], tb[1]}, bb = {tb[2], tb[3]};
            const int b = j >> 1, r = 2 * (j & 1);
            ure[j] = pfma(a, f32x2{acc[b][0][r], acc[b][0][r + 1]}, bb * splat(sbr));
            uim[j] = pfma(a, f32x2{acc[b][1][r], acc[b][1][r + 1]}, bb * splat(sbi));
        }
    }
    f32x2 hr = ure[0], hi = uim[0];
#pragma unroll
    for (int j = 1; j < 8; ++j) {
        const f32x2 tr = pfma(splat(lam.r), hr, ure[j]), ti = pfma(splat(lam.r), hi, uim[j]);
        const f32x2 nr = pfma(splat(-lam.i), hi, tr), ni = pfma(splat(lam.i), hr, ti);
        hr = nr; hi = ni;
    }
    const cf eA = cf{hr[0], hi[0]};
    const cf e = cfma(l8, eA, cf{hr[1], hi[1]});
    const int sig = 4 * wm + g;
    LAS f32x2* eL = (LAS f32x2*)(smem + L_EL);
    eL[sig * 32 + 16 * wn + li] = f32x2{e.r, e.i};
    wait_lgkm0();
    bar();
    cf Lp = cf{0.0f, 0.0f};
#pragma unroll
    for (int s = 0; s < 7; ++s) {
        const f32x2 v = eL[s * 32 + 16 * wn + li];
        const cf n = cfma(l16, Lp, cf{v[0], v[1]});
        if (s < sig) Lp = n;
    }
    wait_vm<0>();
    if (lane == 0) __hip_atomic_store((GAS unsigned*)(ws + WS_FH) + Tw * 32 + 2 * cj + wn, tag, __ATOMIC_RELAXED, __HIP_MEMORY_SCOPE_AGENT);
    GAS u32x4* eg = (GAS u32x4*)(ws + WS_EG);
    if (sig == 7) {
        const cf E = cfma(l16, Lp, e);
        store16_sc1(eg + ci * NP + p, u32x4{__float_as_uint(E.r), tag, __float_as_uint(E.i), tag});
    }
    bar();
    STAMP(4);

    const int wd = wave >> 1, wt = wave & 1;
    f32x4 o[2][2];
    cf Hin = cf{0.0f, 0.0f};
    {
#pragma unroll
        for (int j = 0; j < 2; ++j) {
            const f32x2 st = ((LAS f32x2*)(smem + L_TOK))[64 * (T & 1) + 32 * j + 16 * ((lane >> 3) & 1) + 8 * wt + (lane & 7)];
#pragma unroll
            for (int i = 0; i < 2; ++i) {
                const float xe[4] = {bf_lo(xv[i][j][0]), bf_hi(xv[i][j][0]), bf_lo(xv[i][j][1]), bf_hi(xv[i][j][1])};
                const float de[4] = {bf_lo(dv[i][0]), bf_hi(dv[i][0]), bf_lo(dv[i][1]), bf_hi(dv[i][1])};
                const float be[4] = {bf_lo(bv[i][0]), bf_hi(bv[i][0]), bf_lo(bv[i][1]), bf_hi(bv[i][1])};
#pragma unroll
                for (int k = 0; k < 4; ++k) {
                    const float xn = rbf(rbf(xe[k] - st[0]) * st[1]);
                    o[i][j][k] = xe[k] + rbf(xn * de[k]) + be[k];
                }
            }
        }
        asm volatile("" : "+v"(o[0][0]), "+v"(o[0][1]), "+v"(o[1][0]), "+v"(o[1][1]));
        const GAS u32x4* ep = eg + g * NP + p;
        u32x4 gr[4];
        auto fetch = [&]() {
            asm volatile("global_load_dwordx4 %0, %4, off sc1\n\tglobal_load_dwordx4 %1, %5, off sc1\n\t"
                         "global_load_dwordx4 %2, %6, off sc1\n\tglobal_load_dwordx4 %3, %7, off sc1"
                         : "=&v"(gr[0]), "=&v"(gr[1]), "=&v"(gr[2]), "=&v"(gr[3])
                         : "v"(ep), "v"(ep + 4 * NP), "v"(ep + 8 * NP), "v"(ep + 12 * NP) : "memory");
        };
        auto landed = [&]() { asm volatile("s_waitcnt vmcnt(0)" : "+v"(gr[0]), "+v"(gr[1]), "+v"(gr[2]), "+v"(gr[3])::"memory"); };
        auto complete = [&]() {
            bool ok = true;
#pragma unroll
            for (int i = 0; i < 4; ++i) ok &= (4 * i + g >= ci) | ((gr[i][1] == tag) & (gr[i][3] == tag));
            return __all(ok);
        };
        fetch();
        const cf l256 = cmul(l128, l128), l512 = cmul(l256, l256);
        landed();
#pragma unroll 1
        for (unsigned spins = 0; spins < SPIN_CAP && !complete(); ++spins) { __builtin_amdgcn_s_sleep(2); fetch(); landed(); }
        cf hp = cf{0.0f, 0.0f};
#pragma unroll
        for (int i = 0; i < 4; ++i) {
            const cf n = cfma(l512, hp, cf{__uint_as_float(gr[i][0]), __uint_as_float(gr[i][2])});
            if (4 * i + g < ci) hp = n;
        }
        const int rr = (ci - 1 - g) & 3;
        if (rr & 1) hp = cmul(hp, l128);
        if (rr & 2) hp = cmul(hp, l256);
        Hin = cf{sum_groups(hp.r), sum_groups(hp.i)};
    }
    STAMP(5);
    u32x4 hq[2][2];
    unsigned hdr, hdi;
    auto store_h = [&]() {
#pragma unroll
        for (int sgm = 0; sgm < 2; ++sgm) {
            __builtin_amdgcn_raw_buffer_store_b128(hq[0][sgm], act, hdr + 512 * sgm, 0, ST_AUX);
            __builtin_amdgcn_raw_buffer_store_b128(hq[1][sgm], act, hdi + 512 * sgm, 0, ST_AUX);
        }
    };
    {
        const cf cA = cfma(lsig, Hin, Lp);
        const cf cB = cfma(l8, cA, eA);
        f32x2 sr = {cA.r, cB.r}, si = {cA.i, cB.i};
        f32x2 ore[8], oim[8];
#pragma unroll
        for (int j = 0; j < 8; ++j) {
            const f32x2 tr = pfma(splat(lam.r), sr, ure[j]), ti = pfma(splat(lam.r), si, uim[j]);
            const f32x2 nr = pfma(splat(-lam.i), si, tr), ni = pfma(splat(lam.i), sr, ti);
            sr = nr; si = ni;
            ore[j] = pfma(splat(coef2.r), sr, splat(-coef2.i) * si);
            oim[j] = pfma(splat(-coef2.r), si, splat(-coef2.i) * sr);
        }
        const int kr = FF + p, kim = FF + NP + p;
        hdr = act0 + (kr >> 3) * 1024 + (kr & 7) * 64 + 16 * g;
        hdi = act0 + (kim >> 3) * 1024 + (kim & 7) * 64 + 16 * g;
#pragma unroll
        for (int sgm = 0; sgm < 2; ++sgm) {
            hq[0][sgm] = u32x4{pk_bf16(ore[0][sgm], ore[1][sgm]), pk_bf16(ore[2][sgm], ore[3][sgm]), pk_bf16(ore[4][sgm], ore[5][sgm]), pk_bf16(ore[6][sgm], ore[7][sgm])};
            hq[1][sgm] = u32x4{pk_bf16(oim[0][sgm], oim[1][sgm]), pk_bf16(oim[2][sgm], oim[3][sgm]), pk_bf16(oim[4][sgm], oim[5][sgm]), pk_bf16(oim[6][sgm], oim[7][sgm])};
        }
        if (!DEFER) store_h();
    }
    STAMP(6);

    {
        const int trb = (g >> 1) * 2080 + (g & 1) * 1056 + 64 * q + 16 * (pp >> 1) + 8 * (pp & 1);
        const int tra = trb + 512 * wt, trw = P2_IMG + trb + 512 * wd;
        const int ca0 = P2_IMG + (32 * wd + li) * 128 + 16 * (g ^ (lane & 7));
        s16x8 wf[2][2][2], af[2][2][2];
        int slot = 0;
        auto next = [&]() { slot = slot == P2_RING - P2_SLOT ? 0 : slot + P2_SLOT; };
        auto frag_a = [&](LAS char* sl, int buf, int ks, int i) { LAS char* a = sl + tra + 4160 * ks + 32 * i; af[buf][ks][i] = lds_tr(a, a + 256); };
        auto frag_w = [&](LAS char* sl, int buf, int ks, int i, bool g4) {
            if (g4) { LAS char* a = sl + trw + 4160 * ks + 32 * i; wf[buf][ks][i] = lds_tr(a, a + 256); }
            else wf[buf][ks][i] = lds_row(sl + ((ca0 ^ (64 * ks)) + 2048 * i));
        };
        auto half = [&](int rb, bool rd, bool g4, int k0, bool mm_) {
            LAS char* sl = smem + slot;
            const int ub = rb ^ 1;
#pragma unroll
            for (int ks = 0; ks < 2; ++ks) {
#pragma unroll
                for (int m = 0; m < 4; ++m) {
                    if (mm_ && ks >= k0) mfma_acc(o[m >> 1][m & 1], wf[ub][ks][m >> 1], af[ub][ks][m & 1]);
                    SB0;
                    if (rd) { if (m < 2) frag_a(sl, rb, ks, m); else frag_w(sl, rb, ks, m - 2, g4); }
                    SB0;
                }
            }
            if (rd) next();
        };
        const GAS unsigned* fsp = (const GAS unsigned*)(ws + WS_FS) + T * 32 + (lane & 31);
        const unsigned fsl = (unsigned)(size_t)smem + L_EL + 256 * wave;
        __builtin_amdgcn_s_setprio(CPRIO2);
        bar(); half(0, true, true, 0, false);
        STAMP(7);
        if (DEFER) store_h();
        half(1, true, true, 0, true);
#pragma unroll 1
        for (int j = 1; j < 10; ++j) {
            bar(); half(0, true, true, 0, true);
            half(1, true, true, 0, true);
            if (j == 1 + DEFER) {
                wait_vm<0>();
                if (lane == 0) __hip_atomic_store((GAS unsigned*)(ws + WS_FS) + Tw * 32 + 2 * cj + wn, tag, __ATOMIC_RELAXED, __HIP_MEMORY_SCOPE_AGENT);
            }
            if (j == 6) asm volatile("s_mov_b32 m0, %0\n\ts_nop 0\n\tglobal_load_lds_dword %1, off sc1" ::"s"(fsl), "v"(fsp) : "memory");
            if (j == 7) {
                unsigned fsv;
                asm volatile("s_waitcnt vmcnt(0)\n\tds_read_b32 %0, %1\n\ts_waitcnt lgkmcnt(0)" : "=&v"(fsv) : "v"(fsl + 4 * lane) : "memory");
                if (!__all(fsv == tag)) wait_flags((const GAS unsigned*)(ws + WS_FS) + T * 32, tag);
            }
            SB0;
        }
        bar(); half(0, true, true, 0, true);
        half(1, true, true, 0, true);
        bar(); half(0, true, false, 1, true);
        half(1, true, false, 0, true);
#pragma unroll 1
        for (int j = 12; j < 19; ++j) {
            bar(); half(0, true, false, 0, true);
            half(1, true, false, 0, true);
        }
        half(0, false, false, 0, true);
        asm volatile("s_nop 7\n\ts_nop 7" : "+v"(o[0][0]), "+v"(o[0][1]), "+v"(o[1][0]), "+v"(o[1][1]));
        STAMP(8);

#pragma unroll
        for (int j = 0; j < 2; ++j)
#pragma unroll
            for (int i = 0; i < 2; ++i)
                *(f32x4*)(out + (64 * T + 32 * j + 16 * ((lane >> 3) & 1) + 8 * wt + (lane & 7)) * DM + d0 + 32 * wd + 16 * i + 4 * g) = o[i][j];
        if (ci == 15 && cj == 0 && threadIdx.x == 0) __hip_atomic_store((GAS unsigned*)(ws + WS_EP), tag, __ATOMIC_RELAXED, __HIP_MEMORY_SCOPE_AGENT);
        STAMP(9);
    }
}

}

extern "C" void kernel_launch(void* const* d_in, const int* in_sizes, int n_in, void* d_out, int out_size, void* d_ws, size_t ws_size,
                              hipStream_t stream) {
    typedef const unsigned short* P;
    (void)hipFuncSetAttribute((const void*)k_fused, hipFuncAttributeMaxDynamicSharedMemorySize, LDS_BYTES);
    k_fused<<<256, 512, LDS_BYTES, stream>>>((P)d_in[0], (P)d_in[1], (P)d_in[2], (P)d_in[3], (P)d_in[4], (P)d_in[5], (P)d_in[6], (P)d_in[7],
                                             (P)d_in[8], (P)d_in[10], (P)d_in[11], (P)d_in[12], (P)d_in[13], (char*)d_ws, (float*)d_out);
}
```

```cpp
#include <hip/hip_runtime.h>
#include <hip/hip_bf16.h>
#include <stdint.h>
#include <type_traits>

#define LAS __attribute__((address_space(3)))
#define GAS __attribute__((address_space(1)))

typedef __attribute__((ext_vector_type(8))) short s16x8;
typedef __attribute__((ext_vector_type(4))) short s16x4;
typedef __attribute__((ext_vector_type(8))) __bf16 bf16x8;
typedef __attribute__((ext_vector_type(2))) __bf16 bf16x2;
typedef __attribute__((ext_vector_type(4))) float f32x4;
typedef __attribute__((ext_vector_type(2))) float f32x2;
typedef __attribute__((ext_vector_type(4))) unsigned u32x4;
typedef __attribute__((ext_vector_type(2))) unsigned u32x2;

#define ACT_AUX 0
#define ST_AUX 16
#define PREFETCH 1
#define WARM1 0
#define PRIO 0
#define WARMA 0
#define D4 4
#define D3 3
#define PREF2 0
#define DEFER 0
#define LPRIO2 0
#define CPRIO2 0
#define CODEW 2
#define CODE_LINES 200
#define LAMLATE 0
#define ACQ 0

namespace {

constexpr int SEQ = 2048, DM = 512, NP = 512, FF = 1376;
constexpr int KACT = FF + 2 * NP;
constexpr int ACT_TILE = KACT * 128;
constexpr unsigned WS_ACT = 0;
constexpr unsigned WS_RS = 10u << 20;
constexpr unsigned WS_EG = WS_RS + (64u << 10);
constexpr unsigned WS_FH = WS_EG + (256u << 10);
constexpr unsigned WS_FS = WS_FH + (8u << 10);
constexpr unsigned WS_CS = WS_FS + (8u << 10);
constexpr unsigned WS_EP = WS_CS + (256u << 10);
constexpr size_t WS_STAMP = 200u << 20;
constexpr float LN_EPS = 1e-5f;
constexpr unsigned SPIN_CAP = 1u << 18;

constexpr int P1_SLOT = 36 * 1024, P1_X = 0, P1_B = 16384, P1_W = 24576;
constexpr int P2_IMG = 4 * 2080, P2_SLOT = 2 * P2_IMG, P2_RING = 9 * P2_SLOT, P2_NST = 38;
constexpr int L_TAB1 = P2_RING;
constexpr int L_TAB2 = L_TAB1 + 1024;
constexpr int L_EL = L_TAB2 + 1024;
constexpr int L_CS = L_EL + 2048;
constexpr int L_TOK = L_CS + 1280;
constexpr int L_DUMMY = L_TOK + 1024;
constexpr int L_MAIL = L_DUMMY + 1024;
constexpr int LDS_BYTES = L_MAIL + 64 + 704;

#define STAMP(s) do {} while (0)

#define SB0 __builtin_amdgcn_sched_barrier(0)
template <int N> __device__ __forceinline__ void wait_vm() { asm volatile("s_waitcnt vmcnt(%0)" ::"i"(N) : "memory"); }
__device__ __forceinline__ void wait_lgkm0() { __builtin_amdgcn_s_waitcnt(0xC07F); asm volatile("" ::: "memory"); }
template <int NM, int ND, int DPM = 1, int I = 0> __device__ __forceinline__ void interleave() {
    if constexpr (I * DPM < ND && I < NM) {
        __builtin_amdgcn_sched_group_barrier(0x008, 1, 0);
        __builtin_amdgcn_sched_group_barrier(0x100, (ND - I * DPM) < DPM ? (ND - I * DPM) : DPM, 0);
        interleave<NM, ND, DPM, I + 1>();
    } else if constexpr (I < NM) {
        __builtin_amdgcn_sched_group_barrier(0x008, NM - I, 0);
    }
}
__device__ __forceinline__ void bar() {
    SB0;
    asm volatile("" ::: "memory");
    __builtin_amdgcn_s_barrier();
    asm volatile("" ::: "memory");
    SB0;
}
__device__ __forceinline__ float bf_lo(unsigned u) { return __uint_as_float(u << 16); }
__device__ __forceinline__ float bf_hi(unsigned u) { return __uint_as_float(u & 0xffff0000u); }
__device__ __forceinline__ float bf2f(unsigned short h) { return __uint_as_float((unsigned)h << 16); }
__device__ __forceinline__ unsigned pk_bf16(float a, float b) { bf16x2 v = {(__bf16)a, (__bf16)b}; return __builtin_bit_cast(unsigned, v); }
__device__ __forceinline__ float rbf(float a) { return bf_lo(pk_bf16(a, a)); }
__device__ __forceinline__ f32x2 pfma(f32x2 a, f32x2 b, f32x2 c) { return __builtin_elementwise_fma(a, b, c); }
__device__ __forceinline__ f32x2 splat(float a) { return f32x2{a, a}; }
__device__ __forceinline__ f32x4 mfma(s16x8 a, s16x8 b, f32x4 c) {
    return __builtin_amdgcn_mfma_f32_16x16x32_bf16(__builtin_bit_cast(bf16x8, a), __builtin_bit_cast(bf16x8, b), c, 0, 0, 0);
}
__device__ __forceinline__ void mfma_acc(f32x4& c, s16x8 a, s16x8 b) {
    asm volatile("v_mfma_f32_16x16x32_bf16 %0, %1, %2, %0" : "+v"(c) : "v"(a), "v"(b));
}
__device__ __forceinline__ s16x4 lds_tr1(LAS char* p) { return __builtin_amdgcn_ds_read_tr16_b64_v4i16((LAS s16x4*)p); }
__device__ __forceinline__ s16x8 cat(s16x4 a, s16x4 b) { return __builtin_shufflevector(a, b, 0, 1, 2, 3, 4, 5, 6, 7); }
__device__ __forceinline__ s16x8 lds_row(LAS char* p) { return *(LAS s16x8*)p; }
__device__ __forceinline__ s16x8 lds_tr(LAS char* p0, LAS char* p1) {
    s16x4 a = __builtin_amdgcn_ds_read_tr16_b64_v4i16((LAS s16x4*)p0);
    s16x4 b = __builtin_amdgcn_ds_read_tr16_b64_v4i16((LAS s16x4*)p1);
    return __builtin_shufflevector(a, b, 0, 1, 2, 3, 4, 5, 6, 7);
}
template <int OFF> __device__ __forceinline__ void dma1(unsigned lds, unsigned voff, const GAS char* base) {
    asm volatile("s_mov_b32 m0, %0\n\ts_nop 0\n\tglobal_load_lds_dwordx4 %1, %2 offset:%3" ::"s"(lds), "v"(voff), "s"(base), "i"(OFF) : "memory");
}
template <int OFF> __device__ __forceinline__ void dma2(unsigned lds, unsigned voff0, unsigned voff1, const GAS char* base) {
    asm volatile("s_mov_b32 m0, %0\n\ts_nop 0\n\tglobal_load_lds_dwordx4 %1, %3\n\tglobal_load_lds_dwordx4 %2, %3 offset:%4"
                 ::"s"(lds), "v"(voff0), "v"(voff1), "s"(base), "i"(OFF) : "memory");
}
__device__ __forceinline__ void touch(unsigned lds, const GAS char* a) {
    asm volatile("s_mov_b32 m0, %0\n\ts_nop 0\n\tglobal_load_lds_dword %1, off" ::"s"(lds), "v"(a) : "memory");
}
__device__ __forceinline__ void lds_st32(unsigned a, float v) { asm volatile("ds_write_b32 %0, %1" ::"v"(a), "v"(v) : "memory"); }
__device__ __forceinline__ void lds_st64(unsigned a, f32x2 v) { asm volatile("ds_write_b64 %0, %1" ::"v"(a), "v"(v) : "memory"); }
template <int CTRL, int RM> __device__ __forceinline__ float dpp_add(float v) {
    return v + __uint_as_float(__builtin_amdgcn_update_dpp(0u, __float_as_uint(v), CTRL, RM, 0xf, true));
}
__device__ __forceinline__ float wave_sum(float v) {
    v = dpp_add<0x111, 0xf>(v); v = dpp_add<0x112, 0xf>(v); v = dpp_add<0x114, 0xf>(v); v = dpp_add<0x118, 0xf>(v);
    v = dpp_add<0x142, 0xa>(v); v = dpp_add<0x143, 0xc>(v);
    return __uint_as_float(__builtin_amdgcn_readlane(__float_as_uint(v), 63));
}
__device__ __forceinline__ float sum_groups(float v) {
    const auto a = __builtin_amdgcn_permlane16_swap(__float_as_uint(v), __float_as_uint(v), false, false);
    v = __uint_as_float(a[0]) + __uint_as_float(a[1]);
    const auto b = __builtin_amdgcn_permlane32_swap(__float_as_uint(v), __float_as_uint(v), false, false);
    return __uint_as_float(b[0]) + __uint_as_float(b[1]);
}
__device__ __forceinline__ u32x4 load16_sc1(const GAS void* p) {
    u32x4 v;
    asm volatile("global_load_dwordx4 %0, %1, off sc1\n\ts_waitcnt vmcnt(0)" : "=&v"(v) : "v"(p) : "memory");
    return v;
}
__device__ __forceinline__ void store16_sc1(GAS void* p, u32x4 v) {
    asm volatile("global_store_dwordx4 %0, %1, off sc1\n\ts_nop 1" ::"v"(p), "v"(v) : "memory");
}
__device__ __forceinline__ void wait_flags(const GAS unsigned* f, unsigned tag) {
#pragma unroll 1
    for (unsigned spins = 0; spins < SPIN_CAP; ++spins) {
        const unsigned v = __hip_atomic_load(f + (threadIdx.x & 31), __ATOMIC_RELAXED, __HIP_MEMORY_SCOPE_AGENT);
        if (__all(v == tag)) break;
        __builtin_amdgcn_s_sleep(1);
    }
    asm volatile("" ::: "memory");
}
struct cf { float r, i; };
__device__ __forceinline__ cf cmul(cf a, cf b) { return cf{a.r * b.r - a.i * b.i, a.r * b.i + a.i * b.r}; }
__device__ __forceinline__ cf cfma(cf a, cf b, cf c) { return cf{fmaf(a.r, b.r, fmaf(-a.i, b.i, c.r)), fmaf(a.r, b.i, fmaf(a.i, b.r, c.i))}; }
__device__ __forceinline__ float exp_f(float x) {
    const float hi = x * 1.44269504f, lo = fmaf(x, 1.44269504f, -hi) + x * 1.92596299e-8f;
    return __builtin_amdgcn_exp2f(hi) * fmaf(lo, 0.693147181f, 1.0f);
}
__device__ __forceinline__ void sincos_f(float x, float& s, float& c) {
    const float k = rintf(x * 0.636619772f);
    float r = fmaf(k, -1.57079601e+00f, x);
    r = fmaf(k, -3.13916473e-07f, r);
    r = fmaf(k, -5.39030253e-15f, r);
    const float z = r * r;
    const float sp = fmaf(r * z, fmaf(z, fmaf(z, fmaf(z, 2.75573137e-06f, -1.98412698e-04f), 8.33333333e-03f), -1.66666667e-01f), r);
    const float cp = fmaf(z, fmaf(z, fmaf(z, fmaf(z, fmaf(z, -2.75573144e-07f, 2.48015873e-05f), -1.38888889e-03f), 4.16666667e-02f), -0.5f), 1.0f);
    const int q = (int)k;
    const float a = (q & 1) ? cp : sp, b = (q & 1) ? sp : cp;
    s = (q & 2) ? -a : a;
    c = ((q + 1) & 2) ? -b : b;
}
__device__ __forceinline__ unsigned row_std(float v) {
    const float s1 = rbf(__builtin_amdgcn_sqrtf(rbf(rbf(v) + LN_EPS)));
    const float r1 = __builtin_amdgcn_rcpf(s1);
    const float s2 = __builtin_amdgcn_sqrtf(rbf(rbf(v * r1 * r1) + LN_EPS));
    return pk_bf16(s1, s2);
}

typedef const unsigned short* __restrict__ bfp;

__global__ __launch_bounds__(512) void k_fused(bfp x, bfp lam_re, bfp lam_im, bfp bre, bfp bim, bfp cre, bfp cim, bfp log_dt, bfp dskip,
                                               bfp w1, bfp b1, bfp w2, bfp b2, char* ws_, float* __restrict__ out) {
    extern __shared__ __attribute__((aligned(16))) char smem_[];
    LAS char* smem = (LAS char*)smem_;
    GAS char* ws = (GAS char*)ws_;
    const int wave = __builtin_amdgcn_readfirstlane(threadIdx.x >> 6), lane = threadIdx.x & 63;
    const int g = lane >> 4, li = lane & 15;
    const int xg8 = blockIdx.x & 7, loc = blockIdx.x >> 3, ci = 4 * (xg8 >> 1) + (loc >> 3), cj = 8 * (xg8 & 1) + (loc & 7);
    const int t0 = 128 * ci, p0 = 32 * cj, f0 = cj < 11 ? 96 * cj : 1056 + 64 * (cj - 11), nfu = cj < 11 ? 6 : 4;
    const int T = 2 * ci + (cj >> 3), d0 = 64 * (cj & 7);
    const int rot = ((ci + 15) >> 1) & 7;
    STAMP(0);
    unsigned long long entry_pc;
    asm volatile("s_getpc_b64 %0" : "=s"(entry_pc));
    unsigned epoch;
    asm volatile("global_load_dword %0, %1, off sc1" : "=v"(epoch) : "v"(ws + WS_EP) : "memory");
    const int q = li >> 2, pp = lane & 3;

    if (__builtin_expect(wave >= 4, 1)) {
        const int lw = wave - 4;
        __builtin_amdgcn_s_setprio(PRIO);
        const unsigned lds0 = (unsigned)(size_t)smem;
        unsigned xo[4], bo[2], wo[3];
#pragma unroll
        for (int i = 0; i < 4; ++i) {
            const int R = 8 * (4 * lw + i) + (lane >> 3), ii = R & 15, bb = (R >> 4) & 3;
            const int tok = t0 + (R & 64) + 16 * (ii >> 2) + 8 * (ii & 1) + 2 * bb + ((ii >> 1) & 1);
            xo[i] = tok * 1024 + 16 * ((lane & 7) ^ (lane >> 3)) + 3072 - 1024 * i;
        }
#pragma unroll
        for (int i = 0; i < 2; ++i) {
            const int R = 8 * (2 * lw + i) + (lane >> 3);
            bo[i] = (p0 + (R & 31)) * 1024 + 16 * ((lane & 7) ^ (lane >> 3)) + 3072 - 1024 * i;
        }
#pragma unroll
        for (int i = 0; i < 3; ++i) {
            const int st = 2 * (3 * lw + i) + (lane >> 5), kg = st / 3, ns = st - 3 * kg, kr = (lane & 31) >> 2;
            const int ch = 4 * ns + ((lane & 3) ^ ((2 * kg + (kr >> 2)) & 3));
            const int f = min(f0 + 8 * ch, FF - 8);
            wo[i] = (8 * kg + kr) * (FF * 2) + f * 2 + 3072 - 1024 * i;
        }
        const GAS char* xg = (const GAS char*)x - 3072;
        const GAS char* bg = (const GAS char*)(lw < 2 ? bre : bim) - 3072;
        const GAS char* wg = (const GAS char*)w1 - 3072;
        const unsigned lx = lds0 + P1_X + lw * 4096, lb = lds0 + P1_B + lw * 2048, lwb = lds0 + P1_W + lw * 3072;
        auto piece = [&](int t, auto I) {
            constexpr int i = decltype(I)::value;
            const unsigned so = (t & 3) * P1_SLOT;
            const int kt = (t + rot) & 7;
            if constexpr (i < 4) dma1<1024 * i>(lx + so, xo[i], xg + kt * 128);
            else if constexpr (i < 6) dma1<1024 * (i - 4)>(lb + so, bo[i - 4], bg + kt * 128);
            else dma1<1024 * (i - 6)>(lwb + so, wo[i - 6], wg + (size_t)kt * (64 * FF * 2));
        };
#define PC(t, i) piece(t, std::integral_constant<int, i>{})
        auto issue = [&](int t) { PC(t, 0); PC(t, 1); PC(t, 2); PC(t, 3); PC(t, 4); PC(t, 5); PC(t, 6); PC(t, 7); PC(t, 8); };
        const GAS char* tbase;
        unsigned tstep;
        if (lw == 0) { tbase = (const GAS char*)x + (t0 + 16 * (cj & 7) + li) * 1024; tstep = 128; }
        else if (lw == 1) { const int r = 16 * (ci & 3) + li; tbase = (const GAS char*)(r < 32 ? bre : bim) + (p0 + (r & 31)) * 1024; tstep = 128; }
        else { tbase = (const GAS char*)w1 + (16 * (ci & 3) + li) * (FF * 2) + min(f0 * 2 + (lw == 2 ? 0 : 128), FF * 2 - 4); tstep = 64 * FF * 2; }
        auto warm = [&](int t) { if (WARM1) touch(lds0 + L_DUMMY + 256 * lw, tbase + ((t + rot) & 7) * tstep); };
        STAMP(6);
        if (CODEW == 1) {
            touch(lds0 + L_DUMMY + 256 * lw, (const GAS char*)(entry_pc & ~255ull) + min(((loc & 3) * 4 + lw) * 13 + min(lane, 12), CODE_LINES - 1) * 128);
        }
        issue(0); issue(1);
        warm(2 + (g & 1));
        if (CODEW == 2) {
            touch(lds0 + L_DUMMY + 256 * lw, (const GAS char*)(entry_pc & ~255ull) + min(((loc & 3) * 4 + lw) * 13 + min(lane, 12), CODE_LINES - 1) * 128);
        }
        STAMP(7);

        unsigned w2o[2], co[2], ao[2];
#pragma unroll
        for (int i = 0; i < 2; ++i) {
            const int kg = 2 * lw + i;
            w2o[i] = (8 * kg + ((lane & 31) >> 2)) * (DM * 2) + (d0 + 32 * (lane >> 5) + 8 * (lane & 3)) * 2 + 3072 - 1056 * i;
            co[i] = (d0 + 8 * kg + (lane >> 3)) * (NP * 2) + 16 * ((lane & 7) ^ (lane >> 3)) + 3072 - 1024 * i;
            ao[i] = kg * 1024 + lane * 16 + 3072 - 1056 * i;
        }
        const GAS char* ag = ws + WS_ACT + (size_t)T * ACT_TILE;
        const GAS char* w2g = (const GAS char*)w2 - 3072;
        const unsigned l2 = lds0 + lw * 2080, l2c = lds0 + P2_IMG + lw * 2048;
        auto dmaA = [&](const GAS char* src, unsigned slot) { dma2<1056>(l2 + slot, ao[0], ao[1], src - 3072); };
        auto dmaW = [&](const GAS char* src, unsigned slot) { dma2<1056>(l2 + slot + P2_IMG, w2o[0], w2o[1], src); };
        auto dmaC = [&](const GAS char* src, unsigned slot) { dma2<1024>(l2c + slot, co[0], co[1], src); };

        const GAS char* l2a[2];
#pragma unroll
        for (int i = 0; i < 2; ++i) {
            const int n = 456 * (ci & 3) + 114 * lw + min(64 * i + lane, 113);
            const int m = n - 800;
            l2a[i] = n < 800 ? (const GAS char*)w2 + (576 + n) * 1024 + d0 * 2
                             : (const GAS char*)(m < 512 ? cre : cim) + (d0 + ((m >> 3) & 63)) * 1024 + (m & 7) * 128;
        }
        const int n1 = 75 * blockIdx.x + 19 * lw + min(lane, 18);
        const GAS char* l1a = n1 < 11008 ? (const GAS char*)w2 + n1 * 128
                                         : (n1 < 15104 ? (const GAS char*)cre + (n1 - 11008) * 128 : (const GAS char*)cim + (min(n1, 19199) - 15104) * 128);
        const GAS u32x4* rsp = (const GAS u32x4*)(ws + WS_RS) + t0 + 64 * (lw & 1) + lane;
        const GAS u32x4* csp = (const GAS u32x4*)(ws + WS_CS) + ((cj * 16 + 8 * (lw >> 1) + 2 * g) * 2 + (lw & 1)) * 32 + li;
        asm volatile("" : "+v"(l2a[0]), "+v"(l2a[1]), "+v"(l1a), "+v"(rsp), "+v"(csp));
        asm volatile("s_waitcnt vmcnt(%1)" : "+v"(epoch) : "i"(9 + WARM1 + (CODEW == 2)) : "memory");
        unsigned tag = __builtin_amdgcn_readfirstlane(epoch) + 1u;
        asm volatile("" : "+s"(tag));
        bar();
        STAMP(1);
        issue(2); warm(4);
        if (PREFETCH) touch(lds0 + L_DUMMY + 256 * lw, l1a);
        wait_vm<9 + 2 * WARM1 + PREFETCH>(); bar();
        issue(3); warm(5);
        {
            f32x2 v;
            asm volatile("ds_read_b64 %0, %1\n\ts_waitcnt lgkmcnt(0)" : "=&v"(v) : "v"(lds0 + L_MAIL + 16 * lw + 8 * (lane & 1)) : "memory");
            if (lane < 2) store16_sc1((GAS u32x4*)(ws + WS_RS) + t0 + 8 * cj + 2 * lw + lane, u32x4{__float_as_uint(v[0]), tag, __float_as_uint(v[1]), tag});
        }
        wait_vm<10 + 2 * WARM1 + PREFETCH>(); bar();
        issue(4); warm(6);
        {
            float a, b, c;
            const unsigned ma = lds0 + L_MAIL + 64 + 4 * (((lw >> 1) * 5 + 3 * (lw & 1)) * 16 + li);
            asm volatile("ds_read_b32 %0, %3\n\tds_read_b32 %1, %3 offset:64\n\tds_read_b32 %2, %3 offset:128\n\ts_waitcnt lgkmcnt(0)"
                         : "=&v"(a), "=&v"(b), "=&v"(c) : "v"(ma) : "memory");
            if (g == 0) store16_sc1((GAS u32x4*)(ws + WS_CS) + ((cj * 16 + ci) * 2 + (lw >> 1)) * 32 + 16 * (lw & 1) + li,
                                    u32x4{__float_as_uint(a), __float_as_uint(b), __float_as_uint(c), tag});
        }
        wait_vm<11 + 2 * WARM1>(); bar();
        issue(5); warm(7); wait_vm<10 + 2 * WARM1>(); bar();
        issue(6); wait_vm<9 + WARM1>(); bar();
        issue(7);
        u32x4 rs, cs[4];
        auto gather = [&]() {
            asm volatile("global_load_dwordx4 %0, %5, off sc1\n\t"
                         "global_load_dwordx4 %1, %6, off sc1\n\tglobal_load_dwordx4 %2, %6, off offset:256 sc1\n\t"
                         "global_load_dwordx4 %3, %6, off offset:1024 sc1\n\tglobal_load_dwordx4 %4, %6, off offset:1280 sc1"
                         : "=&v"(rs), "=&v"(cs[0]), "=&v"(cs[1]), "=&v"(cs[2]), "=&v"(cs[3]) : "v"(rsp), "v"(csp) : "memory");
        };
        gather();
        wait_vm<14>(); bar();
        dmaW(w2g, 0); dmaW(w2g + 65536, P2_SLOT); dmaW(w2g + 2 * 65536, 2 * P2_SLOT); dmaW(w2g + 3 * 65536, 3 * P2_SLOT);
        if (PREF2 == 1) { touch(lds0 + L_DUMMY + 256 * lw, l2a[0]); touch(lds0 + L_DUMMY + 256 * lw, l2a[1]); }
        asm volatile("s_waitcnt vmcnt(%5)" : "+v"(rs), "+v"(cs[0]), "+v"(cs[1]), "+v"(cs[2]), "+v"(cs[3]) : "i"(PREF2 == 1 ? 10 : 8) : "memory");
        bar();
        dmaW(w2g + 4 * 65536, 4 * P2_SLOT); dmaW(w2g + 5 * 65536, 5 * P2_SLOT);
        auto fresh = [&]() {
            return __all(rs[1] == tag && rs[3] == tag && cs[0][3] == tag && cs[1][3] == tag && cs[2][3] == tag && cs[3][3] == tag);
        };
#pragma unroll 1
        for (unsigned spins = 0; spins < SPIN_CAP && !fresh(); ++spins) {
            gather();
            asm volatile("s_waitcnt vmcnt(0)" : "+v"(rs), "+v"(cs[0]), "+v"(cs[1]), "+v"(cs[2]), "+v"(cs[3])::"memory");
        }
        if (lw < 2) {
            const float m = __uint_as_float(rs[0]), mb = rbf(m);
            const float r1 = __builtin_amdgcn_rcpf(bf_lo(rs[2])), r12 = r1 * __builtin_amdgcn_rcpf(bf_hi(rs[2]));
            const int tau = 64 * lw + lane;
            const unsigned ta = lds0 + L_TAB1 + 4 * (((tau >> 4) * 8 + (tau & 7)) * 4 + ((tau >> 3) & 1));
            lds_st32(ta, r1); lds_st32(ta + 8, -r1 * mb);
            lds_st32(ta + 1024, r12); lds_st32(ta + 1032, -r12 * m);
            lds_st64(lds0 + L_TOK + 8 * tau, f32x2{mb, r1});
        }
        {
            float v5[5];
#pragma unroll
            for (int k = 0; k < 3; ++k) v5[k] = __uint_as_float(cs[0][k]) + __uint_as_float(cs[2][k]);
#pragma unroll
            for (int k = 0; k < 2; ++k) v5[3 + k] = __uint_as_float(cs[1][k]) + __uint_as_float(cs[3][k]);
#pragma unroll
            for (int k = 0; k < 5; ++k) v5[k] = sum_groups(v5[k]);
            if (g == 0) {
#pragma unroll
                for (int k = 0; k < 5; ++k) lds_st32(lds0 + L_CS + 4 * ((((lw >> 1) * 2 + (lw & 1)) * 5 + k) * 16 + li), v5[k]);
            }
        }
        asm volatile("s_waitcnt lgkmcnt(0)" ::: "memory");
        bar();
        dmaW(w2g + 6 * 65536, 6 * P2_SLOT); dmaW(w2g + 7 * 65536, 7 * P2_SLOT); dmaW(w2g + 8 * 65536, 8 * P2_SLOT);
        STAMP(2);
        if (PREF2 == 2) { touch(lds0 + L_DUMMY + 256 * lw, l2a[0]); touch(lds0 + L_DUMMY + 256 * lw, l2a[1]); }
        bar();
        bar();
        if (PREF2 == 3) { touch(lds0 + L_DUMMY + 256 * lw, l2a[0]); touch(lds0 + L_DUMMY + 256 * lw, l2a[1]); }
        __builtin_amdgcn_s_setprio(LPRIO2);
        wait_flags((const GAS unsigned*)(ws + WS_FH) + T * 32, tag);
        STAMP(3);
        dmaA(ag, 0); dmaA(ag + 8192, P2_SLOT);
        if (WARMA == 1)
            touch(lds0 + L_DUMMY + 256 * lw, ag + (128 + 156 * (cj & 7) + 39 * lw + min(lane, 38)) * 128);
        dmaA(ag + 2 * 8192, 2 * P2_SLOT); dmaA(ag + 3 * 8192, 3 * P2_SLOT);
        wait_vm<4 + (WARMA == 1)>(); bar();
        STAMP(4);
        dmaA(ag + 4 * 8192, 4 * P2_SLOT); dmaA(ag + 5 * 8192, 5 * P2_SLOT); dmaA(ag + 6 * 8192, 6 * P2_SLOT); dmaA(ag + 7 * 8192, 7 * P2_SLOT);
        wait_vm<8>(); bar();
        const GAS char* ap = ag + 9 * 8192;
        const GAS char* wp = w2g + 9 * 65536;
        unsigned slot = 0;
        auto adv = [&]() { ap += 8192; slot = slot == P2_RING - P2_SLOT ? 0 : slot + P2_SLOT; };
        auto stW = [&]() { dmaA(ap, slot); dmaW(wp, slot); wp += 65536; adv(); };
        auto stC = [&]() { dmaA(ap, slot); dmaC(wp, slot); wp += 128; adv(); };
        dmaA(ag + 8 * 8192, 8 * P2_SLOT); stW();
        wait_vm<10>(); bar(); stW(); stW();
        wait_vm<14>(); bar(); stW(); stW();
#pragma unroll 1
        for (int j = 4; j < 7; ++j) { wait_vm<16>(); bar(); stW(); stW(); }
        wait_vm<16>(); bar(); stW();
        ap -= 4096; wp -= 32768;
        stW();
        wp = (const GAS char*)cre - 3072;
        wait_vm<16>(); bar();
        if (WARMA) touch(lds0 + L_DUMMY + 256 * lw, ag + (FF + 128 * (cj & 7) + 32 * lw + (lane & 31)) * 128);
        stC(); stC();
#pragma unroll 1
        for (int j = 9; j < 12; ++j) { wait_vm<16>(); bar(); stC(); stC(); }
        wp = (const GAS char*)cim - 3072;
#pragma unroll 1
        for (int j = 12; j < 16; ++j) { wait_vm<16>(); bar(); stC(); stC(); }
        wait_vm<16>(); bar();
        wait_vm<8>(); bar();
        wait_vm<0>(); bar();
        STAMP(5);
        return;
    }

    const int wm = wave >> 1, wn = wave & 1;
    const int p = p0 + 16 * wn + li;
    cf lam = {0.0f, 0.0f}, l8 = lam, l16 = lam, l128 = lam, lsig = lam, coef2 = lam;
    float b1c[3], p_lr, p_lim, p_ldt;
    unsigned tag;
    {
        u32x4 xr[2];
        unsigned short pr[6];
#pragma unroll
        for (int i = 0; i < 2; ++i) xr[i] = *(const u32x4*)(x + (t0 + 8 * cj + 2 * wave + i) * DM + lane * 8);
        pr[0] = lam_re[p]; pr[1] = lam_im[p]; pr[2] = log_dt[p];
#pragma unroll
        for (int c = 0; c < 3; ++c) pr[3 + c] = b1[min(f0 + 16 * (3 * wn + c) + li, FF - 1)];
        SB0;
        p_lr = bf2f(pr[0]); p_lim = bf2f(pr[1]); p_ldt = bf2f(pr[2]);
#pragma unroll
        for (int c = 0; c < 3; ++c) b1c[c] = bf2f(pr[3 + c]);
        float mean[2], var[2];
#pragma unroll
        for (int i = 0; i < 2; ++i) {
            float e[8], s = 0.0f;
#pragma unroll
            for (int j = 0; j < 4; ++j) { e[2 * j] = bf_lo(xr[i][j]); e[2 * j + 1] = bf_hi(xr[i][j]); s += e[2 * j] + e[2 * j + 1]; }
            mean[i] = wave_sum(s) * (1.0f / DM);
            float qq = 0.0f;
#pragma unroll
            for (int j = 0; j < 8; ++j) qq = fmaf(e[j] - mean[i], e[j] - mean[i], qq);
            var[i] = wave_sum(qq) * (1.0f / DM);
        }
        if (lane < 2) ((LAS f32x2*)(smem + L_MAIL))[2 * wave + lane] = f32x2{lane ? mean[1] : mean[0], __uint_as_float(row_std(lane ? var[1] : var[0]))};
        asm volatile("" : "+v"(b1c[0]), "+v"(b1c[1]), "+v"(b1c[2]), "+v"(p_lr), "+v"(p_lim), "+v"(p_ldt));
    }
    auto lam_setup = [&]() {
        const float lr = p_lr, lim = p_lim, dt = rbf(exp_f(p_ldt));
        const float mag = exp_f(dt * lr);
        float sn, cs;
        sincos_f(dt * lim, sn, cs);
        lam = cf{mag * cs, mag * sn};
        const float nr = lam.r - 1.0f, ni = lam.i, den = 1.0f / (lr * lr + lim * lim);
        coef2 = cf{2.0f * (nr * lr + ni * lim) * den, 2.0f * (ni * lr - nr * lim) * den};
        const cf l2 = cmul(lam, lam), l4 = cmul(l2, l2);
        l8 = cmul(l4, l4); l16 = cmul(l8, l8);
        const cf l32 = cmul(l16, l16), l64 = cmul(l32, l32);
        l128 = cmul(l64, l64);
        lsig = (g & 1) ? l16 : cf{1.0f, 0.0f};
        if (g & 2) lsig = cmul(lsig, l32);
        if (wm) lsig = cmul(lsig, l64);
        asm volatile("" : "+v"(lam.r), "+v"(lam.i), "+v"(l8.r), "+v"(l8.i), "+v"(l16.r), "+v"(l16.i), "+v"(l128.r), "+v"(l128.i), "+v"(lsig.r), "+v"(lsig.i),
                          "+v"(coef2.r), "+v"(coef2.i));
    };
    f32x4 acc[4][5];
    {
        const int sw = 16 * (g ^ (lane & 7));
        const int xa0 = P1_X + (64 * wm + li) * 128 + sw;
        const int ba0 = P1_B + (16 * wn + li) * 128 + sw;
        int wa[2][2];
#pragma unroll
        for (int t = 0; t < 2; ++t)
#pragma unroll
            for (int cl = 0; cl < 2; ++cl) {
                const int par = (3 * wn + cl) & 1;
                wa[t][cl] = P1_W + 1536 * g + 64 * q + 256 * t + 32 * (par ^ (g & 1)) + 16 * ((pp >> 1) ^ t) + 8 * (pp & 1) + (cl == 0 ? 512 * wn : 1024 * wn);
            }
#pragma unroll
        for (int b = 0; b < 4; ++b)
#pragma unroll
            for (int c = 0; c < 5; ++c) acc[b][c] = f32x4{0, 0, 0, 0};
        s16x8 xa[2][4], wb[2][5];
        auto rd = [&](int slot, int ks, int buf) {
            LAS char* s = smem + slot * P1_SLOT;
            LAS char* xs = s + (xa0 ^ (64 * ks));
            LAS char* bs = s + (ba0 ^ (64 * ks));
#pragma unroll
            for (int b = 0; b < 4; ++b) xa[buf][b] = lds_row(xs + b * 2048);
            wb[buf][0] = lds_row(bs);
            wb[buf][1] = lds_row(bs + 4096);
            wb[buf][2] = lds_tr(s + wa[0][0] + 6144 * ks, s + wa[1][0] + 6144 * ks);
            wb[buf][3] = lds_tr(s + wa[0][1] + 6144 * ks, s + wa[1][1] + 6144 * ks);
            wb[buf][4] = lds_tr(s + wa[0][0] + 6144 * ks + 512, s + wa[1][0] + 6144 * ks + 512);
        };
        auto mm = [&](int buf) {
#pragma unroll
            for (int c = 0; c < 5; ++c)
#pragma unroll
                for (int b = 0; b < 4; ++b) acc[b][c] = mfma(xa[buf][b], wb[buf][c], acc[b][c]);
        };
        lam_setup();
        auto colsum = [&](int buf) {
            const s16x8 ones = {0x3f80, 0x3f80, 0x3f80, 0x3f80, 0x3f80, 0x3f80, 0x3f80, 0x3f80};
            float v5[5];
#pragma unroll
            for (int c = 0; c < 5; ++c) v5[c] = mfma(ones, wb[buf][c], f32x4{0, 0, 0, 0})[0];
            if (g == 0) {
#pragma unroll
                for (int c = 0; c < 5; ++c) ((LAS float*)(smem + L_MAIL + 64))[(wn * 5 + c) * 16 + li] = v5[c];
            }
        };
        asm volatile("s_waitcnt vmcnt(0) lgkmcnt(0)" : "+v"(epoch)::"memory");
        tag = __builtin_amdgcn_readfirstlane(epoch) + 1u;
        asm volatile("" : "+s"(tag));
        bar();
        STAMP(1);
        rd(0, 0, 0);
        wait_lgkm0();
#pragma unroll 1
        for (int seg = 0, t = 0; seg < 2; ++seg) {
            const int tend = seg ? 7 : 1;
#pragma unroll 1
            for (; t < tend; ++t) {
                SB0;
                rd(t & 3, 1, 1); mm(0); interleave<20, 12>();
                SB0; wait_lgkm0(); bar();
                rd((t + 1) & 3, 0, 0); mm(1); interleave<20, 12>();
                SB0; wait_lgkm0();
            }
            if (seg == 0 && wm == 0) { if (ci & 1) colsum(1); else colsum(0); }
        }
        rd(3, 1, 1); mm(0); interleave<20, 12>();
        SB0; wait_lgkm0(); bar();
        mm(1);
    }
    STAMP(2);

    LAS f32x4* tab1 = (LAS f32x4*)(smem + L_TAB1) + (wm * 4 + g) * 8;
    LAS f32x4* tab2 = (LAS f32x4*)(smem + L_TAB2) + (wm * 4 + g) * 8;
    auto csum = [&](int k) { return ((LAS float*)(smem + L_CS))[(wn * 5 + k) * 16 + li] + ((LAS float*)(smem + L_CS))[((2 + wn) * 5 + k) * 16 + li]; };
    u32x2 xv[2][2], dv[2], bv[2];
#pragma unroll
    for (int i = 0; i < 2; ++i) {
        const int d = d0 + 32 * (wave >> 1) + 16 * i + 4 * g;
        dv[i] = *(const u32x2*)(dskip + d); bv[i] = *(const u32x2*)(b2 + d);
#pragma unroll
        for (int j = 0; j < 2; ++j) xv[i][j] = *(const u32x2*)(x + (64 * T + 32 * j + 16 * ((lane >> 3) & 1) + 8 * (wave & 1) + (lane & 7)) * DM + d);
    }
    const int Tw = 2 * ci + wm;
    const __amdgpu_buffer_rsrc_t act = __builtin_amdgcn_make_buffer_rsrc((void*)(ws_ + WS_ACT), 0, 32 * ACT_TILE, 0x00020000);
    const unsigned act0 = Tw * ACT_TILE;
    {
        f32x4 tb[8];
#pragma unroll
        for (int j = 0; j < 8; ++j) tb[j] = tab2[j];
        constexpr float C1 = -2.30220819f;
        constexpr float C2 = C1 * 0.044715f;
#pragma unroll
        for (int c = 0; c < 3; ++c) {
            const int col = 16 * (3 * wn + c) + li;
            const float s1c = csum(2 + c);
            f32x2 y[8];
#pragma unroll
            for (int j = 0; j < 8; ++j) {
                const int b = j >> 1, r = 2 * (j & 1);
                const f32x2 a = {tb[j][0], tb[j][1]}, bb = {tb[j][2], tb[j][3]};
                const f32x2 z = pfma(a, f32x2{acc[b][2 + c][r], acc[b][2 + c][r + 1]}, pfma(bb, splat(s1c), splat(b1c[c])));
                const f32x2 u = z * pfma(z * z, splat(C2), splat(C1));
                const f32x2 d = f32x2{__builtin_amdgcn_exp2f(u[0]), __builtin_amdgcn_exp2f(u[1])} + splat(1.0f);
                y[j] = z * f32x2{__builtin_amdgcn_rcpf(d[0]), __builtin_amdgcn_rcpf(d[1])};
            }
            if (3 * wn + c < nfu) {
                const int k = f0 + col;
                const unsigned dst = act0 + (k >> 3) * 1024 + (k & 7) * 64 + 16 * g;
#pragma unroll
                for (int sgm = 0; sgm < 2; ++sgm)
                    __builtin_amdgcn_raw_buffer_store_b128(u32x4{pk_bf16(y[0][sgm], y[1][sgm]), pk_bf16(y[2][sgm], y[3][sgm]), pk_bf16(y[4][sgm], y[5][sgm]), pk_bf16(y[6][sgm], y[7][sgm])},
                                                           act, dst + 512 * sgm, 0, ST_AUX);
            }
        }
    }
    STAMP(3);

    f32x2 ure[8], uim[8];
    {
        const float sbr = csum(0), sbi = csum(1);
#pragma unroll
        for (int j = 0; j < 8; ++j) {
            const f32x4 tb = tab1[j];
            const f32x2 a = {tb[0], tb[1]}, bb = {tb[2], tb[3]};
            const int b = j >> 1, r = 2 * (j & 1);
            ure[j] = pfma(a, f32x2{acc[b][0][r], acc[b][0][r + 1]}, bb * splat(sbr));
            uim[j] = pfma(a, f32x2{acc[b][1][r], acc[b][1][r + 1]}, bb * splat(sbi));
        }
    }
    f32x2 hr = ure[0], hi = uim[0];
#pragma unroll
    for (int j = 1; j < 8; ++j) {
        const f32x2 tr = pfma(splat(lam.r), hr, ure[j]), ti = pfma(splat(lam.r), hi, uim[j]);
        const f32x2 nr = pfma(splat(-lam.i), hi, tr), ni = pfma(splat(lam.i), hr, ti);
        hr = nr; hi = ni;
    }
    const cf eA = cf{hr[0], hi[0]};
    const cf e = cfma(l8, eA, cf{hr[1], hi[1]});
    const int sig = 4 * wm + g;
    LAS f32x2* eL = (LAS f32x2*)(smem + L_EL);
    eL[sig * 32 + 16 * wn + li] = f32x2{e.r, e.i};
    wait_lgkm0();
    bar();
    cf Lp = cf{0.0f, 0.0f};
#pragma unroll
    for (int s = 0; s < 7; ++s) {
        const f32x2 v = eL[s * 32 + 16 * wn + li];
        const cf n = cfma(l16, Lp, cf{v[0], v[1]});
        if (s < sig) Lp = n;
    }
    wait_vm<0>();
    if (lane == 0) __hip_atomic_store((GAS unsigned*)(ws + WS_FH) + Tw * 32 + 2 * cj + wn, tag, __ATOMIC_RELAXED, __HIP_MEMORY_SCOPE_AGENT);
    GAS u32x4* eg = (GAS u32x4*)(ws + WS_EG);
    if (sig == 7) {
        const cf E = cfma(l16, Lp, e);
        store16_sc1(eg + ci * NP + p, u32x4{__float_as_uint(E.r), tag, __float_as_uint(E.i), tag});
    }
    bar();
    STAMP(4);

    const int wd = wave >> 1, wt = wave & 1;
    f32x4 o[2][2];
    cf Hin = cf{0.0f, 0.0f};
    {
#pragma unroll
        for (int j = 0; j < 2; ++j) {
            const f32x2 st = ((LAS f32x2*)(smem + L_TOK))[64 * (T & 1) + 32 * j + 16 * ((lane >> 3) & 1) + 8 * wt + (lane & 7)];
#pragma unroll
            for (int i = 0; i < 2; ++i) {
                const float xe[4] = {bf_lo(xv[i][j][0]), bf_hi(xv[i][j][0]), bf_lo(xv[i][j][1]), bf_hi(xv[i][j][1])};
                const float de[4] = {bf_lo(dv[i][0]), bf_hi(dv[i][0]), bf_lo(dv[i][1]), bf_hi(dv[i][1])};
                const float be[4] = {bf_lo(bv[i][0]), bf_hi(bv[i][0]), bf_lo(bv[i][1]), bf_hi(bv[i][1])};
#pragma unroll
                for (int k = 0; k < 4; ++k) {
                    const float xn = rbf(rbf(xe[k] - st[0]) * st[1]);
                    o[i][j][k] = xe[k] + rbf(xn * de[k]) + be[k];
                }
            }
        }
        asm volatile("" : "+v"(o[0][0]), "+v"(o[0][1]), "+v"(o[1][0]), "+v"(o[1][1]));
        const GAS u32x4* ep = eg + g * NP + p;
        u32x4 gr[4];
        auto fetch = [&]() {
            asm volatile("global_load_dwordx4 %0, %4, off sc1\n\tglobal_load_dwordx4 %1, %5, off sc1\n\t"
                         "global_load_dwordx4 %2, %6, off sc1\n\tglobal_load_dwordx4 %3, %7, off sc1"
                         : "=&v"(gr[0]), "=&v"(gr[1]), "=&v"(gr[2]), "=&v"(gr[3])
                         : "v"(ep), "v"(ep + 4 * NP), "v"(ep + 8 * NP), "v"(ep + 12 * NP) : "memory");
        };
        auto landed = [&]() { asm volatile("s_waitcnt vmcnt(0)" : "+v"(gr[0]), "+v"(gr[1]), "+v"(gr[2]), "+v"(gr[3])::"memory"); };
        auto complete = [&]() {
            bool ok = true;
#pragma unroll
            for (int i = 0; i < 4; ++i) ok &= (4 * i + g >= ci) | ((gr[i][1] == tag) & (gr[i][3] == tag));
            return __all(ok);
        };
        fetch();
        const cf l256 = cmul(l128, l128), l512 = cmul(l256, l256);
        landed();
#pragma unroll 1
        for (unsigned spins = 0; spins < SPIN_CAP && !complete(); ++spins) { __builtin_amdgcn_s_sleep(2); fetch(); landed(); }
        cf hp = cf{0.0f, 0.0f};
#pragma unroll
        for (int i = 0; i < 4; ++i) {
            const cf n = cfma(l512, hp, cf{__uint_as_float(gr[i][0]), __uint_as_float(gr[i][2])});
            if (4 * i + g < ci) hp = n;
        }
        const int rr = (ci - 1 - g) & 3;
        if (rr & 1) hp = cmul(hp, l128);
        if (rr & 2) hp = cmul(hp, l256);
        Hin = cf{sum_groups(hp.r), sum_groups(hp.i)};
    }
    STAMP(5);
    u32x4 hq[2][2];
    unsigned hdr, hdi;
    auto store_h = [&]() {
#pragma unroll
        for (int sgm = 0; sgm < 2; ++sgm) {
            __builtin_amdgcn_raw_buffer_store_b128(hq[0][sgm], act, hdr + 512 * sgm, 0, ST_AUX);
            __builtin_amdgcn_raw_buffer_store_b128(hq[1][sgm], act, hdi + 512 * sgm, 0, ST_AUX);
        }
    };
    {
        const cf cA = cfma(lsig, Hin, Lp);
        const cf cB = cfma(l8, cA, eA);
        f32x2 sr = {cA.r, cB.r}, si = {cA.i, cB.i};
        f32x2 ore[8], oim[8];
#pragma unroll
        for (int j = 0; j < 8; ++j) {
            const f32x2 tr = pfma(splat(lam.r), sr, ure[j]), ti = pfma(splat(lam.r), si, uim[j]);
            const f32x2 nr = pfma(splat(-lam.i), si, tr), ni = pfma(splat(lam.i), sr, ti);
            sr = nr; si = ni;
            ore[j] = pfma(splat(coef2.r), sr, splat(-coef2.i) * si);
            oim[j] = pfma(splat(-coef2.r), si, splat(-coef2.i) * sr);
        }
        const int kr = FF + p, kim = FF + NP + p;
        hdr = act0 + (kr >> 3) * 1024 + (kr & 7) * 64 + 16 * g;
        hdi = act0 + (kim >> 3) * 1024 + (kim & 7) * 64 + 16 * g;
#pragma unroll
        for (int sgm = 0; sgm < 2; ++sgm) {
            hq[0][sgm] = u32x4{pk_bf16(ore[0][sgm], ore[1][sgm]), pk_bf16(ore[2][sgm], ore[3][sgm]), pk_bf16(ore[4][sgm], ore[5][sgm]), pk_bf16(ore[6][sgm], ore[7][sgm])};
            hq[1][sgm] = u32x4{pk_bf16(oim[0][sgm], oim[1][sgm]), pk_bf16(oim[2][sgm], oim[3][sgm]), pk_bf16(oim[4][sgm], oim[5][sgm]), pk_bf16(oim[6][sgm], oim[7][sgm])};
        }
        if (!DEFER) store_h();
    }
    STAMP(6);

    {
        const int trb = (g >> 1) * 2080 + (g & 1) * 1056 + 64 * q + 16 * (pp >> 1) + 8 * (pp & 1);
        const int tra = trb + 512 * wt, trw = P2_IMG + trb + 512 * wd;
        const int ca0 = P2_IMG + (32 * wd + li) * 128 + 16 * (g ^ (lane & 7));
        s16x8 wf[2][2][2], af[2][2][2];
        int slot = 0;
        auto next = [&]() { slot = slot == P2_RING - P2_SLOT ? 0 : slot + P2_SLOT; };
        auto frag_a = [&](LAS char* sl, int buf, int ks, int i) { LAS char* a = sl + tra + 4160 * ks + 32 * i; af[buf][ks][i] = lds_tr(a, a + 256); };
        auto frag_w = [&](LAS char* sl, int buf, int ks, int i, bool g4) {
            if (g4) { LAS char* a = sl + trw + 4160 * ks + 32 * i; wf[buf][ks][i] = lds_tr(a, a + 256); }
            else wf[buf][ks][i] = lds_row(sl + ((ca0 ^ (64 * ks)) + 2048 * i));
        };
        auto half = [&](int rb, bool rd, bool g4, int k0, bool mm_) {
            LAS char* sl = smem + slot;
            const int ub = rb ^ 1;
#pragma unroll
            for (int ks = 0; ks < 2; ++ks) {
#pragma unroll
                for (int m = 0; m < 4; ++m) {
                    if (mm_ && ks >= k0) mfma_acc(o[m >> 1][m & 1], wf[ub][ks][m >> 1], af[ub][ks][m & 1]);
                    SB0;
                    if (rd) { if (m < 2) frag_a(sl, rb, ks, m); else frag_w(sl, rb, ks, m - 2, g4); }
                    SB0;
                }
            }
            if (rd) next();
        };
        const GAS unsigned* fsp = (const GAS unsigned*)(ws + WS_FS) + T * 32 + (lane & 31);
        const unsigned fsl = (unsigned)(size_t)smem + L_EL + 256 * wave;
        __builtin_amdgcn_s_setprio(CPRIO2);
        bar(); half(0, true, true, 0, false);
        STAMP(7);
        if (DEFER) store_h();
        half(1, true, true, 0, true);
#pragma unroll 1
        for (int seg = 0, j = 1; seg < 4; ++seg) {
            const int jend = seg == 0 ? 2 + DEFER : (seg == 1 ? 7 : (seg == 2 ? 8 : 10));
#pragma unroll 1
            for (; j < jend; ++j) {
                bar(); half(0, true, true, 0, true);
                half(1, true, true, 0, true);
            }
            if (seg == 0) {
                wait_vm<0>();
                if (lane == 0) __hip_atomic_store((GAS unsigned*)(ws + WS_FS) + Tw * 32 + 2 * cj + wn, tag, __ATOMIC_RELAXED, __HIP_MEMORY_SCOPE_AGENT);
            } else if (seg == 1) {
                asm volatile("s_mov_b32 m0, %0\n\ts_nop 0\n\tglobal_load_lds_dword %1, off sc1" ::"s"(fsl), "v"(fsp) : "memory");
            } else if (seg == 2) {
                unsigned fsv;
                asm volatile("s_waitcnt vmcnt(0)\n\tds_read_b32 %0, %1\n\ts_waitcnt lgkmcnt(0)" : "=&v"(fsv) : "v"(fsl + 4 * lane) : "memory");
                if (!__all(fsv == tag)) wait_flags((const GAS unsigned*)(ws + WS_FS) + T * 32, tag);
            }
            SB0;
        }
        bar(); half(0, true, true, 0, true);
        half(1, true, true, 0, true);
        bar(); half(0, true, false, 1, true);
        half(1, true, false, 0, true);
#pragma unroll 1
        for (int j = 12; j < 19; ++j) {
            bar(); half(0, true, false, 0, true);
            half(1, true, false, 0, true);
        }
        half(0, false, false, 0, true);
        asm volatile("s_nop 7\n\ts_nop 7" : "+v"(o[0][0]), "+v"(o[0][1]), "+v"(o[1][0]), "+v"(o[1][1]));
        STAMP(8);

#pragma unroll
        for (int j = 0; j < 2; ++j)
#pragma unroll
            for (int i = 0; i < 2; ++i)
                *(f32x4*)(out + (64 * T + 32 * j + 16 * ((lane >> 3) & 1) + 8 * wt + (lane & 7)) * DM + d0 + 32 * wd + 16 * i + 4 * g) = o[i][j];
        if (ci == 15 && cj == 0 && threadIdx.x == 0) __hip_atomic_store((GAS unsigned*)(ws + WS_EP), tag, __ATOMIC_RELAXED, __HIP_MEMORY_SCOPE_AGENT);
        STAMP(9);
    }
}

}

extern "C" void kernel_launch(void* const* d_in, const int* in_sizes, int n_in, void* d_out, int out_size, void* d_ws, size_t ws_size,
                              hipStream_t stream) {
    typedef const unsigned short* P;
    (void)hipFuncSetAttribute((const void*)k_fused, hipFuncAttributeMaxDynamicSharedMemorySize, LDS_BYTES);
    k_fused<<<256, 512, LDS_BYTES, stream>>>((P)d_in[0], (P)d_in[1], (P)d_in[2], (P)d_in[3], (P)d_in[4], (P)d_in[5], (P)d_in[6], (P)d_in[7],
                                             (P)d_in[8], (P)d_in[10], (P)d_in[11], (P)d_in[12], (P)d_in[13], (char*)d_ws, (float*)d_out);
}
```

```cpp
#include <hip/hip_runtime.h>
#include <hip/hip_bf16.h>
#include <stdint.h>
#include <type_traits>

#define LAS __attribute__((address_space(3)))
#define GAS __attribute__((address_space(1)))

typedef __attribute__((ext_vector_type(8))) short s16x8;
typedef __attribute__((ext_vector_type(4))) short s16x4;
typedef __attribute__((ext_vector_type(8))) __bf16 bf16x8;
typedef __attribute__((ext_vector_type(2))) __bf16 bf16x2;
typedef __attribute__((ext_vector_type(4))) float f32x4;
typedef __attribute__((ext_vector_type(2))) float f32x2;
typedef __attribute__((ext_vector_type(4))) unsigned u32x4;
typedef __attribute__((ext_vector_type(2))) unsigned u32x2;

#define ACT_AUX 0
#define ST_AUX 16
#define PREFETCH 1
#define WARM1 0
#define PRIO 0
#define WARMA 0
#define D4 4
#define D3 3
#define PREF2 0
#define DEFER 0
#define LPRIO2 0
#define CPRIO2 0
#define CODEW 2
#define CODE_LINES 200
#define LAMLATE 0
#define JIT 0
#define ACQ 0

namespace {

constexpr int SEQ = 2048, DM = 512, NP = 512, FF = 1376;
constexpr int KACT = FF + 2 * NP;
constexpr int ACT_TILE = KACT * 128;
constexpr unsigned WS_ACT = 0;
constexpr unsigned WS_RS = 10u << 20;
constexpr unsigned WS_EG = WS_RS + (64u << 10);
constexpr unsigned WS_FH = WS_EG + (256u << 10);
constexpr unsigned WS_FS = WS_FH + (8u << 10);
constexpr unsigned WS_CS = WS_FS + (8u << 10);
constexpr unsigned WS_EP = WS_CS + (256u << 10);
constexpr size_t WS_STAMP = 200u << 20;
constexpr float LN_EPS = 1e-5f;
constexpr unsigned SPIN_CAP = 1u << 18;

constexpr int P1_SLOT = 36 * 1024, P1_X = 0, P1_B = 16384, P1_W = 24576;
constexpr int P2_IMG = 4 * 2080, P2_SLOT = 2 * P2_IMG, P2_RING = 9 * P2_SLOT, P2_NST = 38;
constexpr int L_TAB1 = P2_RING;
constexpr int L_TAB2 = L_TAB1 + 1024;
constexpr int L_EL = L_TAB2 + 1024;
constexpr int L_CS = L_EL + 2048;
constexpr int L_TOK = L_CS + 1280;
constexpr int L_DUMMY = L_TOK + 1024;
constexpr int L_MAIL = L_DUMMY + 1024;
constexpr int LDS_BYTES = L_MAIL + 64 + 704;

#define STAMP(s) do {} while (0)

#define SB0 __builtin_amdgcn_sched_barrier(0)
template <int N> __device__ __forceinline__ void wait_vm() { asm volatile("s_waitcnt vmcnt(%0)" ::"i"(N) : "memory"); }
__device__ __forceinline__ void wait_lgkm0() { __builtin_amdgcn_s_waitcnt(0xC07F); asm volatile("" ::: "memory"); }
template <int NM, int ND, int DPM = 1, int I = 0> __device__ __forceinline__ void interleave() {
    if constexpr (I * DPM < ND && I < NM) {
        __builtin_amdgcn_sched_group_barrier(0x008, 1, 0);
        __builtin_amdgcn_sched_group_barrier(0x100, (ND - I * DPM) < DPM ? (ND - I * DPM) : DPM, 0);
        interleave<NM, ND, DPM, I + 1>();
    } else if constexpr (I < NM) {
        __builtin_amdgcn_sched_group_barrier(0x008, NM - I, 0);
    }
}
__device__ __forceinline__ void bar() {
    SB0;
    asm volatile("" ::: "memory");
    __builtin_amdgcn_s_barrier();
    asm volatile("" ::: "memory");
    SB0;
}
__device__ __forceinline__ float bf_lo(unsigned u) { return __uint_as_float(u << 16); }
__device__ __forceinline__ float bf_hi(unsigned u) { return __uint_as_float(u & 0xffff0000u); }
__device__ __forceinline__ float bf2f(unsigned short h) { return __uint_as_float((unsigned)h << 16); }
__device__ __forceinline__ unsigned pk_bf16(float a, float b) { bf16x2 v = {(__bf16)a, (__bf16)b}; return __builtin_bit_cast(unsigned, v); }
__device__ __forceinline__ float rbf(float a) { return bf_lo(pk_bf16(a, a)); }
__device__ __forceinline__ f32x2 pfma(f32x2 a, f32x2 b, f32x2 c) { return __builtin_elementwise_fma(a, b, c); }
__device__ __forceinline__ f32x2 splat(float a) { return f32x2{a, a}; }
__device__ __forceinline__ f32x4 mfma(s16x8 a, s16x8 b, f32x4 c) {
    return __builtin_amdgcn_mfma_f32_16x16x32_bf16(__builtin_bit_cast(bf16x8, a), __builtin_bit_cast(bf16x8, b), c, 0, 0, 0);
}
__device__ __forceinline__ void mfma_acc(f32x4& c, s16x8 a, s16x8 b) {
    asm volatile("v_mfma_f32_16x16x32_bf16 %0, %1, %2, %0" : "+v"(c) : "v"(a), "v"(b));
}
__device__ __forceinline__ s16x4 lds_tr1(LAS char* p) { return __builtin_amdgcn_ds_read_tr16_b64_v4i16((LAS s16x4*)p); }
__device__ __forceinline__ s16x8 cat(s16x4 a, s16x4 b) { return __builtin_shufflevector(a, b, 0, 1, 2, 3, 4, 5, 6, 7); }
__device__ __forceinline__ s16x8 lds_row(LAS char* p) { return *(LAS s16x8*)p; }
__device__ __forceinline__ s16x8 lds_tr(LAS char* p0, LAS char* p1) {
    s16x4 a = __builtin_amdgcn_ds_read_tr16_b64_v4i16((LAS s16x4*)p0);
    s16x4 b = __builtin_amdgcn_ds_read_tr16_b64_v4i16((LAS s16x4*)p1);
    return __builtin_shufflevector(a, b, 0, 1, 2, 3, 4, 5, 6, 7);
}
template <int OFF> __device__ __forceinline__ void dma1(unsigned lds, unsigned voff, const GAS char* base) {
    asm volatile("s_mov_b32 m0, %0\n\ts_nop 0\n\tglobal_load_lds_dwordx4 %1, %2 offset:%3" ::"s"(lds), "v"(voff), "s"(base), "i"(OFF) : "memory");
}
template <int OFF> __device__ __forceinline__ void dma2(unsigned lds, unsigned voff0, unsigned voff1, const GAS char* base) {
    asm volatile("s_mov_b32 m0, %0\n\ts_nop 0\n\tglobal_load_lds_dwordx4 %1, %3\n\tglobal_load_lds_dwordx4 %2, %3 offset:%4"
                 ::"s"(lds), "v"(voff0), "v"(voff1), "s"(base), "i"(OFF) : "memory");
}
__device__ __forceinline__ void touch(unsigned lds, const GAS char* a) {
    asm volatile("s_mov_b32 m0, %0\n\ts_nop 0\n\tglobal_load_lds_dword %1, off" ::"s"(lds), "v"(a) : "memory");
}
__device__ __forceinline__ void lds_st32(unsigned a, float v) { asm volatile("ds_write_b32 %0, %1" ::"v"(a), "v"(v) : "memory"); }
__device__ __forceinline__ void lds_st64(unsigned a, f32x2 v) { asm volatile("ds_write_b64 %0, %1" ::"v"(a), "v"(v) : "memory"); }
template <int CTRL, int RM> __device__ __forceinline__ float dpp_add(float v) {
    return v + __uint_as_float(__builtin_amdgcn_update_dpp(0u, __float_as_uint(v), CTRL, RM, 0xf, true));
}
__device__ __forceinline__ float wave_sum(float v) {
    v = dpp_add<0x111, 0xf>(v); v = dpp_add<0x112, 0xf>(v); v = dpp_add<0x114, 0xf>(v); v = dpp_add<0x118, 0xf>(v);
    v = dpp_add<0x142, 0xa>(v); v = dpp_add<0x143, 0xc>(v);
    return __uint_as_float(__builtin_amdgcn_readlane(__float_as_uint(v), 63));
}
__device__ __forceinline__ float sum_groups(float v) {
    const auto a = __builtin_amdgcn_permlane16_swap(__float_as_uint(v), __float_as_uint(v), false, false);
    v = __uint_as_float(a[0]) + __uint_as_float(a[1]);
    const auto b = __builtin_amdgcn_permlane32_swap(__float_as_uint(v), __float_as_uint(v), false, false);
    return __uint_as_float(b[0]) + __uint_as_float(b[1]);
}
__device__ __forceinline__ u32x4 load16_sc1(const GAS void* p) {
    u32x4 v;
    asm volatile("global_load_dwordx4 %0, %1, off sc1\n\ts_waitcnt vmcnt(0)" : "=&v"(v) : "v"(p) : "memory");
    return v;
}
__device__ __forceinline__ void store16_sc1(GAS void* p, u32x4 v) {
    asm volatile("global_store_dwordx4 %0, %1, off sc1\n\ts_nop 1" ::"v"(p), "v"(v) : "memory");
}
__device__ __forceinline__ void wait_flags(const GAS unsigned* f, unsigned tag) {
#pragma unroll 1
    for (unsigned spins = 0; spins < SPIN_CAP; ++spins) {
        const unsigned v = __hip_atomic_load(f + (threadIdx.x & 31), __ATOMIC_RELAXED, __HIP_MEMORY_SCOPE_AGENT);
        if (__all(v == tag)) break;
        __builtin_amdgcn_s_sleep(1);
    }
    asm volatile("" ::: "memory");
}
struct cf { float r, i; };
__device__ __forceinline__ cf cmul(cf a, cf b) { return cf{a.r * b.r - a.i * b.i, a.r * b.i + a.i * b.r}; }
__device__ __forceinline__ cf cfma(cf a, cf b, cf c) { return cf{fmaf(a.r, b.r, fmaf(-a.i, b.i, c.r)), fmaf(a.r, b.i, fmaf(a.i, b.r, c.i))}; }
__device__ __forceinline__ float exp_f(float x) {
    const float hi = x * 1.44269504f, lo = fmaf(x, 1.44269504f, -hi) + x * 1.92596299e-8f;
    return __builtin_amdgcn_exp2f(hi) * fmaf(lo, 0.693147181f, 1.0f);
}
__device__ __forceinline__ void sincos_f(float x, float& s, float& c) {
    const float k = rintf(x * 0.636619772f);
    float r = fmaf(k, -1.57079601e+00f, x);
    r = fmaf(k, -3.13916473e-07f, r);
    r = fmaf(k, -5.39030253e-15f, r);
    const float z = r * r;
    const float sp = fmaf(r * z, fmaf(z, fmaf(z, fmaf(z, 2.75573137e-06f, -1.98412698e-04f), 8.33333333e-03f), -1.66666667e-01f), r);
    const float cp = fmaf(z, fmaf(z, fmaf(z, fmaf(z, fmaf(z, -2.75573144e-07f, 2.48015873e-05f), -1.38888889e-03f), 4.16666667e-02f), -0.5f), 1.0f);
    const int q = (int)k;
    const float a = (q & 1) ? cp : sp, b = (q & 1) ? sp : cp;
    s = (q & 2) ? -a : a;
    c = ((q + 1) & 2) ? -b : b;
}
__device__ __forceinline__ unsigned row_std(float v) {
    const float s1 = rbf(__builtin_amdgcn_sqrtf(rbf(rbf(v) + LN_EPS)));
    const float r1 = __builtin_amdgcn_rcpf(s1);
    const float s2 = __builtin_amdgcn_sqrtf(rbf(rbf(v * r1 * r1) + LN_EPS));
    return pk_bf16(s1, s2);
}

typedef const unsigned short* __restrict__ bfp;

__global__ __launch_bounds__(512) void k_fused(bfp x, bfp lam_re, bfp lam_im, bfp bre, bfp bim, bfp cre, bfp cim, bfp log_dt, bfp dskip,
                                               bfp w1, bfp b1, bfp w2, bfp b2, char* ws_, float* __restrict__ out) {
    extern __shared__ __attribute__((aligned(16))) char smem_[];
    LAS char* smem = (LAS char*)smem_;
    GAS char* ws = (GAS char*)ws_;
    const int wave = __builtin_amdgcn_readfirstlane(threadIdx.x >> 6), lane = threadIdx.x & 63;
    const int g = lane >> 4, li = lane & 15;
    const int xg8 = blockIdx.x & 7, loc = blockIdx.x >> 3, ci = 4 * (xg8 >> 1) + (loc >> 3), cj = 8 * (xg8 & 1) + (loc & 7);
    const int t0 = 128 * ci, p0 = 32 * cj, f0 = cj < 11 ? 96 * cj : 1056 + 64 * (cj - 11), nfu = cj < 11 ? 6 : 4;
    const int T = 2 * ci + (cj >> 3), d0 = 64 * (cj & 7);
    const int rot = ((ci + 15) >> 1) & 7;
    STAMP(0);
    unsigned long long entry_pc;
    asm volatile("s_getpc_b64 %0" : "=s"(entry_pc));
    unsigned epoch;
    asm volatile("global_load_dword %0, %1, off sc1" : "=v"(epoch) : "v"(ws + WS_EP) : "memory");
    const int q = li >> 2, pp = lane & 3;

    if (__builtin_expect(wave >= 4, 1)) {
        const int lw = wave - 4;
        __builtin_amdgcn_s_setprio(PRIO);
        const unsigned lds0 = (unsigned)(size_t)smem;
        unsigned xo[4], bo[2], wo[3];
#pragma unroll
        for (int i = 0; i < 4; ++i) {
            const int R = 8 * (4 * lw + i) + (lane >> 3), ii = R & 15, bb = (R >> 4) & 3;
            const int tok = t0 + (R & 64) + 16 * (ii >> 2) + 8 * (ii & 1) + 2 * bb + ((ii >> 1) & 1);
            xo[i] = tok * 1024 + 16 * ((lane & 7) ^ (lane >> 3)) + 3072 - 1024 * i;
        }
#pragma unroll
        for (int i = 0; i < 2; ++i) {
            const int R = 8 * (2 * lw + i) + (lane >> 3);
            bo[i] = (p0 + (R & 31)) * 1024 + 16 * ((lane & 7) ^ (lane >> 3)) + 3072 - 1024 * i;
        }
#pragma unroll
        for (int i = 0; i < 3; ++i) {
            const int st = 2 * (3 * lw + i) + (lane >> 5), kg = st / 3, ns = st - 3 * kg, kr = (lane & 31) >> 2;
            const int ch = 4 * ns + ((lane & 3) ^ ((2 * kg + (kr >> 2)) & 3));
            const int f = min(f0 + 8 * ch, FF - 8);
            wo[i] = (8 * kg + kr) * (FF * 2) + f * 2 + 3072 - 1024 * i;
        }
        const GAS char* xg = (const GAS char*)x - 3072;
        const GAS char* bg = (const GAS char*)(lw < 2 ? bre : bim) - 3072;
        const GAS char* wg = (const GAS char*)w1 - 3072;
        const unsigned lx = lds0 + P1_X + lw * 4096, lb = lds0 + P1_B + lw * 2048, lwb = lds0 + P1_W + lw * 3072;
        auto piece = [&](int t, auto I) {
            constexpr int i = decltype(I)::value;
            const unsigned so = (t & 3) * P1_SLOT;
            const int kt = (t + rot) & 7;
            if constexpr (i < 4) dma1<1024 * i>(lx + so, xo[i], xg + kt * 128);
            else if constexpr (i < 6) dma1<1024 * (i - 4)>(lb + so, bo[i - 4], bg + kt * 128);
            else dma1<1024 * (i - 6)>(lwb + so, wo[i - 6], wg + (size_t)kt * (64 * FF * 2));
        };
#define PC(t, i) piece(t, std::integral_constant<int, i>{})
        auto issue = [&](int t) { PC(t, 0); PC(t, 1); PC(t, 2); PC(t, 3); PC(t, 4); PC(t, 5); PC(t, 6); PC(t, 7); PC(t, 8); };
        const GAS char* tbase;
        unsigned tstep;
        if (lw == 0) { tbase = (const GAS char*)x + (t0 + 16 * (cj & 7) + li) * 1024; tstep = 128; }
        else if (lw == 1) { const int r = 16 * (ci & 3) + li; tbase = (const GAS char*)(r < 32 ? bre : bim) + (p0 + (r & 31)) * 1024; tstep = 128; }
        else { tbase = (const GAS char*)w1 + (16 * (ci & 3) + li) * (FF * 2) + min(f0 * 2 + (lw == 2 ? 0 : 128), FF * 2 - 4); tstep = 64 * FF * 2; }
        auto warm = [&](int t) { if (WARM1) touch(lds0 + L_DUMMY + 256 * lw, tbase + ((t + rot) & 7) * tstep); };
        STAMP(6);
        if (CODEW == 1) {
            touch(lds0 + L_DUMMY + 256 * lw, (const GAS char*)(entry_pc & ~255ull) + min(((loc & 3) * 4 + lw) * 13 + min(lane, 12), CODE_LINES - 1) * 128);
        }
        issue(0); issue(1);
        warm(2 + (g & 1));
        if (CODEW == 2) {
            touch(lds0 + L_DUMMY + 256 * lw, (const GAS char*)(entry_pc & ~255ull) + min(((loc & 3) * 4 + lw) * 13 + min(lane, 12), CODE_LINES - 1) * 128);
        }
        STAMP(7);

        unsigned w2o[2], co[2], ao[2];
#pragma unroll
        for (int i = 0; i < 2; ++i) {
            const int kg = 2 * lw + i;
            w2o[i] = (8 * kg + ((lane & 31) >> 2)) * (DM * 2) + (d0 + 32 * (lane >> 5) + 8 * (lane & 3)) * 2 + 3072 - 1056 * i;
            co[i] = (d0 + 8 * kg + (lane >> 3)) * (NP * 2) + 16 * ((lane & 7) ^ (lane >> 3)) + 3072 - 1024 * i;
            ao[i] = kg * 1024 + lane * 16 + 3072 - 1056 * i;
        }
        const GAS char* ag = ws + WS_ACT + (size_t)T * ACT_TILE;
        const GAS char* w2g = (const GAS char*)w2 - 3072;
        const unsigned l2 = lds0 + lw * 2080, l2c = lds0 + P2_IMG + lw * 2048;
        auto dmaA = [&](const GAS char* src, unsigned slot) { dma2<1056>(l2 + slot, ao[0], ao[1], src - 3072); };
        auto dmaW = [&](const GAS char* src, unsigned slot) { dma2<1056>(l2 + slot + P2_IMG, w2o[0], w2o[1], src); };
        auto dmaC = [&](const GAS char* src, unsigned slot) { dma2<1024>(l2c + slot, co[0], co[1], src); };

        const GAS char* l2a[2];
#pragma unroll
        for (int i = 0; i < 2; ++i) {
            const int n = 456 * (ci & 3) + 114 * lw + min(64 * i + lane, 113);
            const int m = n - 800;
            l2a[i] = n < 800 ? (const GAS char*)w2 + (576 + n) * 1024 + d0 * 2
                             : (const GAS char*)(m < 512 ? cre : cim) + (d0 + ((m >> 3) & 63)) * 1024 + (m & 7) * 128;
        }
        const int n1 = 75 * blockIdx.x + 19 * lw + min(lane, 18);
        const GAS char* l1a = n1 < 11008 ? (const GAS char*)w2 + n1 * 128
                                         : (n1 < 15104 ? (const GAS char*)cre + (n1 - 11008) * 128 : (const GAS char*)cim + (min(n1, 19199) - 15104) * 128);
        const GAS u32x4* rsp = (const GAS u32x4*)(ws + WS_RS) + t0 + 64 * (lw & 1) + lane;
        const GAS u32x4* csp = (const GAS u32x4*)(ws + WS_CS) + ((cj * 16 + 8 * (lw >> 1) + 2 * g) * 2 + (lw & 1)) * 32 + li;
        asm volatile("" : "+v"(l2a[0]), "+v"(l2a[1]), "+v"(l1a), "+v"(rsp), "+v"(csp));
        asm volatile("s_waitcnt vmcnt(%1)" : "+v"(epoch) : "i"(9 + WARM1 + (CODEW == 2)) : "memory");
        unsigned tag = __builtin_amdgcn_readfirstlane(epoch) + 1u;
        asm volatile("" : "+s"(tag));
        bar();
        STAMP(1);
        issue(2); warm(4);
        if (PREFETCH) touch(lds0 + L_DUMMY + 256 * lw, l1a);
        wait_vm<9 + 2 * WARM1 + PREFETCH>(); bar();
        issue(3); warm(5);
        {
            f32x2 v;
            asm volatile("ds_read_b64 %0, %1\n\ts_waitcnt lgkmcnt(0)" : "=&v"(v) : "v"(lds0 + L_MAIL + 16 * lw + 8 * (lane & 1)) : "memory");
            if (lane < 2) store16_sc1((GAS u32x4*)(ws + WS_RS) + t0 + 8 * cj + 2 * lw + lane, u32x4{__float_as_uint(v[0]), tag, __float_as_uint(v[1]), tag});
        }
        wait_vm<10 + 2 * WARM1 + PREFETCH>(); bar();
        issue(4); warm(6);
        {
            float a, b, c;
            const unsigned ma = lds0 + L_MAIL + 64 + 4 * (((lw >> 1) * 5 + 3 * (lw & 1)) * 16 + li);
            asm volatile("ds_read_b32 %0, %3\n\tds_read_b32 %1, %3 offset:64\n\tds_read_b32 %2, %3 offset:128\n\ts_waitcnt lgkmcnt(0)"
                         : "=&v"(a), "=&v"(b), "=&v"(c) : "v"(ma) : "memory");
            if (g == 0) store16_sc1((GAS u32x4*)(ws + WS_CS) + ((cj * 16 + ci) * 2 + (lw >> 1)) * 32 + 16 * (lw & 1) + li,
                                    u32x4{__float_as_uint(a), __float_as_uint(b), __float_as_uint(c), tag});
        }
        wait_vm<11 + 2 * WARM1>(); bar();
        issue(5); warm(7); wait_vm<10 + 2 * WARM1>(); bar();
        issue(6); wait_vm<9 + WARM1>(); bar();
        issue(7);
        u32x4 rs, cs[4];
        auto gather = [&]() {
            asm volatile("global_load_dwordx4 %0, %5, off sc1\n\t"
                         "global_load_dwordx4 %1, %6, off sc1\n\tglobal_load_dwordx4 %2, %6, off offset:256 sc1\n\t"
                         "global_load_dwordx4 %3, %6, off offset:1024 sc1\n\tglobal_load_dwordx4 %4, %6, off offset:1280 sc1"
                         : "=&v"(rs), "=&v"(cs[0]), "=&v"(cs[1]), "=&v"(cs[2]), "=&v"(cs[3]) : "v"(rsp), "v"(csp) : "memory");
        };
        gather();
        wait_vm<14>(); bar();
        dmaW(w2g, 0); dmaW(w2g + 65536, P2_SLOT); dmaW(w2g + 2 * 65536, 2 * P2_SLOT); dmaW(w2g + 3 * 65536, 3 * P2_SLOT);
        if (PREF2 == 1) { touch(lds0 + L_DUMMY + 256 * lw, l2a[0]); touch(lds0 + L_DUMMY + 256 * lw, l2a[1]); }
        asm volatile("s_waitcnt vmcnt(%5)" : "+v"(rs), "+v"(cs[0]), "+v"(cs[1]), "+v"(cs[2]), "+v"(cs[3]) : "i"(PREF2 == 1 ? 10 : 8) : "memory");
        bar();
        dmaW(w2g + 4 * 65536, 4 * P2_SLOT); dmaW(w2g + 5 * 65536, 5 * P2_SLOT);
        auto fresh = [&]() {
            return __all(rs[1] == tag && rs[3] == tag && cs[0][3] == tag && cs[1][3] == tag && cs[2][3] == tag && cs[3][3] == tag);
        };
#pragma unroll 1
        for (unsigned spins = 0; spins < SPIN_CAP && !fresh(); ++spins) {
            gather();
            asm volatile("s_waitcnt vmcnt(0)" : "+v"(rs), "+v"(cs[0]), "+v"(cs[1]), "+v"(cs[2]), "+v"(cs[3])::"memory");
        }
        if (lw < 2) {
            const float m = __uint_as_float(rs[0]), mb = rbf(m);
            const float r1 = __builtin_amdgcn_rcpf(bf_lo(rs[2])), r12 = r1 * __builtin_amdgcn_rcpf(bf_hi(rs[2]));
            const int tau = 64 * lw + lane;
            const unsigned ta = lds0 + L_TAB1 + 4 * (((tau >> 4) * 8 + (tau & 7)) * 4 + ((tau >> 3) & 1));
            lds_st32(ta, r1); lds_st32(ta + 8, -r1 * mb);
            lds_st32(ta + 1024, r12); lds_st32(ta + 1032, -r12 * m);
            lds_st64(lds0 + L_TOK + 8 * tau, f32x2{mb, r1});
        }
        {
            float v5[5];
#pragma unroll
            for (int k = 0; k < 3; ++k) v5[k] = __uint_as_float(cs[0][k]) + __uint_as_float(cs[2][k]);
#pragma unroll
            for (int k = 0; k < 2; ++k) v5[3 + k] = __uint_as_float(cs[1][k]) + __uint_as_float(cs[3][k]);
#pragma unroll
            for (int k = 0; k < 5; ++k) v5[k] = sum_groups(v5[k]);
            if (g == 0) {
#pragma unroll
                for (int k = 0; k < 5; ++k) lds_st32(lds0 + L_CS + 4 * ((((lw >> 1) * 2 + (lw & 1)) * 5 + k) * 16 + li), v5[k]);
            }
        }
        asm volatile("s_waitcnt lgkmcnt(0)" ::: "memory");
        bar();
        dmaW(w2g + 6 * 65536, 6 * P2_SLOT); dmaW(w2g + 7 * 65536, 7 * P2_SLOT); dmaW(w2g + 8 * 65536, 8 * P2_SLOT);
        STAMP(2);
        if (PREF2 == 2) { touch(lds0 + L_DUMMY + 256 * lw, l2a[0]); touch(lds0 + L_DUMMY + 256 * lw, l2a[1]); }
        bar();
        bar();
        if (PREF2 == 3) { touch(lds0 + L_DUMMY + 256 * lw, l2a[0]); touch(lds0 + L_DUMMY + 256 * lw, l2a[1]); }
        __builtin_amdgcn_s_setprio(LPRIO2);
        wait_flags((const GAS unsigned*)(ws + WS_FH) + T * 32, tag);
        STAMP(3);
        constexpr int JX = JIT ? 1 : 0;
        const bool jw = (lane & 15) < 8;
        const int jn = 32 * (ci & 3) + 8 * lw + (lane & 7), ja = 16 * (cj & 7) + 4 * lw + (lane & 3);
        const GAS char* jp = jw ? (const GAS char*)w2 + (640 + jn) * 1024 + d0 * 2 : ag + (640 + ja) * 128;
        unsigned jstep = jw ? 131072 : 16384;
        auto jit = [&]() { if (JIT) { touch(lds0 + L_DUMMY + 256 * lw, jp); jp += jstep; } };
        dmaA(ag, 0); dmaA(ag + 8192, P2_SLOT); dmaA(ag + 2 * 8192, 2 * P2_SLOT); dmaA(ag + 3 * 8192, 3 * P2_SLOT);
        wait_vm<4>(); bar();
        STAMP(4);
        dmaA(ag + 4 * 8192, 4 * P2_SLOT); dmaA(ag + 5 * 8192, 5 * P2_SLOT); dmaA(ag + 6 * 8192, 6 * P2_SLOT); dmaA(ag + 7 * 8192, 7 * P2_SLOT);
        wait_vm<8>(); bar();
        const GAS char* ap = ag + 9 * 8192;
        const GAS char* wp = w2g + 9 * 65536;
        unsigned slot = 0;
        auto adv = [&]() { ap += 8192; slot = slot == P2_RING - P2_SLOT ? 0 : slot + P2_SLOT; };
        auto stW = [&]() { dmaA(ap, slot); dmaW(wp, slot); wp += 65536; adv(); };
        auto stC = [&]() { dmaA(ap, slot); dmaC(wp, slot); wp += 128; adv(); };
        jit(); dmaA(ag + 8 * 8192, 8 * P2_SLOT); stW();
        wait_vm<10 + JX>(); bar(); jit(); stW(); stW();
        wait_vm<14 + 2 * JX>(); bar(); jit(); stW(); stW();
#pragma unroll 1
        for (int j = 4; j < 6; ++j) { wait_vm<16 + 2 * JX>(); bar(); jit(); stW(); stW(); }
        jp = jw ? (const GAS char*)w2 + min(1280 + jn, FF - 1) * 1024 + d0 * 2 : ag + min(1280 + ja, FF - 1) * 128;
        wait_vm<16 + 2 * JX>(); bar(); jit(); stW(); stW();
        jp = (const GAS char*)cre + (d0 + (jn >> 1)) * 1024 + (jn & 1) * 128;
        jstep = 256;
        wait_vm<16 + 2 * JX>(); bar(); jit(); stW();
        ap -= 4096; wp -= 32768;
        stW();
        wp = (const GAS char*)cre - 3072;
        if (!jw) { jp = ag + (FF + 128 + ja) * 128; jstep = 16384; }
        wait_vm<16 + 2 * JX>(); bar();
        jit(); stC(); stC();
#pragma unroll 1
        for (int j = 9; j < 11; ++j) { wait_vm<16 + 2 * JX>(); bar(); jit(); stC(); stC(); }
        if (jw) jp = (const GAS char*)cim + (d0 + (jn >> 1)) * 1024 + (jn & 1) * 128;
        wait_vm<16 + 2 * JX>(); bar(); jit(); stC(); stC();
        wp = (const GAS char*)cim - 3072;
#pragma unroll 1
        for (int j = 12; j < 15; ++j) { wait_vm<16 + 2 * JX>(); bar(); jit(); stC(); stC(); }
        wait_vm<16 + 2 * JX>(); bar(); stC(); stC();
        wait_vm<16 + JX>(); bar();
        wait_vm<8>(); bar();
        wait_vm<0>(); bar();
        STAMP(5);
        return;
    }

    const int wm = wave >> 1, wn = wave & 1;
    const int p = p0 + 16 * wn + li;
    cf lam = {0.0f, 0.0f}, l8 = lam, l16 = lam, l128 = lam, lsig = lam, coef2 = lam;
    float b1c[3], p_lr, p_lim, p_ldt;
    unsigned tag;
    {
        u32x4 xr[2];
        unsigned short pr[6];
#pragma unroll
        for (int i = 0; i < 2; ++i) xr[i] = *(const u32x4*)(x + (t0 + 8 * cj + 2 * wave + i) * DM + lane * 8);
        pr[0] = lam_re[p]; pr[1] = lam_im[p]; pr[2] = log_dt[p];
#pragma unroll
        for (int c = 0; c < 3; ++c) pr[3 + c] = b1[min(f0 + 16 * (3 * wn + c) + li, FF - 1)];
        SB0;
        p_lr = bf2f(pr[0]); p_lim = bf2f(pr[1]); p_ldt = bf2f(pr[2]);
#pragma unroll
        for (int c = 0; c < 3; ++c) b1c[c] = bf2f(pr[3 + c]);
        float mean[2], var[2];
#pragma unroll
        for (int i = 0; i < 2; ++i) {
            float e[8], s = 0.0f;
#pragma unroll
            for (int j = 0; j < 4; ++j) { e[2 * j] = bf_lo(xr[i][j]); e[2 * j + 1] = bf_hi(xr[i][j]); s += e[2 * j] + e[2 * j + 1]; }
            mean[i] = wave_sum(s) * (1.0f / DM);
            float qq = 0.0f;
#pragma unroll
            for (int j = 0; j < 8; ++j) qq = fmaf(e[j] - mean[i], e[j] - mean[i], qq);
            var[i] = wave_sum(qq) * (1.0f / DM);
        }
        if (lane < 2) ((LAS f32x2*)(smem + L_MAIL))[2 * wave + lane] = f32x2{lane ? mean[1] : mean[0], __uint_as_float(row_std(lane ? var[1] : var[0]))};
        asm volatile("" : "+v"(b1c[0]), "+v"(b1c[1]), "+v"(b1c[2]), "+v"(p_lr), "+v"(p_lim), "+v"(p_ldt));
    }
    auto lam_setup = [&]() {
        const float lr = p_lr, lim = p_lim, dt = rbf(exp_f(p_ldt));
        const float mag = exp_f(dt * lr);
        float sn, cs;
        sincos_f(dt * lim, sn, cs);
        lam = cf{mag * cs, mag * sn};
        const float nr = lam.r - 1.0f, ni = lam.i, den = 1.0f / (lr * lr + lim * lim);
        coef2 = cf{2.0f * (nr * lr + ni * lim) * den, 2.0f * (ni * lr - nr * lim) * den};
        const cf l2 = cmul(lam, lam), l4 = cmul(l2, l2);
        l8 = cmul(l4, l4); l16 = cmul(l8, l8);
        const cf l32 = cmul(l16, l16), l64 = cmul(l32, l32);
        l128 = cmul(l64, l64);
        lsig = (g & 1) ? l16 : cf{1.0f, 0.0f};
        if (g & 2) lsig = cmul(lsig, l32);
        if (wm) lsig = cmul(lsig, l64);
        asm volatile("" : "+v"(lam.r), "+v"(lam.i), "+v"(l8.r), "+v"(l8.i), "+v"(l16.r), "+v"(l16.i), "+v"(l128.r), "+v"(l128.i), "+v"(lsig.r), "+v"(lsig.i),
                          "+v"(coef2.r), "+v"(coef2.i));
    };
    f32x4 acc[4][5];
    {
        const int sw = 16 * (g ^ (lane & 7));
        const int xa0 = P1_X + (64 * wm + li) * 128 + sw;
        const int ba0 = P1_B + (16 * wn + li) * 128 + sw;
        int wa[2][2];
#pragma unroll
        for (int t = 0; t < 2; ++t)
#pragma unroll
            for (int cl = 0; cl < 2; ++cl) {
                const int par = (3 * wn + cl) & 1;
                wa[t][cl] = P1_W + 1536 * g + 64 * q + 256 * t + 32 * (par ^ (g & 1)) + 16 * ((pp >> 1) ^ t) + 8 * (pp & 1) + (cl == 0 ? 512 * wn : 1024 * wn);
            }
#pragma unroll
        for (int b = 0; b < 4; ++b)
#pragma unroll
            for (int c = 0; c < 5; ++c) acc[b][c] = f32x4{0, 0, 0, 0};
        s16x8 xa[2][4], wb[2][5];
        auto rd = [&](int slot, int ks, int buf) {
            LAS char* s = smem + slot * P1_SLOT;
            LAS char* xs = s + (xa0 ^ (64 * ks));
            LAS char* bs = s + (ba0 ^ (64 * ks));
#pragma unroll
            for (int b = 0; b < 4; ++b) xa[buf][b] = lds_row(xs + b * 2048);
            wb[buf][0] = lds_row(bs);
            wb[buf][1] = lds_row(bs + 4096);
            wb[buf][2] = lds_tr(s + wa[0][0] + 6144 * ks, s + wa[1][0] + 6144 * ks);
            wb[buf][3] = lds_tr(s + wa[0][1] + 6144 * ks, s + wa[1][1] + 6144 * ks);
            wb[buf][4] = lds_tr(s + wa[0][0] + 6144 * ks + 512, s + wa[1][0] + 6144 * ks + 512);
        };
        auto mm = [&](int buf) {
#pragma unroll
            for (int c = 0; c < 5; ++c)
#pragma unroll
                for (int b = 0; b < 4; ++b) acc[b][c] = mfma(xa[buf][b], wb[buf][c], acc[b][c]);
        };
        lam_setup();
        auto colsum = [&](int buf) {
            const s16x8 ones = {0x3f80, 0x3f80, 0x3f80, 0x3f80, 0x3f80, 0x3f80, 0x3f80, 0x3f80};
            float v5[5];
#pragma unroll
            for (int c = 0; c < 5; ++c) v5[c] = mfma(ones, wb[buf][c], f32x4{0, 0, 0, 0})[0];
            if (g == 0) {
#pragma unroll
                for (int c = 0; c < 5; ++c) ((LAS float*)(smem + L_MAIL + 64))[(wn * 5 + c) * 16 + li] = v5[c];
            }
        };
        asm volatile("s_waitcnt vmcnt(0) lgkmcnt(0)" : "+v"(epoch)::"memory");
        tag = __builtin_amdgcn_readfirstlane(epoch) + 1u;
        asm volatile("" : "+s"(tag));
        bar();
        STAMP(1);
        rd(0, 0, 0);
        wait_lgkm0();
#pragma unroll 1
        for (int seg = 0, t = 0; seg < 2; ++seg) {
            const int tend = seg ? 7 : 1;
#pragma unroll 1
            for (; t < tend; ++t) {
                SB0;
                rd(t & 3, 1, 1); mm(0); interleave<20, 12>();
                SB0; wait_lgkm0(); bar();
                rd((t + 1) & 3, 0, 0); mm(1); interleave<20, 12>();
                SB0; wait_lgkm0();
            }
            if (seg == 0 && wm == 0) { if (ci & 1) colsum(1); else colsum(0); }
        }
        rd(3, 1, 1); mm(0); interleave<20, 12>();
        SB0; wait_lgkm0(); bar();
        mm(1);
    }
    STAMP(2);

    LAS f32x4* tab1 = (LAS f32x4*)(smem + L_TAB1) + (wm * 4 + g) * 8;
    LAS f32x4* tab2 = (LAS f32x4*)(smem + L_TAB2) + (wm * 4 + g) * 8;
    auto csum = [&](int k) { return ((LAS float*)(smem + L_CS))[(wn * 5 + k) * 16 + li] + ((LAS float*)(smem + L_CS))[((2 + wn) * 5 + k) * 16 + li]; };
    u32x2 xv[2][2], dv[2], bv[2];
#pragma unroll
    for (int i = 0; i < 2; ++i) {
        const int d = d0 + 32 * (wave >> 1) + 16 * i + 4 * g;
        dv[i] = *(const u32x2*)(dskip + d); bv[i] = *(const u32x2*)(b2 + d);
#pragma unroll
        for (int j = 0; j < 2; ++j) xv[i][j] = *(const u32x2*)(x + (64 * T + 32 * j + 16 * ((lane >> 3) & 1) + 8 * (wave & 1) + (lane & 7)) * DM + d);
    }
    const int Tw = 2 * ci + wm;
    const __amdgpu_buffer_rsrc_t act = __builtin_amdgcn_make_buffer_rsrc((void*)(ws_ + WS_ACT), 0, 32 * ACT_TILE, 0x00020000);
    const unsigned act0 = Tw * ACT_TILE;
    {
        f32x4 tb[8];
#pragma unroll
        for (int j = 0; j < 8; ++j) tb[j] = tab2[j];
        constexpr float C1 = -2.30220819f;
        constexpr float C2 = C1 * 0.044715f;
#pragma unroll
        for (int c = 0; c < 3; ++c) {
            const int col = 16 * (3 * wn + c) + li;
            const float s1c = csum(2 + c);
            f32x2 y[8];
#pragma unroll
            for (int j = 0; j < 8; ++j) {
                const int b = j >> 1, r = 2 * (j & 1);
                const f32x2 a = {tb[j][0], tb[j][1]}, bb = {tb[j][2], tb[j][3]};
                const f32x2 z = pfma(a, f32x2{acc[b][2 + c][r], acc[b][2 + c][r + 1]}, pfma(bb, splat(s1c), splat(b1c[c])));
                const f32x2 u = z * pfma(z * z, splat(C2), splat(C1));
                const f32x2 d = f32x2{__builtin_amdgcn_exp2f(u[0]), __builtin_amdgcn_exp2f(u[1])} + splat(1.0f);
                y[j] = z * f32x2{__builtin_amdgcn_rcpf(d[0]), __builtin_amdgcn_rcpf(d[1])};
            }
            if (3 * wn + c < nfu) {
                const int k = f0 + col;
                const unsigned dst = act0 + (k >> 3) * 1024 + (k & 7) * 64 + 16 * g;
#pragma unroll
                for (int sgm = 0; sgm < 2; ++sgm)
                    __builtin_amdgcn_raw_buffer_store_b128(u32x4{pk_bf16(y[0][sgm], y[1][sgm]), pk_bf16(y[2][sgm], y[3][sgm]), pk_bf16(y[4][sgm], y[5][sgm]), pk_bf16(y[6][sgm], y[7][sgm])},
                                                           act, dst + 512 * sgm, 0, ST_AUX);
            }
        }
    }
    STAMP(3);

    f32x2 ure[8], uim[8];
    {
        const float sbr = csum(0), sbi = csum(1);
#pragma unroll
        for (int j = 0; j < 8; ++j) {
            const f32x4 tb = tab1[j];
            const f32x2 a = {tb[0], tb[1]}, bb = {tb[2], tb[3]};
            const int b = j >> 1, r = 2 * (j & 1);
            ure[j] = pfma(a, f32x2{acc[b][0][r], acc[b][0][r + 1]}, bb * splat(sbr));
            uim[j] = pfma(a, f32x2{acc[b][1][r], acc[b][1][r + 1]}, bb * splat(sbi));
        }
    }
    f32x2 hr = ure[0], hi = uim[0];
#pragma unroll
    for (int j = 1; j < 8; ++j) {
        const f32x2 tr = pfma(splat(lam.r), hr, ure[j]), ti = pfma(splat(lam.r), hi, uim[j]);
        const f32x2 nr = pfma(splat(-lam.i), hi, tr), ni = pfma(splat(lam.i), hr, ti);
        hr = nr; hi = ni;
    }
    const cf eA = cf{hr[0], hi[0]};
    const cf e = cfma(l8, eA, cf{hr[1], hi[1]});
    const int sig = 4 * wm + g;
    LAS f32x2* eL = (LAS f32x2*)(smem + L_EL);
    eL[sig * 32 + 16 * wn + li] = f32x2{e.r, e.i};
    wait_lgkm0();
    bar();
    cf Lp = cf{0.0f, 0.0f};
#pragma unroll
    for (int s = 0; s < 7; ++s) {
        const f32x2 v = eL[s * 32 + 16 * wn + li];
        const cf n = cfma(l16, Lp, cf{v[0], v[1]});
        if (s < sig) Lp = n;
    }
    wait_vm<0>();
    if (lane == 0) __hip_atomic_store((GAS unsigned*)(ws + WS_FH) + Tw * 32 + 2 * cj + wn, tag, __ATOMIC_RELAXED, __HIP_MEMORY_SCOPE_AGENT);
    GAS u32x4* eg = (GAS u32x4*)(ws + WS_EG);
    if (sig == 7) {
        const cf E = cfma(l16, Lp, e);
        store16_sc1(eg + ci * NP + p, u32x4{__float_as_uint(E.r), tag, __float_as_uint(E.i), tag});
    }
    bar();
    STAMP(4);

    const int wd = wave >> 1, wt = wave & 1;
    f32x4 o[2][2];
    cf Hin = cf{0.0f, 0.0f};
    {
#pragma unroll
        for (int j = 0; j < 2; ++j) {
            const f32x2 st = ((LAS f32x2*)(smem + L_TOK))[64 * (T & 1) + 32 * j + 16 * ((lane >> 3) & 1) + 8 * wt + (lane & 7)];
#pragma unroll
            for (int i = 0; i < 2; ++i) {
                const float xe[4] = {bf_lo(xv[i][j][0]), bf_hi(xv[i][j][0]), bf_lo(xv[i][j][1]), bf_hi(xv[i][j][1])};
                const float de[4] = {bf_lo(dv[i][0]), bf_hi(dv[i][0]), bf_lo(dv[i][1]), bf_hi(dv[i][1])};
                const float be[4] = {bf_lo(bv[i][0]), bf_hi(bv[i][0]), bf_lo(bv[i][1]), bf_hi(bv[i][1])};
#pragma unroll
                for (int k = 0; k < 4; ++k) {
                    const float xn = rbf(rbf(xe[k] - st[0]) * st[1]);
                    o[i][j][k] = xe[k] + rbf(xn * de[k]) + be[k];
                }
            }
        }
        asm volatile("" : "+v"(o[0][0]), "+v"(o[0][1]), "+v"(o[1][0]), "+v"(o[1][1]));
        const GAS u32x4* ep = eg + g * NP + p;
        u32x4 gr[4];
        auto fetch = [&]() {
            asm volatile("global_load_dwordx4 %0, %4, off sc1\n\tglobal_load_dwordx4 %1, %5, off sc1\n\t"
                         "global_load_dwordx4 %2, %6, off sc1\n\tglobal_load_dwordx4 %3, %7, off sc1"
                         : "=&v"(gr[0]), "=&v"(gr[1]), "=&v"(gr[2]), "=&v"(gr[3])
                         : "v"(ep), "v"(ep + 4 * NP), "v"(ep + 8 * NP), "v"(ep + 12 * NP) : "memory");
        };
        auto landed = [&]() { asm volatile("s_waitcnt vmcnt(0)" : "+v"(gr[0]), "+v"(gr[1]), "+v"(gr[2]), "+v"(gr[3])::"memory"); };
        auto complete = [&]() {
            bool ok = true;
#pragma unroll
            for (int i = 0; i < 4; ++i) ok &= (4 * i + g >= ci) | ((gr[i][1] == tag) & (gr[i][3] == tag));
            return __all(ok);
        };
        fetch();
        const cf l256 = cmul(l128, l128), l512 = cmul(l256, l256);
        landed();
#pragma unroll 1
        for (unsigned spins = 0; spins < SPIN_CAP && !complete(); ++spins) { __builtin_amdgcn_s_sleep(2); fetch(); landed(); }
        cf hp = cf{0.0f, 0.0f};
#pragma unroll
        for (int i = 0; i < 4; ++i) {
            const cf n = cfma(l512, hp, cf{__uint_as_float(gr[i][0]), __uint_as_float(gr[i][2])});
            if (4 * i + g < ci) hp = n;
        }
        const int rr = (ci - 1 - g) & 3;
        if (rr & 1) hp = cmul(hp, l128);
        if (rr & 2) hp = cmul(hp, l256);
        Hin = cf{sum_groups(hp.r), sum_groups(hp.i)};
    }
    STAMP(5);
    u32x4 hq[2][2];
    unsigned hdr, hdi;
    auto store_h = [&]() {
#pragma unroll
        for (int sgm = 0; sgm < 2; ++sgm) {
            __builtin_amdgcn_raw_buffer_store_b128(hq[0][sgm], act, hdr + 512 * sgm, 0, ST_AUX);
            __builtin_amdgcn_raw_buffer_store_b128(hq[1][sgm], act, hdi + 512 * sgm, 0, ST_AUX);
        }
    };
    {
        const cf cA = cfma(lsig, Hin, Lp);
        const cf cB = cfma(l8, cA, eA);
        f32x2 sr = {cA.r, cB.r}, si = {cA.i, cB.i};
        f32x2 ore[8], oim[8];
#pragma unroll
        for (int j = 0; j < 8; ++j) {
            const f32x2 tr = pfma(splat(lam.r), sr, ure[j]), ti = pfma(splat(lam.r), si, uim[j]);
            const f32x2 nr = pfma(splat(-lam.i), si, tr), ni = pfma(splat(lam.i), sr, ti);
            sr = nr; si = ni;
            ore[j] = pfma(splat(coef2.r), sr, splat(-coef2.i) * si);
            oim[j] = pfma(splat(-coef2.r), si, splat(-coef2.i) * sr);
        }
        const int kr = FF + p, kim = FF + NP + p;
        hdr = act0 + (kr >> 3) * 1024 + (kr & 7) * 64 + 16 * g;
        hdi = act0 + (kim >> 3) * 1024 + (kim & 7) * 64 + 16 * g;
#pragma unroll
        for (int sgm = 0; sgm < 2; ++sgm) {
            hq[0][sgm] = u32x4{pk_bf16(ore[0][sgm], ore[1][sgm]), pk_bf16(ore[2][sgm], ore[3][sgm]), pk_bf16(ore[4][sgm], ore[5][sgm]), pk_bf16(ore[6][sgm], ore[7][sgm])};
            hq[1][sgm] = u32x4{pk_bf16(oim[0][sgm], oim[1][sgm]), pk_bf16(oim[2][sgm], oim[3][sgm]), pk_bf16(oim[4][sgm], oim[5][sgm]), pk_bf16(oim[6][sgm], oim[7][sgm])};
        }
        if (!DEFER) store_h();
    }
    STAMP(6);

    {
        const int trb = (g >> 1) * 2080 + (g & 1) * 1056 + 64 * q + 16 * (pp >> 1) + 8 * (pp & 1);
        const int tra = trb + 512 * wt, trw = P2_IMG + trb + 512 * wd;
        const int ca0 = P2_IMG + (32 * wd + li) * 128 + 16 * (g ^ (lane & 7));
        s16x8 wf[2][2][2], af[2][2][2];
        int slot = 0;
        auto next = [&]() { slot = slot == P2_RING - P2_SLOT ? 0 : slot + P2_SLOT; };
        auto frag_a = [&](LAS char* sl, int buf, int ks, int i) { LAS char* a = sl + tra + 4160 * ks + 32 * i; af[buf][ks][i] = lds_tr(a, a + 256); };
        auto frag_w = [&](LAS char* sl, int buf, int ks, int i, bool g4) {
            if (g4) { LAS char* a = sl + trw + 4160 * ks + 32 * i; wf[buf][ks][i] = lds_tr(a, a + 256); }
            else wf[buf][ks][i] = lds_row(sl + ((ca0 ^ (64 * ks)) + 2048 * i));
        };
        auto half = [&](int rb, bool rd, bool g4, int k0, bool mm_) {
            LAS char* sl = smem + slot;
            const int ub = rb ^ 1;
#pragma unroll
            for (int ks = 0; ks < 2; ++ks) {
#pragma unroll
                for (int m = 0; m < 4; ++m) {
                    if (mm_ && ks >= k0) mfma_acc(o[m >> 1][m & 1], wf[ub][ks][m >> 1], af[ub][ks][m & 1]);
                    SB0;
                    if (rd) { if (m < 2) frag_a(sl, rb, ks, m); else frag_w(sl, rb, ks, m - 2, g4); }
                    SB0;
                }
            }
            if (rd) next();
        };
        const GAS unsigned* fsp = (const GAS unsigned*)(ws + WS_FS) + T * 32 + (lane & 31);
        const unsigned fsl = (unsigned)(size_t)smem + L_EL + 256 * wave;
        __builtin_amdgcn_s_setprio(CPRIO2);
        bar(); half(0, true, true, 0, false);
        STAMP(7);
        if (DEFER) store_h();
        half(1, true, true, 0, true);
#pragma unroll 1
        for (int seg = 0, j = 1; seg < 4; ++seg) {
            const int jend = seg == 0 ? 2 + DEFER : (seg == 1 ? 7 : (seg == 2 ? 8 : 10));
#pragma unroll 1
            for (; j < jend; ++j) {
                bar(); half(0, true, true, 0, true);
                half(1, true, true, 0, true);
            }
            if (seg == 0) {
                wait_vm<0>();
                if (lane == 0) __hip_atomic_store((GAS unsigned*)(ws + WS_FS) + Tw * 32 + 2 * cj + wn, tag, __ATOMIC_RELAXED, __HIP_MEMORY_SCOPE_AGENT);
            } else if (seg == 1) {
                asm volatile("s_mov_b32 m0, %0\n\ts_nop 0\n\tglobal_load_lds_dword %1, off sc1" ::"s"(fsl), "v"(fsp) : "memory");
            } else if (seg == 2) {
                unsigned fsv;
                asm volatile("s_waitcnt vmcnt(0)\n\tds_read_b32 %0, %1\n\ts_waitcnt lgkmcnt(0)" : "=&v"(fsv) : "v"(fsl + 4 * lane) : "memory");
                if (!__all(fsv == tag)) wait_flags((const GAS unsigned*)(ws + WS_FS) + T * 32, tag);
            }
            SB0;
        }
        bar(); half(0, true, true, 0, true);
        half(1, true, true, 0, true);
        bar(); half(0, true, false, 1, true);
        half(1, true, false, 0, true);
#pragma unroll 1
        for (int j = 12; j < 19; ++j) {
            bar(); half(0, true, false, 0, true);
            half(1, true, false, 0, true);
        }
        half(0, false, false, 0, true);
        asm volatile("s_nop 7\n\ts_nop 7" : "+v"(o[0][0]), "+v"(o[0][1]), "+v"(o[1][0]), "+v"(o[1][1]));
        STAMP(8);

#pragma unroll
        for (int j = 0; j < 2; ++j)
#pragma unroll
            for (int i = 0; i < 2; ++i)
                *(f32x4*)(out + (64 * T + 32 * j + 16 * ((lane >> 3) & 1) + 8 * wt + (lane & 7)) * DM + d0 + 32 * wd + 16 * i + 4 * g) = o[i][j];
        if (ci == 15 && cj == 0 && threadIdx.x == 0) __hip_atomic_store((GAS unsigned*)(ws + WS_EP), tag, __ATOMIC_RELAXED, __HIP_MEMORY_SCOPE_AGENT);
        STAMP(9);
    }
}

}

extern "C" void kernel_launch(void* const* d_in, const int* in_sizes, int n_in, void* d_out, int out_size, void* d_ws, size_t ws_size,
                              hipStream_t stream) {
    typedef const unsigned short* P;
    (void)hipFuncSetAttribute((const void*)k_fused, hipFuncAttributeMaxDynamicSharedMemorySize, LDS_BYTES);
    k_fused<<<256, 512, LDS_BYTES, stream>>>((P)d_in[0], (P)d_in[1], (P)d_in[2], (P)d_in[3], (P)d_in[4], (P)d_in[5], (P)d_in[6], (P)d_in[7],
                                             (P)d_in[8], (P)d_in[10], (P)d_in[11], (P)d_in[12], (P)d_in[13], (char*)d_ws, (float*)d_out);
}
```

```cpp
#include <hip/hip_runtime.h>
#include <hip/hip_bf16.h>
#include <stdint.h>
#include <type_traits>

#define LAS __attribute__((address_space(3)))
#define GAS __attribute__((address_space(1)))

typedef __attribute__((ext_vector_type(8))) short s16x8;
typedef __attribute__((ext_vector_type(4))) short s16x4;
typedef __attribute__((ext_vector_type(8))) __bf16 bf16x8;
typedef __attribute__((ext_vector_type(2))) __bf16 bf16x2;
typedef __attribute__((ext_vector_type(4))) float f32x4;
typedef __attribute__((ext_vector_type(2))) float f32x2;
typedef __attribute__((ext_vector_type(4))) unsigned u32x4;
typedef __attribute__((ext_vector_type(2))) unsigned u32x2;

#define ACT_AUX 0
#define ST_AUX 16
#define PREFETCH 1
#define WARM1 0
#define PRIO 0
#define WARMA 0
#define D4 4
#define D3 3
#define PREF2 0
#define DEFER 0
#define LPRIO2 0
#define CPRIO2 0
#define CODEW 2
#define CODE_LINES 200
#define LAMLATE 0
#define JIT 0
#define KROT 0
#define HANDW 1
#define ACQ 0

namespace {

constexpr int SEQ = 2048, DM = 512, NP = 512, FF = 1376;
constexpr int KACT = FF + 2 * NP;
constexpr int ACT_TILE = KACT * 128;
constexpr unsigned WS_ACT = 0;
constexpr unsigned WS_RS = 10u << 20;
constexpr unsigned WS_EG = WS_RS + (64u << 10);
constexpr unsigned WS_FH = WS_EG + (256u << 10);
constexpr unsigned WS_FS = WS_FH + (8u << 10);
constexpr unsigned WS_CS = WS_FS + (8u << 10);
constexpr unsigned WS_EP = WS_CS + (256u << 10);
constexpr size_t WS_STAMP = 200u << 20;
constexpr float LN_EPS = 1e-5f;
constexpr unsigned SPIN_CAP = 1u << 18;

constexpr int P1_SLOT = 36 * 1024, P1_X = 0, P1_B = 16384, P1_W = 24576;
constexpr int P2_IMG = 4 * 2080, P2_SLOT = 2 * P2_IMG, P2_RING = 9 * P2_SLOT, P2_NST = 38;
constexpr int L_TAB1 = P2_RING;
constexpr int L_TAB2 = L_TAB1 + 1024;
constexpr int L_EL = L_TAB2 + 1024;
constexpr int L_CS = L_EL + 2048;
constexpr int L_TOK = L_CS + 1280;
constexpr int L_DUMMY = L_TOK + 1024;
constexpr int L_MAIL = L_DUMMY + 1024;
constexpr int LDS_BYTES = L_MAIL + 64 + 704;

#define STAMP(s) do {} while (0)

#define SB0 __builtin_amdgcn_sched_barrier(0)
template <int N> __device__ __forceinline__ void wait_vm() { asm volatile("s_waitcnt vmcnt(%0)" ::"i"(N) : "memory"); }
__device__ __forceinline__ void wait_lgkm0() { __builtin_amdgcn_s_waitcnt(0xC07F); asm volatile("" ::: "memory"); }
template <int NM, int ND, int DPM = 1, int I = 0> __device__ __forceinline__ void interleave() {
    if constexpr (I * DPM < ND && I < NM) {
        __builtin_amdgcn_sched_group_barrier(0x008, 1, 0);
        __builtin_amdgcn_sched_group_barrier(0x100, (ND - I * DPM) < DPM ? (ND - I * DPM) : DPM, 0);
        interleave<NM, ND, DPM, I + 1>();
    } else if constexpr (I < NM) {
        __builtin_amdgcn_sched_group_barrier(0x008, NM - I, 0);
    }
}
__device__ __forceinline__ void bar() {
    SB0;
    asm volatile("" ::: "memory");
    __builtin_amdgcn_s_barrier();
    asm volatile("" ::: "memory");
    SB0;
}
__device__ __forceinline__ float bf_lo(unsigned u) { return __uint_as_float(u << 16); }
__device__ __forceinline__ float bf_hi(unsigned u) { return __uint_as_float(u & 0xffff0000u); }
__device__ __forceinline__ float bf2f(unsigned short h) { return __uint_as_float((unsigned)h << 16); }
__device__ __forceinline__ unsigned pk_bf16(float a, float b) { bf16x2 v = {(__bf16)a, (__bf16)b}; return __builtin_bit_cast(unsigned, v); }
__device__ __forceinline__ float rbf(float a) { return bf_lo(pk_bf16(a, a)); }
__device__ __forceinline__ f32x2 pfma(f32x2 a, f32x2 b, f32x2 c) { return __builtin_elementwise_fma(a, b, c); }
__device__ __forceinline__ f32x2 splat(float a) { return f32x2{a, a}; }
__device__ __forceinline__ f32x4 mfma(s16x8 a, s16x8 b, f32x4 c) {
    return __builtin_amdgcn_mfma_f32_16x16x32_bf16(__builtin_bit_cast(bf16x8, a), __builtin_bit_cast(bf16x8, b), c, 0, 0, 0);
}
__device__ __forceinline__ void mfma_acc(f32x4& c, s16x8 a, s16x8 b) {
    asm volatile("v_mfma_f32_16x16x32_bf16 %0, %1, %2, %0" : "+v"(c) : "v"(a), "v"(b));
}
__device__ __forceinline__ s16x4 lds_tr1(LAS char* p) { return __builtin_amdgcn_ds_read_tr16_b64_v4i16((LAS s16x4*)p); }
__device__ __forceinline__ s16x8 cat(s16x4 a, s16x4 b) { return __builtin_shufflevector(a, b, 0, 1, 2, 3, 4, 5, 6, 7); }
__device__ __forceinline__ s16x8 lds_row(LAS char* p) { return *(LAS s16x8*)p; }
__device__ __forceinline__ s16x8 lds_tr(LAS char* p0, LAS char* p1) {
    s16x4 a = __builtin_amdgcn_ds_read_tr16_b64_v4i16((LAS s16x4*)p0);
    s16x4 b = __builtin_amdgcn_ds_read_tr16_b64_v4i16((LAS s16x4*)p1);
    return __builtin_shufflevector(a, b, 0, 1, 2, 3, 4, 5, 6, 7);
}
template <int OFF> __device__ __forceinline__ void dma1(unsigned lds, unsigned voff, const GAS char* base) {
    asm volatile("s_mov_b32 m0, %0\n\ts_nop 0\n\tglobal_load_lds_dwordx4 %1, %2 offset:%3" ::"s"(lds), "v"(voff), "s"(base), "i"(OFF) : "memory");
}
template <int OFF> __device__ __forceinline__ void dma2(unsigned lds, unsigned voff0, unsigned voff1, const GAS char* base) {
    asm volatile("s_mov_b32 m0, %0\n\ts_nop 0\n\tglobal_load_lds_dwordx4 %1, %3\n\tglobal_load_lds_dwordx4 %2, %3 offset:%4"
                 ::"s"(lds), "v"(voff0), "v"(voff1), "s"(base), "i"(OFF) : "memory");
}
__device__ __forceinline__ void touch(unsigned lds, const GAS char* a) {
    asm volatile("s_mov_b32 m0, %0\n\ts_nop 0\n\tglobal_load_lds_dword %1, off" ::"s"(lds), "v"(a) : "memory");
}
__device__ __forceinline__ void lds_st32(unsigned a, float v) { asm volatile("ds_write_b32 %0, %1" ::"v"(a), "v"(v) : "memory"); }
__device__ __forceinline__ void lds_st64(unsigned a, f32x2 v) { asm volatile("ds_write_b64 %0, %1" ::"v"(a), "v"(v) : "memory"); }
template <int CTRL, int RM> __device__ __forceinline__ float dpp_add(float v) {
    return v + __uint_as_float(__builtin_amdgcn_update_dpp(0u, __float_as_uint(v), CTRL, RM, 0xf, true));
}
__device__ __forceinline__ float wave_sum(float v) {
    v = dpp_add<0x111, 0xf>(v); v = dpp_add<0x112, 0xf>(v); v = dpp_add<0x114, 0xf>(v); v = dpp_add<0x118, 0xf>(v);
    v = dpp_add<0x142, 0xa>(v); v = dpp_add<0x143, 0xc>(v);
    return __uint_as_float(__builtin_amdgcn_readlane(__float_as_uint(v), 63));
}
__device__ __forceinline__ float sum_groups(float v) {
    const auto a = __builtin_amdgcn_permlane16_swap(__float_as_uint(v), __float_as_uint(v), false, false);
    v = __uint_as_float(a[0]) + __uint_as_float(a[1]);
    const auto b = __builtin_amdgcn_permlane32_swap(__float_as_uint(v), __float_as_uint(v), false, false);
    return __uint_as_float(b[0]) + __uint_as_float(b[1]);
}
__device__ __forceinline__ u32x4 load16_sc1(const GAS void* p) {
    u32x4 v;
    asm volatile("global_load_dwordx4 %0, %1, off sc1\n\ts_waitcnt vmcnt(0)" : "=&v"(v) : "v"(p) : "memory");
    return v;
}
__device__ __forceinline__ void store16_sc1(GAS void* p, u32x4 v) {
    asm volatile("global_store_dwordx4 %0, %1, off sc1\n\ts_nop 1" ::"v"(p), "v"(v) : "memory");
}
__device__ __forceinline__ void wait_flags(const GAS unsigned* f, unsigned tag) {
#pragma unroll 1
    for (unsigned spins = 0; spins < SPIN_CAP; ++spins) {
        const unsigned v = __hip_atomic_load(f + (threadIdx.x & 31), __ATOMIC_RELAXED, __HIP_MEMORY_SCOPE_AGENT);
        if (__all(v == tag)) break;
        __builtin_amdgcn_s_sleep(1);
    }
    asm volatile("" ::: "memory");
}
struct cf { float r, i; };
__device__ __forceinline__ cf cmul(cf a, cf b) { return cf{a.r * b.r - a.i * b.i, a.r * b.i + a.i * b.r}; }
__device__ __forceinline__ cf cfma(cf a, cf b, cf c) { return cf{fmaf(a.r, b.r, fmaf(-a.i, b.i, c.r)), fmaf(a.r, b.i, fmaf(a.i, b.r, c.i))}; }
__device__ __forceinline__ float exp_f(float x) {
    const float hi = x * 1.44269504f, lo = fmaf(x, 1.44269504f, -hi) + x * 1.92596299e-8f;
    return __builtin_amdgcn_exp2f(hi) * fmaf(lo, 0.693147181f, 1.0f);
}
__device__ __forceinline__ void sincos_f(float x, float& s, float& c) {
    const float k = rintf(x * 0.636619772f);
    float r = fmaf(k, -1.57079601e+00f, x);
    r = fmaf(k, -3.13916473e-07f, r);
    r = fmaf(k, -5.39030253e-15f, r);
    const float z = r * r;
    const float sp = fmaf(r * z, fmaf(z, fmaf(z, fmaf(z, 2.75573137e-06f, -1.98412698e-04f), 8.33333333e-03f), -1.66666667e-01f), r);
    const float cp = fmaf(z, fmaf(z, fmaf(z, fmaf(z, fmaf(z, -2.75573144e-07f, 2.48015873e-05f), -1.38888889e-03f), 4.16666667e-02f), -0.5f), 1.0f);
    const int q = (int)k;
    const float a = (q & 1) ? cp : sp, b = (q & 1) ? sp : cp;
    s = (q & 2) ? -a : a;
    c = ((q + 1) & 2) ? -b : b;
}
__device__ __forceinline__ unsigned row_std(float v) {
    const float s1 = rbf(__builtin_amdgcn_sqrtf(rbf(rbf(v) + LN_EPS)));
    const float r1 = __builtin_amdgcn_rcpf(s1);
    const float s2 = __builtin_amdgcn_sqrtf(rbf(rbf(v * r1 * r1) + LN_EPS));
    return pk_bf16(s1, s2);
}

typedef const unsigned short* __restrict__ bfp;

__global__ __launch_bounds__(512) void k_fused(bfp x, bfp lam_re, bfp lam_im, bfp bre, bfp bim, bfp cre, bfp cim, bfp log_dt, bfp dskip,
                                               bfp w1, bfp b1, bfp w2, bfp b2, char* ws_, float* __restrict__ out) {
    extern __shared__ __attribute__((aligned(16))) char smem_[];
    LAS char* smem = (LAS char*)smem_;
    GAS char* ws = (GAS char*)ws_;
    const int wave = __builtin_amdgcn_readfirstlane(threadIdx.x >> 6), lane = threadIdx.x & 63;
    const int g = lane >> 4, li = lane & 15;
    const int xg8 = blockIdx.x & 7, loc = blockIdx.x >> 3, ci = 4 * (xg8 >> 1) + (loc >> 3), cj = 8 * (xg8 & 1) + (loc & 7);
    const int t0 = 128 * ci, p0 = 32 * cj, f0 = cj < 11 ? 96 * cj : 1056 + 64 * (cj - 11), nfu = cj < 11 ? 6 : 4;
    const int T = 2 * ci + (cj >> 3), d0 = 64 * (cj & 7);
    const int rot = ((ci + 15) >> 1) & 7;
    STAMP(0);
    unsigned long long entry_pc;
    asm volatile("s_getpc_b64 %0" : "=s"(entry_pc));
    unsigned epoch;
    asm volatile("global_load_dword %0, %1, off sc1" : "=v"(epoch) : "v"(ws + WS_EP) : "memory");
    const int q = li >> 2, pp = lane & 3;

    if (__builtin_expect(wave >= 4, 1)) {
        const int lw = wave - 4;
        __builtin_amdgcn_s_setprio(PRIO);
        const unsigned lds0 = (unsigned)(size_t)smem;
        unsigned xo[4], bo[2], wo[3];
#pragma unroll
        for (int i = 0; i < 4; ++i) {
            const int R = 8 * (4 * lw + i) + (lane >> 3), ii = R & 15, bb = (R >> 4) & 3;
            const int tok = t0 + (R & 64) + 16 * (ii >> 2) + 8 * (ii & 1) + 2 * bb + ((ii >> 1) & 1);
            xo[i] = tok * 1024 + 16 * ((lane & 7) ^ (lane >> 3)) + 3072 - 1024 * i;
        }
#pragma unroll
        for (int i = 0; i < 2; ++i) {
            const int R = 8 * (2 * lw + i) + (lane >> 3);
            bo[i] = (p0 + (R & 31)) * 1024 + 16 * ((lane & 7) ^ (lane >> 3)) + 3072 - 1024 * i;
        }
#pragma unroll
        for (int i = 0; i < 3; ++i) {
            const int st = 2 * (3 * lw + i) + (lane >> 5), kg = st / 3, ns = st - 3 * kg, kr = (lane & 31) >> 2;
            const int ch = 4 * ns + ((lane & 3) ^ ((2 * kg + (kr >> 2)) & 3));
            const int f = min(f0 + 8 * ch, FF - 8);
            wo[i] = (8 * kg + kr) * (FF * 2) + f * 2 + 3072 - 1024 * i;
        }
        const GAS char* xg = (const GAS char*)x - 3072;
        const GAS char* bg = (const GAS char*)(lw < 2 ? bre : bim) - 3072;
        const GAS char* wg = (const GAS char*)w1 - 3072;
        const unsigned lx = lds0 + P1_X + lw * 4096, lb = lds0 + P1_B + lw * 2048, lwb = lds0 + P1_W + lw * 3072;
        auto piece = [&](int t, auto I) {
            constexpr int i = decltype(I)::value;
            const unsigned so = (t & 3) * P1_SLOT;
            const int kt = (t + rot) & 7;
            if constexpr (i < 4) dma1<1024 * i>(lx + so, xo[i], xg + kt * 128);
            else if constexpr (i < 6) dma1<1024 * (i - 4)>(lb + so, bo[i - 4], bg + kt * 128);
            else dma1<1024 * (i - 6)>(lwb + so, wo[i - 6], wg + (size_t)kt * (64 * FF * 2));
        };
#define PC(t, i) piece(t, std::integral_constant<int, i>{})
        auto issue = [&](int t) { PC(t, 0); PC(t, 1); PC(t, 2); PC(t, 3); PC(t, 4); PC(t, 5); PC(t, 6); PC(t, 7); PC(t, 8); };
        const GAS char* tbase;
        unsigned tstep;
        if (lw == 0) { tbase = (const GAS char*)x + (t0 + 16 * (cj & 7) + li) * 1024; tstep = 128; }
        else if (lw == 1) { const int r = 16 * (ci & 3) + li; tbase = (const GAS char*)(r < 32 ? bre : bim) + (p0 + (r & 31)) * 1024; tstep = 128; }
        else { tbase = (const GAS char*)w1 + (16 * (ci & 3) + li) * (FF * 2) + min(f0 * 2 + (lw == 2 ? 0 : 128), FF * 2 - 4); tstep = 64 * FF * 2; }
        auto warm = [&](int t) { if (WARM1) touch(lds0 + L_DUMMY + 256 * lw, tbase + ((t + rot) & 7) * tstep); };
        STAMP(6);
        if (CODEW == 1) {
            touch(lds0 + L_DUMMY + 256 * lw, (const GAS char*)(entry_pc & ~255ull) + min(((loc & 3) * 4 + lw) * 13 + min(lane, 12), CODE_LINES - 1) * 128);
        }
        issue(0); issue(1);
        warm(2 + (g & 1));
        if (CODEW == 2) {
            touch(lds0 + L_DUMMY + 256 * lw, (const GAS char*)(entry_pc & ~255ull) + min(((loc & 3) * 4 + lw) * 13 + min(lane, 12), CODE_LINES - 1) * 128);
        }
        STAMP(7);

        unsigned w2o[2], co[2], ao[2];
#pragma unroll
        for (int i = 0; i < 2; ++i) {
            const int kg = 2 * lw + i;
            w2o[i] = (8 * kg + ((lane & 31) >> 2)) * (DM * 2) + (d0 + 32 * (lane >> 5) + 8 * (lane & 3)) * 2 + 3072 - 1056 * i;
            co[i] = (d0 + 8 * kg + (lane >> 3)) * (NP * 2) + 16 * ((lane & 7) ^ (lane >> 3)) + 3072 - 1024 * i;
            ao[i] = kg * 1024 + lane * 16 + 3072 - 1056 * i;
        }
        const GAS char* ag = ws + WS_ACT + (size_t)T * ACT_TILE;
        const GAS char* w2g = (const GAS char*)w2 - 3072;
        const unsigned l2 = lds0 + lw * 2080, l2c = lds0 + P2_IMG + lw * 2048;
        auto dmaA = [&](const GAS char* src, unsigned slot) { dma2<1056>(l2 + slot, ao[0], ao[1], src - 3072); };
        auto dmaW = [&](const GAS char* src, unsigned slot) { dma2<1056>(l2 + slot + P2_IMG, w2o[0], w2o[1], src); };
        auto dmaC = [&](const GAS char* src, unsigned slot) { dma2<1024>(l2c + slot, co[0], co[1], src); };
        const int u4 = KROT ? 2 * (((cj & 7) + 3 * (ci & 3)) % 10) : 0, u3 = KROT ? 2 * (((cj & 7) + 2 * (ci & 3)) & 7) : 0;
        auto wrap20 = [](int u) { return u >= 20 ? u - 20 : u; };
        auto preW = [&](int s) { dmaW(w2g + wrap20(u4 + s) * 65536, s * P2_SLOT); };

        const GAS char* l2a[2];
#pragma unroll
        for (int i = 0; i < 2; ++i) {
            const int n = 456 * (ci & 3) + 114 * lw + min(64 * i + lane, 113);
            const int m = n - 800;
            l2a[i] = n < 800 ? (const GAS char*)w2 + (576 + n) * 1024 + d0 * 2
                             : (const GAS char*)(m < 512 ? cre : cim) + (d0 + ((m >> 3) & 63)) * 1024 + (m & 7) * 128;
        }
        const int n1 = 75 * blockIdx.x + 19 * lw + min(lane, 18);
        const GAS char* l1a = n1 < 11008 ? (const GAS char*)w2 + n1 * 128
                                         : (n1 < 15104 ? (const GAS char*)cre + (n1 - 11008) * 128 : (const GAS char*)cim + (min(n1, 19199) - 15104) * 128);
        if (HANDW && lane >= 19 && lane < 24)
            l1a = lane == 19 ? ws + WS_RS + (t0 + 8 * cj) * 16 : ws + (lane < 22 ? WS_FH : WS_FS) + (2 * ci + (lane & 1)) * 128;
        const GAS u32x4* rsp = (const GAS u32x4*)(ws + WS_RS) + t0 + 64 * (lw & 1) + lane;
        const GAS u32x4* csp = (const GAS u32x4*)(ws + WS_CS) + ((cj * 16 + 8 * (lw >> 1) + 2 * g) * 2 + (lw & 1)) * 32 + li;
        asm volatile("" : "+v"(l2a[0]), "+v"(l2a[1]), "+v"(l1a), "+v"(rsp), "+v"(csp));
        asm volatile("s_waitcnt vmcnt(%1)" : "+v"(epoch) : "i"(9 + WARM1 + (CODEW == 2)) : "memory");
        unsigned tag = __builtin_amdgcn_readfirstlane(epoch) + 1u;
        asm volatile("" : "+s"(tag));
        bar();
        STAMP(1);
        issue(2); warm(4);
        if (PREFETCH) touch(lds0 + L_DUMMY + 256 * lw, l1a);
        wait_vm<9 + 2 * WARM1 + PREFETCH>(); bar();
        issue(3); warm(5);
        {
            f32x2 v;
            asm volatile("ds_read_b64 %0, %1\n\ts_waitcnt lgkmcnt(0)" : "=&v"(v) : "v"(lds0 + L_MAIL + 16 * lw + 8 * (lane & 1)) : "memory");
            if (lane < 2) store16_sc1((GAS u32x4*)(ws + WS_RS) + t0 + 8 * cj + 2 * lw + lane, u32x4{__float_as_uint(v[0]), tag, __float_as_uint(v[1]), tag});
        }
        wait_vm<10 + 2 * WARM1 + PREFETCH>(); bar();
        issue(4); warm(6);
        {
            float a, b, c;
            const unsigned ma = lds0 + L_MAIL + 64 + 4 * (((lw >> 1) * 5 + 3 * (lw & 1)) * 16 + li);
            asm volatile("ds_read_b32 %0, %3\n\tds_read_b32 %1, %3 offset:64\n\tds_read_b32 %2, %3 offset:128\n\ts_waitcnt lgkmcnt(0)"
                         : "=&v"(a), "=&v"(b), "=&v"(c) : "v"(ma) : "memory");
            if (g == 0) store16_sc1((GAS u32x4*)(ws + WS_CS) + ((cj * 16 + ci) * 2 + (lw >> 1)) * 32 + 16 * (lw & 1) + li,
                                    u32x4{__float_as_uint(a), __float_as_uint(b), __float_as_uint(c), tag});
        }
        wait_vm<11 + 2 * WARM1>(); bar();
        issue(5); warm(7); wait_vm<10 + 2 * WARM1>(); bar();
        issue(6); wait_vm<9 + WARM1>(); bar();
        issue(7);
        u32x4 rs, cs[4];
        auto gather = [&]() {
            asm volatile("global_load_dwordx4 %0, %5, off sc1\n\t"
                         "global_load_dwordx4 %1, %6, off sc1\n\tglobal_load_dwordx4 %2, %6, off offset:256 sc1\n\t"
                         "global_load_dwordx4 %3, %6, off offset:1024 sc1\n\tglobal_load_dwordx4 %4, %6, off offset:1280 sc1"
                         : "=&v"(rs), "=&v"(cs[0]), "=&v"(cs[1]), "=&v"(cs[2]), "=&v"(cs[3]) : "v"(rsp), "v"(csp) : "memory");
        };
        gather();
        wait_vm<14>(); bar();
        preW(0); preW(1); preW(2); preW(3);
        if (PREF2 == 1) { touch(lds0 + L_DUMMY + 256 * lw, l2a[0]); touch(lds0 + L_DUMMY + 256 * lw, l2a[1]); }
        asm volatile("s_waitcnt vmcnt(%5)" : "+v"(rs), "+v"(cs[0]), "+v"(cs[1]), "+v"(cs[2]), "+v"(cs[3]) : "i"(PREF2 == 1 ? 10 : 8) : "memory");
        bar();
        preW(4); preW(5);
        auto fresh = [&]() {
            return __all(rs[1] == tag && rs[3] == tag && cs[0][3] == tag && cs[1][3] == tag && cs[2][3] == tag && cs[3][3] == tag);
        };
#pragma unroll 1
        for (unsigned spins = 0; spins < SPIN_CAP && !fresh(); ++spins) {
            gather();
            asm volatile("s_waitcnt vmcnt(0)" : "+v"(rs), "+v"(cs[0]), "+v"(cs[1]), "+v"(cs[2]), "+v"(cs[3])::"memory");
        }
        if (lw < 2) {
            const float m = __uint_as_float(rs[0]), mb = rbf(m);
            const float r1 = __builtin_amdgcn_rcpf(bf_lo(rs[2])), r12 = r1 * __builtin_amdgcn_rcpf(bf_hi(rs[2]));
            const int tau = 64 * lw + lane;
            const unsigned ta = lds0 + L_TAB1 + 4 * (((tau >> 4) * 8 + (tau & 7)) * 4 + ((tau >> 3) & 1));
            lds_st32(ta, r1); lds_st32(ta + 8, -r1 * mb);
            lds_st32(ta + 1024, r12); lds_st32(ta + 1032, -r12 * m);
            lds_st64(lds0 + L_TOK + 8 * tau, f32x2{mb, r1});
        }
        {
            float v5[5];
#pragma unroll
            for (int k = 0; k < 3; ++k) v5[k] = __uint_as_float(cs[0][k]) + __uint_as_float(cs[2][k]);
#pragma unroll
            for (int k = 0; k < 2; ++k) v5[3 + k] = __uint_as_float(cs[1][k]) + __uint_as_float(cs[3][k]);
#pragma unroll
            for (int k = 0; k < 5; ++k) v5[k] = sum_groups(v5[k]);
            if (g == 0) {
#pragma unroll
                for (int k = 0; k < 5; ++k) lds_st32(lds0 + L_CS + 4 * ((((lw >> 1) * 2 + (lw & 1)) * 5 + k) * 16 + li), v5[k]);
            }
        }
        asm volatile("s_waitcnt lgkmcnt(0)" ::: "memory");
        bar();
        preW(6); preW(7); preW(8);
        STAMP(2);
        if (PREF2 == 2) { touch(lds0 + L_DUMMY + 256 * lw, l2a[0]); touch(lds0 + L_DUMMY + 256 * lw, l2a[1]); }
        bar();
        bar();
        if (PREF2 == 3) { touch(lds0 + L_DUMMY + 256 * lw, l2a[0]); touch(lds0 + L_DUMMY + 256 * lw, l2a[1]); }
        __builtin_amdgcn_s_setprio(LPRIO2);
        wait_flags((const GAS unsigned*)(ws + WS_FH) + T * 32, tag);
        STAMP(3);
        auto preA = [&](int st) { dmaA(ag + wrap20(u4 + st) * 8192, st * P2_SLOT); };
        preA(0); preA(1); preA(2); preA(3);
        wait_vm<4>(); bar();
        STAMP(4);
        preA(4); preA(5); preA(6); preA(7);
        wait_vm<8>(); bar();
        int u = wrap20(u4 + 9);
        unsigned slot = 0;
        auto nxt = [&]() { slot = slot == P2_RING - P2_SLOT ? 0 : slot + P2_SLOT; };
        auto stW = [&]() { dmaA(ag + u * 8192, slot); dmaW(w2g + u * 65536, slot); u = u == 19 ? 0 : u + 1; nxt(); };
        auto stC = [&]() {
            dmaA(ag + (FF * 128 + u * 8192), slot);
            dmaC((const GAS char*)(u < 8 ? cre : cim) - 3072 + (u & 7) * 128, slot);
            u = (u + 1) & 15; nxt();
        };
        preA(8); stW();
        wait_vm<10>(); bar(); stW(); stW();
        wait_vm<14>(); bar(); stW(); stW();
#pragma unroll 1
        for (int j = 4; j < 7; ++j) { wait_vm<16>(); bar(); stW(); stW(); }
        wait_vm<16>(); bar();
        dmaA(ag + 20 * 8192, slot); dmaW(w2g + 20 * 65536, slot); nxt();
        dmaA(ag + 21 * 8192 - 4096, slot); dmaW(w2g + 21 * 65536 - 32768, slot); nxt();
        u = u3;
        wait_vm<16>(); bar();
        stC(); stC();
#pragma unroll 1
        for (int j = 9; j < 16; ++j) { wait_vm<16>(); bar(); stC(); stC(); }
        wait_vm<16>(); bar();
        wait_vm<8>(); bar();
        wait_vm<0>(); bar();
        STAMP(5);
        return;
    }

    const int wm = wave >> 1, wn = wave & 1;
    const int p = p0 + 16 * wn + li;
    cf lam = {0.0f, 0.0f}, l8 = lam, l16 = lam, l128 = lam, lsig = lam, coef2 = lam;
    float b1c[3], p_lr, p_lim, p_ldt;
    unsigned tag;
    {
        u32x4 xr[2];
        unsigned short pr[6];
#pragma unroll
        for (int i = 0; i < 2; ++i) xr[i] = *(const u32x4*)(x + (t0 + 8 * cj + 2 * wave + i) * DM + lane * 8);
        pr[0] = lam_re[p]; pr[1] = lam_im[p]; pr[2] = log_dt[p];
#pragma unroll
        for (int c = 0; c < 3; ++c) pr[3 + c] = b1[min(f0 + 16 * (3 * wn + c) + li, FF - 1)];
        SB0;
        p_lr = bf2f(pr[0]); p_lim = bf2f(pr[1]); p_ldt = bf2f(pr[2]);
#pragma unroll
        for (int c = 0; c < 3; ++c) b1c[c] = bf2f(pr[3 + c]);
        float mean[2], var[2];
#pragma unroll
        for (int i = 0; i < 2; ++i) {
            float e[8], s = 0.0f;
#pragma unroll
            for (int j = 0; j < 4; ++j) { e[2 * j] = bf_lo(xr[i][j]); e[2 * j + 1] = bf_hi(xr[i][j]); s += e[2 * j] + e[2 * j + 1]; }
            mean[i] = wave_sum(s) * (1.0f / DM);
            float qq = 0.0f;
#pragma unroll
            for (int j = 0; j < 8; ++j) qq = fmaf(e[j] - mean[i], e[j] - mean[i], qq);
            var[i] = wave_sum(qq) * (1.0f / DM);
        }
        if (lane < 2) ((LAS f32x2*)(smem + L_MAIL))[2 * wave + lane] = f32x2{lane ? mean[1] : mean[0], __uint_as_float(row_std(lane ? var[1] : var[0]))};
        asm volatile("" : "+v"(b1c[0]), "+v"(b1c[1]), "+v"(b1c[2]), "+v"(p_lr), "+v"(p_lim), "+v"(p_ldt));
    }
    auto lam_setup = [&]() {
        const float lr = p_lr, lim = p_lim, dt = rbf(exp_f(p_ldt));
        const float mag = exp_f(dt * lr);
        float sn, cs;
        sincos_f(dt * lim, sn, cs);
        lam = cf{mag * cs, mag * sn};
        const float nr = lam.r - 1.0f, ni = lam.i, den = 1.0f / (lr * lr + lim * lim);
        coef2 = cf{2.0f * (nr * lr + ni * lim) * den, 2.0f * (ni * lr - nr * lim) * den};
        const cf l2 = cmul(lam, lam), l4 = cmul(l2, l2);
        l8 = cmul(l4, l4); l16 = cmul(l8, l8);
        const cf l32 = cmul(l16, l16), l64 = cmul(l32, l32);
        l128 = cmul(l64, l64);
        lsig = (g & 1) ? l16 : cf{1.0f, 0.0f};
        if (g & 2) lsig = cmul(lsig, l32);
        if (wm) lsig = cmul(lsig, l64);
        asm volatile("" : "+v"(lam.r), "+v"(lam.i), "+v"(l8.r), "+v"(l8.i), "+v"(l16.r), "+v"(l16.i), "+v"(l128.r), "+v"(l128.i), "+v"(lsig.r), "+v"(lsig.i),
                          "+v"(coef2.r), "+v"(coef2.i));
    };
    f32x4 acc[4][5];
    {
        const int sw = 16 * (g ^ (lane & 7));
        const int xa0 = P1_X + (64 * wm + li) * 128 + sw;
        const int ba0 = P1_B + (16 * wn + li) * 128 + sw;
        int wa[2][2];
#pragma unroll
        for (int t = 0; t < 2; ++t)
#pragma unroll
            for (int cl = 0; cl < 2; ++cl) {
                const int par = (3 * wn + cl) & 1;
                wa[t][cl] = P1_W + 1536 * g + 64 * q + 256 * t + 32 * (par ^ (g & 1)) + 16 * ((pp >> 1) ^ t) + 8 * (pp & 1) + (cl == 0 ? 512 * wn : 1024 * wn);
            }
#pragma unroll
        for (int b = 0; b < 4; ++b)
#pragma unroll
            for (int c = 0; c < 5; ++c) acc[b][c] = f32x4{0, 0, 0, 0};
        s16x8 xa[2][4], wb[2][5];
        auto rd = [&](int slot, int ks, int buf) {
            LAS char* s = smem + slot * P1_SLOT;
            LAS char* xs = s + (xa0 ^ (64 * ks));
            LAS char* bs = s + (ba0 ^ (64 * ks));
#pragma unroll
            for (int b = 0; b < 4; ++b) xa[buf][b] = lds_row(xs + b * 2048);
            wb[buf][0] = lds_row(bs);
            wb[buf][1] = lds_row(bs + 4096);
            wb[buf][2] = lds_tr(s + wa[0][0] + 6144 * ks, s + wa[1][0] + 6144 * ks);
            wb[buf][3] = lds_tr(s + wa[0][1] + 6144 * ks, s + wa[1][1] + 6144 * ks);
            wb[buf][4] = lds_tr(s + wa[0][0] + 6144 * ks + 512, s + wa[1][0] + 6144 * ks + 512);
        };
        auto mm = [&](int buf) {
#pragma unroll
            for (int c = 0; c < 5; ++c)
#pragma unroll
                for (int b = 0; b < 4; ++b) acc[b][c] = mfma(xa[buf][b], wb[buf][c], acc[b][c]);
        };
        lam_setup();
        auto colsum = [&](int buf) {
            const s16x8 ones = {0x3f80, 0x3f80, 0x3f80, 0x3f80, 0x3f80, 0x3f80, 0x3f80, 0x3f80};
            float v5[5];
#pragma unroll
            for (int c = 0; c < 5; ++c) v5[c] = mfma(ones, wb[buf][c], f32x4{0, 0, 0, 0})[0];
            if (g == 0) {
#pragma unroll
                for (int c = 0; c < 5; ++c) ((LAS float*)(smem + L_MAIL + 64))[(wn * 5 + c) * 16 + li] = v5[c];
            }
        };
        asm volatile("s_waitcnt vmcnt(0) lgkmcnt(0)" : "+v"(epoch)::"memory");
        tag = __builtin_amdgcn_readfirstlane(epoch) + 1u;
        asm volatile("" : "+s"(tag));
        bar();
        STAMP(1);
        rd(0, 0, 0);
        wait_lgkm0();
#pragma unroll 1
        for (int seg = 0, t = 0; seg < 2; ++seg) {
            const int tend = seg ? 7 : 1;
#pragma unroll 1
            for (; t < tend; ++t) {
                SB0;
                rd(t & 3, 1, 1); mm(0); interleave<20, 12>();
                SB0; wait_lgkm0(); bar();
                rd((t + 1) & 3, 0, 0); mm(1); interleave<20, 12>();
                SB0; wait_lgkm0();
            }
            if (seg == 0 && wm == 0) { if (ci & 1) colsum(1); else colsum(0); }
        }
        rd(3, 1, 1); mm(0); interleave<20, 12>();
        SB0; wait_lgkm0(); bar();
        mm(1);
    }
    STAMP(2);

    LAS f32x4* tab1 = (LAS f32x4*)(smem + L_TAB1) + (wm * 4 + g) * 8;
    LAS f32x4* tab2 = (LAS f32x4*)(smem + L_TAB2) + (wm * 4 + g) * 8;
    auto csum = [&](int k) { return ((LAS float*)(smem + L_CS))[(wn * 5 + k) * 16 + li] + ((LAS float*)(smem + L_CS))[((2 + wn) * 5 + k) * 16 + li]; };
    u32x2 xv[2][2], dv[2], bv[2];
#pragma unroll
    for (int i = 0; i < 2; ++i) {
        const int d = d0 + 32 * (wave >> 1) + 16 * i + 4 * g;
        dv[i] = *(const u32x2*)(dskip + d); bv[i] = *(const u32x2*)(b2 + d);
#pragma unroll
        for (int j = 0; j < 2; ++j) xv[i][j] = *(const u32x2*)(x + (64 * T + 32 * j + 16 * ((lane >> 3) & 1) + 8 * (wave & 1) + (lane & 7)) * DM + d);
    }
    const int Tw = 2 * ci + wm;
    const __amdgpu_buffer_rsrc_t act = __builtin_amdgcn_make_buffer_rsrc((void*)(ws_ + WS_ACT), 0, 32 * ACT_TILE, 0x00020000);
    const unsigned act0 = Tw * ACT_TILE;
    {
        f32x4 tb[8];
#pragma unroll
        for (int j = 0; j < 8; ++j) tb[j] = tab2[j];
        constexpr float C1 = -2.30220819f;
        constexpr float C2 = C1 * 0.044715f;
#pragma unroll
        for (int c = 0; c < 3; ++c) {
            const int col = 16 * (3 * wn + c) + li;
            const float s1c = csum(2 + c);
            f32x2 y[8];
#pragma unroll
            for (int j = 0; j < 8; ++j) {
                const int b = j >> 1, r = 2 * (j & 1);
                const f32x2 a = {tb[j][0], tb[j][1]}, bb = {tb[j][2], tb[j][3]};
                const f32x2 z = pfma(a, f32x2{acc[b][2 + c][r], acc[b][2 + c][r + 1]}, pfma(bb, splat(s1c), splat(b1c[c])));
                const f32x2 u = z * pfma(z * z, splat(C2), splat(C1));
                const f32x2 d = f32x2{__builtin_amdgcn_exp2f(u[0]), __builtin_amdgcn_exp2f(u[1])} + splat(1.0f);
                y[j] = z * f32x2{__builtin_amdgcn_rcpf(d[0]), __builtin_amdgcn_rcpf(d[1])};
            }
            if (3 * wn + c < nfu) {
                const int k = f0 + col;
                const unsigned dst = act0 + (k >> 3) * 1024 + (k & 7) * 64 + 16 * g;
#pragma unroll
                for (int sgm = 0; sgm < 2; ++sgm)
                    __builtin_amdgcn_raw_buffer_store_b128(u32x4{pk_bf16(y[0][sgm], y[1][sgm]), pk_bf16(y[2][sgm], y[3][sgm]), pk_bf16(y[4][sgm], y[5][sgm]), pk_bf16(y[6][sgm], y[7][sgm])},
                                                           act, dst + 512 * sgm, 0, ST_AUX);
            }
        }
    }
    STAMP(3);

    f32x2 ure[8], uim[8];
    {
        const float sbr = csum(0), sbi = csum(1);
#pragma unroll
        for (int j = 0; j < 8; ++j) {
            const f32x4 tb = tab1[j];
            const f32x2 a = {tb[0], tb[1]}, bb = {tb[2], tb[3]};
            const int b = j >> 1, r = 2 * (j & 1);
            ure[j] = pfma(a, f32x2{acc[b][0][r], acc[b][0][r + 1]}, bb * splat(sbr));
            uim[j] = pfma(a, f32x2{acc[b][1][r], acc[b][1][r + 1]}, bb * splat(sbi));
        }
    }
    f32x2 hr = ure[0], hi = uim[0];
#pragma unroll
    for (int j = 1; j < 8; ++j) {
        const f32x2 tr = pfma(splat(lam.r), hr, ure[j]), ti = pfma(splat(lam.r), hi, uim[j]);
        const f32x2 nr = pfma(splat(-lam.i), hi, tr), ni = pfma(splat(lam.i), hr, ti);
        hr = nr; hi = ni;
    }
    const cf eA = cf{hr[0], hi[0]};
    const cf e = cfma(l8, eA, cf{hr[1], hi[1]});
    const int sig = 4 * wm + g;
    LAS f32x2* eL = (LAS f32x2*)(smem + L_EL);
    eL[sig * 32 + 16 * wn + li] = f32x2{e.r, e.i};
    wait_lgkm0();
    bar();
    cf Lp = cf{0.0f, 0.0f};
#pragma unroll
    for (int s = 0; s < 7; ++s) {
        const f32x2 v = eL[s * 32 + 16 * wn + li];
        const cf n = cfma(l16, Lp, cf{v[0], v[1]});
        if (s < sig) Lp = n;
    }
    wait_vm<0>();
    if (lane == 0) __hip_atomic_store((GAS unsigned*)(ws + WS_FH) + Tw * 32 + 2 * cj + wn, tag, __ATOMIC_RELAXED, __HIP_MEMORY_SCOPE_AGENT);
    GAS u32x4* eg = (GAS u32x4*)(ws + WS_EG);
    if (sig == 7) {
        const cf E = cfma(l16, Lp, e);
        store16_sc1(eg + ci * NP + p, u32x4{__float_as_uint(E.r), tag, __float_as_uint(E.i), tag});
    }
    bar();
    STAMP(4);

    const int wd = wave >> 1, wt = wave & 1;
    f32x4 o[2][2];
    cf Hin = cf{0.0f, 0.0f};
    {
#pragma unroll
        for (int j = 0; j < 2; ++j) {
            const f32x2 st = ((LAS f32x2*)(smem + L_TOK))[64 * (T & 1) + 32 * j + 16 * ((lane >> 3) & 1) + 8 * wt + (lane & 7)];
#pragma unroll
            for (int i = 0; i < 2; ++i) {
                const float xe[4] = {bf_lo(xv[i][j][0]), bf_hi(xv[i][j][0]), bf_lo(xv[i][j][1]), bf_hi(xv[i][j][1])};
                const float de[4] = {bf_lo(dv[i][0]), bf_hi(dv[i][0]), bf_lo(dv[i][1]), bf_hi(dv[i][1])};
                const float be[4] = {bf_lo(bv[i][0]), bf_hi(bv[i][0]), bf_lo(bv[i][1]), bf_hi(bv[i][1])};
#pragma unroll
                for (int k = 0; k < 4; ++k) {
                    const float xn = rbf(rbf(xe[k] - st[0]) * st[1]);
                    o[i][j][k] = xe[k] + rbf(xn * de[k]) + be[k];
                }
            }
        }
        asm volatile("" : "+v"(o[0][0]), "+v"(o[0][1]), "+v"(o[1][0]), "+v"(o[1][1]));
        const GAS u32x4* ep = eg + g * NP + p;
        u32x4 gr[4];
        auto fetch = [&]() {
            asm volatile("global_load_dwordx4 %0, %4, off sc1\n\tglobal_load_dwordx4 %1, %5, off sc1\n\t"
                         "global_load_dwordx4 %2, %6, off sc1\n\tglobal_load_dwordx4 %3, %7, off sc1"
                         : "=&v"(gr[0]), "=&v"(gr[1]), "=&v"(gr[2]), "=&v"(gr[3])
                         : "v"(ep), "v"(ep + 4 * NP), "v"(ep + 8 * NP), "v"(ep + 12 * NP) : "memory");
        };
        auto landed = [&]() { asm volatile("s_waitcnt vmcnt(0)" : "+v"(gr[0]), "+v"(gr[1]), "+v"(gr[2]), "+v"(gr[3])::"memory"); };
        auto complete = [&]() {
            bool ok = true;
#pragma unroll
            for (int i = 0; i < 4; ++i) ok &= (4 * i + g >= ci) | ((gr[i][1] == tag) & (gr[i][3] == tag));
            return __all(ok);
        };
        fetch();
        const cf l256 = cmul(l128, l128), l512 = cmul(l256, l256);
        landed();
#pragma unroll 1
        for (unsigned spins = 0; spins < SPIN_CAP && !complete(); ++spins) { __builtin_amdgcn_s_sleep(2); fetch(); landed(); }
        cf hp = cf{0.0f, 0.0f};
#pragma unroll
        for (int i = 0; i < 4; ++i) {
            const cf n = cfma(l512, hp, cf{__uint_as_float(gr[i][0]), __uint_as_float(gr[i][2])});
            if (4 * i + g < ci) hp = n;
        }
        const int rr = (ci - 1 - g) & 3;
        if (rr & 1) hp = cmul(hp, l128);
        if (rr & 2) hp = cmul(hp, l256);
        Hin = cf{sum_groups(hp.r), sum_groups(hp.i)};
    }
    STAMP(5);
    u32x4 hq[2][2];
    unsigned hdr, hdi;
    auto store_h = [&]() {
#pragma unroll
        for (int sgm = 0; sgm < 2; ++sgm) {
            __builtin_amdgcn_raw_buffer_store_b128(hq[0][sgm], act, hdr + 512 * sgm, 0, ST_AUX);
            __builtin_amdgcn_raw_buffer_store_b128(hq[1][sgm], act, hdi + 512 * sgm, 0, ST_AUX);
        }
    };
    {
        const cf cA = cfma(lsig, Hin, Lp);
        const cf cB = cfma(l8, cA, eA);
        f32x2 sr = {cA.r, cB.r}, si = {cA.i, cB.i};
        f32x2 ore[8], oim[8];
#pragma unroll
        for (int j = 0; j < 8; ++j) {
            const f32x2 tr = pfma(splat(lam.r), sr, ure[j]), ti = pfma(splat(lam.r), si, uim[j]);
            const f32x2 nr = pfma(splat(-lam.i), si, tr), ni = pfma(splat(lam.i), sr, ti);
            sr = nr; si = ni;
            ore[j] = pfma(splat(coef2.r), sr, splat(-coef2.i) * si);
            oim[j] = pfma(splat(-coef2.r), si, splat(-coef2.i) * sr);
        }
        const int kr = FF + p, kim = FF + NP + p;
        hdr = act0 + (kr >> 3) * 1024 + (kr & 7) * 64 + 16 * g;
        hdi = act0 + (kim >> 3) * 1024 + (kim & 7) * 64 + 16 * g;
#pragma unroll
        for (int sgm = 0; sgm < 2; ++sgm) {
            hq[0][sgm] = u32x4{pk_bf16(ore[0][sgm], ore[1][sgm]), pk_bf16(ore[2][sgm], ore[3][sgm]), pk_bf16(ore[4][sgm], ore[5][sgm]), pk_bf16(ore[6][sgm], ore[7][sgm])};
            hq[1][sgm] = u32x4{pk_bf16(oim[0][sgm], oim[1][sgm]), pk_bf16(oim[2][sgm], oim[3][sgm]), pk_bf16(oim[4][sgm], oim[5][sgm]), pk_bf16(oim[6][sgm], oim[7][sgm])};
        }
        if (!DEFER) store_h();
    }
    STAMP(6);

    {
        const int trb = (g >> 1) * 2080 + (g & 1) * 1056 + 64 * q + 16 * (pp >> 1) + 8 * (pp & 1);
        const int tra = trb + 512 * wt, trw = P2_IMG + trb + 512 * wd;
        const int ca0 = P2_IMG + (32 * wd + li) * 128 + 16 * (g ^ (lane & 7));
        s16x8 wf[2][2][2], af[2][2][2];
        int slot = 0;
        auto next = [&]() { slot = slot == P2_RING - P2_SLOT ? 0 : slot + P2_SLOT; };
        auto frag_a = [&](LAS char* sl, int buf, int ks, int i) { LAS char* a = sl + tra + 4160 * ks + 32 * i; af[buf][ks][i] = lds_tr(a, a + 256); };
        auto frag_w = [&](LAS char* sl, int buf, int ks, int i, bool g4) {
            if (g4) { LAS char* a = sl + trw + 4160 * ks + 32 * i; wf[buf][ks][i] = lds_tr(a, a + 256); }
            else wf[buf][ks][i] = lds_row(sl + ((ca0 ^ (64 * ks)) + 2048 * i));
        };
        auto half = [&](int rb, bool rd, bool g4, int k0, bool mm_) {
            LAS char* sl = smem + slot;
            const int ub = rb ^ 1;
#pragma unroll
            for (int ks = 0; ks < 2; ++ks) {
#pragma unroll
                for (int m = 0; m < 4; ++m) {
                    if (mm_ && ks >= k0) mfma_acc(o[m >> 1][m & 1], wf[ub][ks][m >> 1], af[ub][ks][m & 1]);
                    SB0;
                    if (rd) { if (m < 2) frag_a(sl, rb, ks, m); else frag_w(sl, rb, ks, m - 2, g4); }
                    SB0;
                }
            }
            if (rd) next();
        };
        const GAS unsigned* fsp = (const GAS unsigned*)(ws + WS_FS) + T * 32 + (lane & 31);
        const unsigned fsl = (unsigned)(size_t)smem + L_EL + 256 * wave;
        __builtin_amdgcn_s_setprio(CPRIO2);
        bar(); half(0, true, true, 0, false);
        STAMP(7);
        if (DEFER) store_h();
        half(1, true, true, 0, true);
#pragma unroll 1
        for (int seg = 0, j = 1; seg < 4; ++seg) {
            const int jend = seg == 0 ? 2 + DEFER : (seg == 1 ? 7 : (seg == 2 ? 8 : 10));
#pragma unroll 1
            for (; j < jend; ++j) {
                bar(); half(0, true, true, 0, true);
                half(1, true, true, 0, true);
            }
            if (seg == 0) {
                wait_vm<0>();
                if (lane == 0) __hip_atomic_store((GAS unsigned*)(ws + WS_FS) + Tw * 32 + 2 * cj + wn, tag, __ATOMIC_RELAXED, __HIP_MEMORY_SCOPE_AGENT);
            } else if (seg == 1) {
                asm volatile("s_mov_b32 m0, %0\n\ts_nop 0\n\tglobal_load_lds_dword %1, off sc1" ::"s"(fsl), "v"(fsp) : "memory");
            } else if (seg == 2) {
                unsigned fsv;
                asm volatile("s_waitcnt vmcnt(0)\n\tds_read_b32 %0, %1\n\ts_waitcnt lgkmcnt(0)" : "=&v"(fsv) : "v"(fsl + 4 * lane) : "memory");
                if (!__all(fsv == tag)) wait_flags((const GAS unsigned*)(ws + WS_FS) + T * 32, tag);
            }
            SB0;
        }
        bar(); half(0, true, true, 0, true);
        half(1, true, true, 0, true);
        bar(); half(0, true, false, 1, true);
        half(1, true, false, 0, true);
#pragma unroll 1
        for (int j = 12; j < 19; ++j) {
            bar(); half(0, true, false, 0, true);
            half(1, true, false, 0, true);
        }
        half(0, false, false, 0, true);
        asm volatile("s_nop 7\n\ts_nop 7" : "+v"(o[0][0]), "+v"(o[0][1]), "+v"(o[1][0]), "+v"(o[1][1]));
        STAMP(8);

#pragma unroll
        for (int j = 0; j < 2; ++j)
#pragma unroll
            for (int i = 0; i < 2; ++i)
                *(f32x4*)(out + (64 * T + 32 * j + 16 * ((lane >> 3) & 1) + 8 * wt + (lane & 7)) * DM + d0 + 32 * wd + 16 * i + 4 * g) = o[i][j];
        if (ci == 15 && cj == 0 && threadIdx.x == 0) __hip_atomic_store((GAS unsigned*)(ws + WS_EP), tag, __ATOMIC_RELAXED, __HIP_MEMORY_SCOPE_AGENT);
        STAMP(9);
    }
}

}

extern "C" void kernel_launch(void* const* d_in, const int* in_sizes, int n_in, void* d_out, int out_size, void* d_ws, size_t ws_size,
                              hipStream_t stream) {
    typedef const unsigned short* P;
    (void)hipFuncSetAttribute((const void*)k_fused, hipFuncAttributeMaxDynamicSharedMemorySize, LDS_BYTES);
    k_fused<<<256, 512, LDS_BYTES, stream>>>((P)d_in[0], (P)d_in[1], (P)d_in[2], (P)d_in[3], (P)d_in[4], (P)d_in[5], (P)d_in[6], (P)d_in[7],
                                             (P)d_in[8], (P)d_in[10], (P)d_in[11], (P)d_in[12], (P)d_in[13], (char*)d_ws, (float*)d_out);
}
```

```cpp
#include <hip/hip_runtime.h>
#include <hip/hip_bf16.h>
#include <stdint.h>
#include <type_traits>

#define LAS __attribute__((address_space(3)))
#define GAS __attribute__((address_space(1)))

typedef __attribute__((ext_vector_type(8))) short s16x8;
typedef __attribute__((ext_vector_type(4))) short s16x4;
typedef __attribute__((ext_vector_type(8))) __bf16 bf16x8;
typedef __attribute__((ext_vector_type(2))) __bf16 bf16x2;
typedef __attribute__((ext_vector_type(4))) float f32x4;
typedef __attribute__((ext_vector_type(2))) float f32x2;
typedef __attribute__((ext_vector_type(4))) unsigned u32x4;
typedef __attribute__((ext_vector_type(2))) unsigned u32x2;

#define ACT_AUX 0
#define ST_AUX 16
#define PREFETCH 1
#define WARM1 0
#define PRIO 0
#define WARMA 0
#define D4 4
#define D3 3
#define PREF2 0
#define DEFER 0
#define LPRIO2 0
#define CPRIO2 0
#define CODEW 2
#define CODE_LINES 200
#define LAMLATE 0
#define JIT 0
#define KROT 1
#define ACQ 0

namespace {

constexpr int SEQ = 2048, DM = 512, NP = 512, FF = 1376;
constexpr int KACT = FF + 2 * NP;
constexpr int ACT_TILE = KACT * 128;
constexpr unsigned WS_ACT = 0;
constexpr unsigned WS_RS = 10u << 20;
constexpr unsigned WS_EG = WS_RS + (64u << 10);
constexpr unsigned WS_FH = WS_EG + (256u << 10);
constexpr unsigned WS_FS = WS_FH + (8u << 10);
constexpr unsigned WS_CS = WS_FS + (8u << 10);
constexpr unsigned WS_EP = WS_CS + (256u << 10);
constexpr size_t WS_STAMP = 200u << 20;
constexpr float LN_EPS = 1e-5f;
constexpr unsigned SPIN_CAP = 1u << 18;

constexpr int P1_SLOT = 36 * 1024, P1_X = 0, P1_B = 16384, P1_W = 24576;
constexpr int P2_IMG = 4 * 2080, P2_SLOT = 2 * P2_IMG, P2_RING = 9 * P2_SLOT, P2_NST = 38;
constexpr int L_TAB1 = P2_RING;
constexpr int L_TAB2 = L_TAB1 + 1024;
constexpr int L_EL = L_TAB2 + 1024;
constexpr int L_CS = L_EL + 2048;
constexpr int L_TOK = L_CS + 1280;
constexpr int L_DUMMY = L_TOK + 1024;
constexpr int L_MAIL = L_DUMMY + 1024;
constexpr int LDS_BYTES = L_MAIL + 64 + 704;

#define STAMP(s) do {} while (0)

#define SB0 __builtin_amdgcn_sched_barrier(0)
template <int N> __device__ __forceinline__ void wait_vm() { asm volatile("s_waitcnt vmcnt(%0)" ::"i"(N) : "memory"); }
__device__ __forceinline__ void wait_lgkm0() { __builtin_amdgcn_s_waitcnt(0xC07F); asm volatile("" ::: "memory"); }
template <int NM, int ND, int DPM = 1, int I = 0> __device__ __forceinline__ void interleave() {
    if constexpr (I * DPM < ND && I < NM) {
        __builtin_amdgcn_sched_group_barrier(0x008, 1, 0);
        __builtin_amdgcn_sched_group_barrier(0x100, (ND - I * DPM) < DPM ? (ND - I * DPM) : DPM, 0);
        interleave<NM, ND, DPM, I + 1>();
    } else if constexpr (I < NM) {
        __builtin_amdgcn_sched_group_barrier(0x008, NM - I, 0);
    }
}
__device__ __forceinline__ void bar() {
    SB0;
    asm volatile("" ::: "memory");
    __builtin_amdgcn_s_barrier();
    asm volatile("" ::: "memory");
    SB0;
}
__device__ __forceinline__ float bf_lo(unsigned u) { return __uint_as_float(u << 16); }
__device__ __forceinline__ float bf_hi(unsigned u) { return __uint_as_float(u & 0xffff0000u); }
__device__ __forceinline__ float bf2f(unsigned short h) { return __uint_as_float((unsigned)h << 16); }
__device__ __forceinline__ unsigned pk_bf16(float a, float b) { bf16x2 v = {(__bf16)a, (__bf16)b}; return __builtin_bit_cast(unsigned, v); }
__device__ __forceinline__ float rbf(float a) { return bf_lo(pk_bf16(a, a)); }
__device__ __forceinline__ f32x2 pfma(f32x2 a, f32x2 b, f32x2 c) { return __builtin_elementwise_fma(a, b, c); }
__device__ __forceinline__ f32x2 splat(float a) { return f32x2{a, a}; }
__device__ __forceinline__ f32x4 mfma(s16x8 a, s16x8 b, f32x4 c) {
    return __builtin_amdgcn_mfma_f32_16x16x32_bf16(__builtin_bit_cast(bf16x8, a), __builtin_bit_cast(bf16x8, b), c, 0, 0, 0);
}
__device__ __forceinline__ void mfma_acc(f32x4& c, s16x8 a, s16x8 b) {
    asm volatile("v_mfma_f32_16x16x32_bf16 %0, %1, %2, %0" : "+v"(c) : "v"(a), "v"(b));
}
__device__ __forceinline__ s16x4 lds_tr1(LAS char* p) { return __builtin_amdgcn_ds_read_tr16_b64_v4i16((LAS s16x4*)p); }
__device__ __forceinline__ s16x8 cat(s16x4 a, s16x4 b) { return __builtin_shufflevector(a, b, 0, 1, 2, 3, 4, 5, 6, 7); }
__device__ __forceinline__ s16x8 lds_row(LAS char* p) { return *(LAS s16x8*)p; }
__device__ __forceinline__ s16x8 lds_tr(LAS char* p0, LAS char* p1) {
    s16x4 a = __builtin_amdgcn_ds_read_tr16_b64_v4i16((LAS s16x4*)p0);
    s16x4 b = __builtin_amdgcn_ds_read_tr16_b64_v4i16((LAS s16x4*)p1);
    return __builtin_shufflevector(a, b, 0, 1, 2, 3, 4, 5, 6, 7);
}
template <int OFF> __device__ __forceinline__ void dma1(unsigned lds, unsigned voff, const GAS char* base) {
    asm volatile("s_mov_b32 m0, %0\n\ts_nop 0\n\tglobal_load_lds_dwordx4 %1, %2 offset:%3" ::"s"(lds), "v"(voff), "s"(base), "i"(OFF) : "memory");
}
template <int OFF> __device__ __forceinline__ void dma2(unsigned lds, unsigned voff0, unsigned voff1, const GAS char* base) {
    asm volatile("s_mov_b32 m0, %0\n\ts_nop 0\n\tglobal_load_lds_dwordx4 %1, %3\n\tglobal_load_lds_dwordx4 %2, %3 offset:%4"
                 ::"s"(lds), "v"(voff0), "v"(voff1), "s"(base), "i"(OFF) : "memory");
}
__device__ __forceinline__ void touch(unsigned lds, const GAS char* a) {
    asm volatile("s_mov_b32 m0, %0\n\ts_nop 0\n\tglobal_load_lds_dword %1, off" ::"s"(lds), "v"(a) : "memory");
}
__device__ __forceinline__ void lds_st32(unsigned a, float v) { asm volatile("ds_write_b32 %0, %1" ::"v"(a), "v"(v) : "memory"); }
__device__ __forceinline__ void lds_st64(unsigned a, f32x2 v) { asm volatile("ds_write_b64 %0, %1" ::"v"(a), "v"(v) : "memory"); }
template <int CTRL, int RM> __device__ __forceinline__ float dpp_add(float v) {
    return v + __uint_as_float(__builtin_amdgcn_update_dpp(0u, __float_as_uint(v), CTRL, RM, 0xf, true));
}
__device__ __forceinline__ float wave_sum(float v) {
    v = dpp_add<0x111, 0xf>(v); v = dpp_add<0x112, 0xf>(v); v = dpp_add<0x114, 0xf>(v); v = dpp_add<0x118, 0xf>(v);
    v = dpp_add<0x142, 0xa>(v); v = dpp_add<0x143, 0xc>(v);
    return __uint_as_float(__builtin_amdgcn_readlane(__float_as_uint(v), 63));
}
__device__ __forceinline__ float sum_groups(float v) {
    const auto a = __builtin_amdgcn_permlane16_swap(__float_as_uint(v), __float_as_uint(v), false, false);
    v = __uint_as_float(a[0]) + __uint_as_float(a[1]);
    const auto b = __builtin_amdgcn_permlane32_swap(__float_as_uint(v), __float_as_uint(v), false, false);
    return __uint_as_float(b[0]) + __uint_as_float(b[1]);
}
__device__ __forceinline__ u32x4 load16_sc1(const GAS void* p) {
    u32x4 v;
    asm volatile("global_load_dwordx4 %0, %1, off sc1\n\ts_waitcnt vmcnt(0)" : "=&v"(v) : "v"(p) : "memory");
    return v;
}
__device__ __forceinline__ void store16_sc1(GAS void* p, u32x4 v) {
    asm volatile("global_store_dwordx4 %0, %1, off sc1\n\ts_nop 1" ::"v"(p), "v"(v) : "memory");
}
__device__ __forceinline__ void wait_flags(const GAS unsigned* f, unsigned tag) {
#pragma unroll 1
    for (unsigned spins = 0; spins < SPIN_CAP; ++spins) {
        const unsigned v = __hip_atomic_load(f + (threadIdx.x & 31), __ATOMIC_RELAXED, __HIP_MEMORY_SCOPE_AGENT);
        if (__all(v == tag)) break;
        __builtin_amdgcn_s_sleep(1);
    }
    asm volatile("" ::: "memory");
}
struct cf { float r, i; };
__device__ __forceinline__ cf cmul(cf a, cf b) { return cf{a.r * b.r - a.i * b.i, a.r * b.i + a.i * b.r}; }
__device__ __forceinline__ cf cfma(cf a, cf b, cf c) { return cf{fmaf(a.r, b.r, fmaf(-a.i, b.i, c.r)), fmaf(a.r, b.i, fmaf(a.i, b.r, c.i))}; }
__device__ __forceinline__ float exp_f(float x) {
    const float hi = x * 1.44269504f, lo = fmaf(x, 1.44269504f, -hi) + x * 1.92596299e-8f;
    return __builtin_amdgcn_exp2f(hi) * fmaf(lo, 0.693147181f, 1.0f);
}
__device__ __forceinline__ void sincos_f(float x, float& s, float& c) {
    const float k = rintf(x * 0.636619772f);
    float r = fmaf(k, -1.57079601e+00f, x);
    r = fmaf(k, -3.13916473e-07f, r);
    r = fmaf(k, -5.39030253e-15f, r);
    const float z = r * r;
    const float sp = fmaf(r * z, fmaf(z, fmaf(z, fmaf(z, 2.75573137e-06f, -1.98412698e-04f), 8.33333333e-03f), -1.66666667e-01f), r);
    const float cp = fmaf(z, fmaf(z, fmaf(z, fmaf(z, fmaf(z, -2.75573144e-07f, 2.48015873e-05f), -1.38888889e-03f), 4.16666667e-02f), -0.5f), 1.0f);
    const int q = (int)k;
    const float a = (q & 1) ? cp : sp, b = (q & 1) ? sp : cp;
    s = (q & 2) ? -a : a;
    c = ((q + 1) & 2) ? -b : b;
}
__device__ __forceinline__ unsigned row_std(float v) {
    const float s1 = rbf(__builtin_amdgcn_sqrtf(rbf(rbf(v) + LN_EPS)));
    const float r1 = __builtin_amdgcn_rcpf(s1);
    const float s2 = __builtin_amdgcn_sqrtf(rbf(rbf(v * r1 * r1) + LN_EPS));
    return pk_bf16(s1, s2);
}

typedef const unsigned short* __restrict__ bfp;

__global__ __launch_bounds__(512) void k_fused(bfp x, bfp lam_re, bfp lam_im, bfp bre, bfp bim, bfp cre, bfp cim, bfp log_dt, bfp dskip,
                                               bfp w1, bfp b1, bfp w2, bfp b2, char* ws_, float* __restrict__ out) {
    extern __shared__ __attribute__((aligned(16))) char smem_[];
    LAS char* smem = (LAS char*)smem_;
    GAS char* ws = (GAS char*)ws_;
    const int wave = __builtin_amdgcn_readfirstlane(threadIdx.x >> 6), lane = threadIdx.x & 63;
    const int g = lane >> 4, li = lane & 15;
    const int xg8 = blockIdx.x & 7, loc = blockIdx.x >> 3, ci = 4 * (xg8 >> 1) + (loc >> 3), cj = 8 * (xg8 & 1) + (loc & 7);
    const int t0 = 128 * ci, p0 = 32 * cj, f0 = cj < 11 ? 96 * cj : 1056 + 64 * (cj - 11), nfu = cj < 11 ? 6 : 4;
    const int T = 2 * ci + (cj >> 3), d0 = 64 * (cj & 7);
    const int rot = ((ci + 15) >> 1) & 7;
    STAMP(0);
    unsigned long long entry_pc;
    asm volatile("s_getpc_b64 %0" : "=s"(entry_pc));
    unsigned epoch;
    asm volatile("global_load_dword %0, %1, off sc1" : "=v"(epoch) : "v"(ws + WS_EP) : "memory");
    const int q = li >> 2, pp = lane & 3;

    if (__builtin_expect(wave >= 4, 1)) {
        const int lw = wave - 4;
        __builtin_amdgcn_s_setprio(PRIO);
        const unsigned lds0 = (unsigned)(size_t)smem;
        unsigned xo[4], bo[2], wo[3];
#pragma unroll
        for (int i = 0; i < 4; ++i) {
            const int R = 8 * (4 * lw + i) + (lane >> 3), ii = R & 15, bb = (R >> 4) & 3;
            const int tok = t0 + (R & 64) + 16 * (ii >> 2) + 8 * (ii & 1) + 2 * bb + ((ii >> 1) & 1);
            xo[i] = tok * 1024 + 16 * ((lane & 7) ^ (lane >> 3)) + 3072 - 1024 * i;
        }
#pragma unroll
        for (int i = 0; i < 2; ++i) {
            const int R = 8 * (2 * lw + i) + (lane >> 3);
            bo[i] = (p0 + (R & 31)) * 1024 + 16 * ((lane & 7) ^ (lane >> 3)) + 3072 - 1024 * i;
        }
#pragma unroll
        for (int i = 0; i < 3; ++i) {
            const int st = 2 * (3 * lw + i) + (lane >> 5), kg = st / 3, ns = st - 3 * kg, kr = (lane & 31) >> 2;
            const int ch = 4 * ns + ((lane & 3) ^ ((2 * kg + (kr >> 2)) & 3));
            const int f = min(f0 + 8 * ch, FF - 8);
            wo[i] = (8 * kg + kr) * (FF * 2) + f * 2 + 3072 - 1024 * i;
        }
        const GAS char* xg = (const GAS char*)x - 3072;
        const GAS char* bg = (const GAS char*)(lw < 2 ? bre : bim) - 3072;
        const GAS char* wg = (const GAS char*)w1 - 3072;
        const unsigned lx = lds0 + P1_X + lw * 4096, lb = lds0 + P1_B + lw * 2048, lwb = lds0 + P1_W + lw * 3072;
        auto piece = [&](int t, auto I) {
            constexpr int i = decltype(I)::value;
            const unsigned so = (t & 3) * P1_SLOT;
            const int kt = (t + rot) & 7;
            if constexpr (i < 4) dma1<1024 * i>(lx + so, xo[i], xg + kt * 128);
            else if constexpr (i < 6) dma1<1024 * (i - 4)>(lb + so, bo[i - 4], bg + kt * 128);
            else dma1<1024 * (i - 6)>(lwb + so, wo[i - 6], wg + (size_t)kt * (64 * FF * 2));
        };
#define PC(t, i) piece(t, std::integral_constant<int, i>{})
        auto issue = [&](int t) { PC(t, 0); PC(t, 1); PC(t, 2); PC(t, 3); PC(t, 4); PC(t, 5); PC(t, 6); PC(t, 7); PC(t, 8); };
        const GAS char* tbase;
        unsigned tstep;
        if (lw == 0) { tbase = (const GAS char*)x + (t0 + 16 * (cj & 7) + li) * 1024; tstep = 128; }
        else if (lw == 1) { const int r = 16 * (ci & 3) + li; tbase = (const GAS char*)(r < 32 ? bre : bim) + (p0 + (r & 31)) * 1024; tstep = 128; }
        else { tbase = (const GAS char*)w1 + (16 * (ci & 3) + li) * (FF * 2) + min(f0 * 2 + (lw == 2 ? 0 : 128), FF * 2 - 4); tstep = 64 * FF * 2; }
        auto warm = [&](int t) { if (WARM1) touch(lds0 + L_DUMMY + 256 * lw, tbase + ((t + rot) & 7) * tstep); };
        STAMP(6);
        if (CODEW == 1) {
            touch(lds0 + L_DUMMY + 256 * lw, (const GAS char*)(entry_pc & ~255ull) + min(((loc & 3) * 4 + lw) * 13 + min(lane, 12), CODE_LINES - 1) * 128);
        }
        issue(0); issue(1);
        warm(2 + (g & 1));
        if (CODEW == 2) {
            touch(lds0 + L_DUMMY + 256 * lw, (const GAS char*)(entry_pc & ~255ull) + min(((loc & 3) * 4 + lw) * 13 + min(lane, 12), CODE_LINES - 1) * 128);
        }
        STAMP(7);

        unsigned w2o[2], co[2], ao[2];
#pragma unroll
        for (int i = 0; i < 2; ++i) {
            const int kg = 2 * lw + i;
            w2o[i] = (8 * kg + ((lane & 31) >> 2)) * (DM * 2) + (d0 + 32 * (lane >> 5) + 8 * (lane & 3)) * 2 + 3072 - 1056 * i;
            co[i] = (d0 + 8 * kg + (lane >> 3)) * (NP * 2) + 16 * ((lane & 7) ^ (lane >> 3)) + 3072 - 1024 * i;
            ao[i] = kg * 1024 + lane * 16 + 3072 - 1056 * i;
        }
        const GAS char* ag = ws + WS_ACT + (size_t)T * ACT_TILE;
        const GAS char* w2g = (const GAS char*)w2 - 3072;
        const unsigned l2 = lds0 + lw * 2080, l2c = lds0 + P2_IMG + lw * 2048;
        auto dmaA = [&](const GAS char* src, unsigned slot) { dma2<1056>(l2 + slot, ao[0], ao[1], src - 3072); };
        auto dmaW = [&](const GAS char* src, unsigned slot) { dma2<1056>(l2 + slot + P2_IMG, w2o[0], w2o[1], src); };
        auto dmaC = [&](const GAS char* src, unsigned slot) { dma2<1024>(l2c + slot, co[0], co[1], src); };
        const int u4 = 2 * ((ci & 3) + (cj & 1)), u3 = 2 * ((ci & 3) + (cj & 1));
        auto wrap20 = [](int u) { return u >= 20 ? u - 20 : u; };
        auto preW = [&](int s) { dmaW(w2g + wrap20(u4 + s) * 65536, s * P2_SLOT); };

        const GAS char* l2a[2];
#pragma unroll
        for (int i = 0; i < 2; ++i) {
            const int n = 456 * (ci & 3) + 114 * lw + min(64 * i + lane, 113);
            const int m = n - 800;
            l2a[i] = n < 800 ? (const GAS char*)w2 + (576 + n) * 1024 + d0 * 2
                             : (const GAS char*)(m < 512 ? cre : cim) + (d0 + ((m >> 3) & 63)) * 1024 + (m & 7) * 128;
        }
        const int n1 = 75 * blockIdx.x + 19 * lw + min(lane, 18);
        const GAS char* l1a = n1 < 11008 ? (const GAS char*)w2 + n1 * 128
                                         : (n1 < 15104 ? (const GAS char*)cre + (n1 - 11008) * 128 : (const GAS char*)cim + (min(n1, 19199) - 15104) * 128);
        const GAS u32x4* rsp = (const GAS u32x4*)(ws + WS_RS) + t0 + 64 * (lw & 1) + lane;
        const GAS u32x4* csp = (const GAS u32x4*)(ws + WS_CS) + ((cj * 16 + 8 * (lw >> 1) + 2 * g) * 2 + (lw & 1)) * 32 + li;
        asm volatile("" : "+v"(l2a[0]), "+v"(l2a[1]), "+v"(l1a), "+v"(rsp), "+v"(csp));
        asm volatile("s_waitcnt vmcnt(%1)" : "+v"(epoch) : "i"(9 + WARM1 + (CODEW == 2)) : "memory");
        unsigned tag = __builtin_amdgcn_readfirstlane(epoch) + 1u;
        asm volatile("" : "+s"(tag));
        bar();
        STAMP(1);
        issue(2); warm(4);
        if (PREFETCH) touch(lds0 + L_DUMMY + 256 * lw, l1a);
        wait_vm<9 + 2 * WARM1 + PREFETCH>(); bar();
        issue(3); warm(5);
        {
            f32x2 v;
            asm volatile("ds_read_b64 %0, %1\n\ts_waitcnt lgkmcnt(0)" : "=&v"(v) : "v"(lds0 + L_MAIL + 16 * lw + 8 * (lane & 1)) : "memory");
            if (lane < 2) store16_sc1((GAS u32x4*)(ws + WS_RS) + t0 + 8 * cj + 2 * lw + lane, u32x4{__float_as_uint(v[0]), tag, __float_as_uint(v[1]), tag});
        }
        wait_vm<10 + 2 * WARM1 + PREFETCH>(); bar();
        issue(4); warm(6);
        {
            float a, b, c;
            const unsigned ma = lds0 + L_MAIL + 64 + 4 * (((lw >> 1) * 5 + 3 * (lw & 1)) * 16 + li);
            asm volatile("ds_read_b32 %0, %3\n\tds_read_b32 %1, %3 offset:64\n\tds_read_b32 %2, %3 offset:128\n\ts_waitcnt lgkmcnt(0)"
                         : "=&v"(a), "=&v"(b), "=&v"(c) : "v"(ma) : "memory");
            if (g == 0) store16_sc1((GAS u32x4*)(ws + WS_CS) + ((cj * 16 + ci) * 2 + (lw >> 1)) * 32 + 16 * (lw & 1) + li,
                                    u32x4{__float_as_uint(a), __float_as_uint(b), __float_as_uint(c), tag});
        }
        wait_vm<11 + 2 * WARM1>(); bar();
        issue(5); warm(7); wait_vm<10 + 2 * WARM1>(); bar();
        issue(6); wait_vm<9 + WARM1>(); bar();
        issue(7);
        u32x4 rs, cs[4];
        auto gather = [&]() {
            asm volatile("global_load_dwordx4 %0, %5, off sc1\n\t"
                         "global_load_dwordx4 %1, %6, off sc1\n\tglobal_load_dwordx4 %2, %6, off offset:256 sc1\n\t"
                         "global_load_dwordx4 %3, %6, off offset:1024 sc1\n\tglobal_load_dwordx4 %4, %6, off offset:1280 sc1"
                         : "=&v"(rs), "=&v"(cs[0]), "=&v"(cs[1]), "=&v"(cs[2]), "=&v"(cs[3]) : "v"(rsp), "v"(csp) : "memory");
        };
        gather();
        wait_vm<14>(); bar();
        preW(0); preW(1); preW(2); preW(3);
        if (PREF2 == 1) { touch(lds0 + L_DUMMY + 256 * lw, l2a[0]); touch(lds0 + L_DUMMY + 256 * lw, l2a[1]); }
        asm volatile("s_waitcnt vmcnt(%5)" : "+v"(rs), "+v"(cs[0]), "+v"(cs[1]), "+v"(cs[2]), "+v"(cs[3]) : "i"(PREF2 == 1 ? 10 : 8) : "memory");
        bar();
        preW(4); preW(5);
        auto fresh = [&]() {
            return __all(rs[1] == tag && rs[3] == tag && cs[0][3] == tag && cs[1][3] == tag && cs[2][3] == tag && cs[3][3] == tag);
        };
#pragma unroll 1
        for (unsigned spins = 0; spins < SPIN_CAP && !fresh(); ++spins) {
            gather();
            asm volatile("s_waitcnt vmcnt(0)" : "+v"(rs), "+v"(cs[0]), "+v"(cs[1]), "+v"(cs[2]), "+v"(cs[3])::"memory");
        }
        if (lw < 2) {
            const float m = __uint_as_float(rs[0]), mb = rbf(m);
            const float r1 = __builtin_amdgcn_rcpf(bf_lo(rs[2])), r12 = r1 * __builtin_amdgcn_rcpf(bf_hi(rs[2]));
            const int tau = 64 * lw + lane;
            const unsigned ta = lds0 + L_TAB1 + 4 * (((tau >> 4) * 8 + (tau & 7)) * 4 + ((tau >> 3) & 1));
            lds_st32(ta, r1); lds_st32(ta + 8, -r1 * mb);
            lds_st32(ta + 1024, r12); lds_st32(ta + 1032, -r12 * m);
            lds_st64(lds0 + L_TOK + 8 * tau, f32x2{mb, r1});
        }
        {
            float v5[5];
#pragma unroll
            for (int k = 0; k < 3; ++k) v5[k] = __uint_as_float(cs[0][k]) + __uint_as_float(cs[2][k]);
#pragma unroll
            for (int k = 0; k < 2; ++k) v5[3 + k] = __uint_as_float(cs[1][k]) + __uint_as_float(cs[3][k]);
#pragma unroll
            for (int k = 0; k < 5; ++k) v5[k] = sum_groups(v5[k]);
            if (g == 0) {
#pragma unroll
                for (int k = 0; k < 5; ++k) lds_st32(lds0 + L_CS + 4 * ((((lw >> 1) * 2 + (lw & 1)) * 5 + k) * 16 + li), v5[k]);
            }
        }
        asm volatile("s_waitcnt lgkmcnt(0)" ::: "memory");
        bar();
        preW(6); preW(7); preW(8);
        STAMP(2);
        if (PREF2 == 2) { touch(lds0 + L_DUMMY + 256 * lw, l2a[0]); touch(lds0 + L_DUMMY + 256 * lw, l2a[1]); }
        bar();
        bar();
        if (PREF2 == 3) { touch(lds0 + L_DUMMY + 256 * lw, l2a[0]); touch(lds0 + L_DUMMY + 256 * lw, l2a[1]); }
        __builtin_amdgcn_s_setprio(LPRIO2);
        wait_flags((const GAS unsigned*)(ws + WS_FH) + T * 32, tag);
        STAMP(3);
        auto preA = [&](int st) { dmaA(ag + wrap20(u4 + st) * 8192, st * P2_SLOT); };
        preA(0); preA(1); preA(2); preA(3);
        wait_vm<4>(); bar();
        STAMP(4);
        preA(4); preA(5); preA(6); preA(7);
        wait_vm<8>(); bar();
        int u = wrap20(u4 + 9);
        unsigned slot = 0;
        auto nxt = [&]() { slot = slot == P2_RING - P2_SLOT ? 0 : slot + P2_SLOT; };
        auto stW = [&]() { dmaA(ag + u * 8192, slot); dmaW(w2g + u * 65536, slot); u = u == 19 ? 0 : u + 1; nxt(); };
        auto stC = [&]() {
            dmaA(ag + (FF * 128 + u * 8192), slot);
            dmaC((const GAS char*)(u < 8 ? cre : cim) - 3072 + (u & 7) * 128, slot);
            u = (u + 1) & 15; nxt();
        };
        preA(8); stW();
        wait_vm<10>(); bar(); stW(); stW();
        wait_vm<14>(); bar(); stW(); stW();
#pragma unroll 1
        for (int j = 4; j < 7; ++j) { wait_vm<16>(); bar(); stW(); stW(); }
        wait_vm<16>(); bar();
        dmaA(ag + 20 * 8192, slot); dmaW(w2g + 20 * 65536, slot); nxt();
        dmaA(ag + 21 * 8192 - 4096, slot); dmaW(w2g + 21 * 65536 - 32768, slot); nxt();
        u = u3;
        wait_vm<16>(); bar();
        stC(); stC();
#pragma unroll 1
        for (int j = 9; j < 16; ++j) { wait_vm<16>(); bar(); stC(); stC(); }
        wait_vm<16>(); bar();
        wait_vm<8>(); bar();
        wait_vm<0>(); bar();
        STAMP(5);
        return;
    }

    const int wm = wave >> 1, wn = wave & 1;
    const int p = p0 + 16 * wn + li;
    cf lam = {0.0f, 0.0f}, l8 = lam, l16 = lam, l128 = lam, lsig = lam, coef2 = lam;
    float b1c[3], p_lr, p_lim, p_ldt;
    unsigned tag;
    {
        u32x4 xr[2];
        unsigned short pr[6];
#pragma unroll
        for (int i = 0; i < 2; ++i) xr[i] = *(const u32x4*)(x + (t0 + 8 * cj + 2 * wave + i) * DM + lane * 8);
        pr[0] = lam_re[p]; pr[1] = lam_im[p]; pr[2] = log_dt[p];
#pragma unroll
        for (int c = 0; c < 3; ++c) pr[3 + c] = b1[min(f0 + 16 * (3 * wn + c) + li, FF - 1)];
        SB0;
        p_lr = bf2f(pr[0]); p_lim = bf2f(pr[1]); p_ldt = bf2f(pr[2]);
#pragma unroll
        for (int c = 0; c < 3; ++c) b1c[c] = bf2f(pr[3 + c]);
        float mean[2], var[2];
#pragma unroll
        for (int i = 0; i < 2; ++i) {
            float e[8], s = 0.0f;
#pragma unroll
            for (int j = 0; j < 4; ++j) { e[2 * j] = bf_lo(xr[i][j]); e[2 * j + 1] = bf_hi(xr[i][j]); s += e[2 * j] + e[2 * j + 1]; }
            mean[i] = wave_sum(s) * (1.0f / DM);
            float qq = 0.0f;
#pragma unroll
            for (int j = 0; j < 8; ++j) qq = fmaf(e[j] - mean[i], e[j] - mean[i], qq);
            var[i] = wave_sum(qq) * (1.0f / DM);
        }
        if (lane < 2) ((LAS f32x2*)(smem + L_MAIL))[2 * wave + lane] = f32x2{lane ? mean[1] : mean[0], __uint_as_float(row_std(lane ? var[1] : var[0]))};
        asm volatile("" : "+v"(b1c[0]), "+v"(b1c[1]), "+v"(b1c[2]), "+v"(p_lr), "+v"(p_lim), "+v"(p_ldt));
    }
    auto lam_setup = [&]() {
        const float lr = p_lr, lim = p_lim, dt = rbf(exp_f(p_ldt));
        const float mag = exp_f(dt * lr);
        float sn, cs;
        sincos_f(dt * lim, sn, cs);
        lam = cf{mag * cs, mag * sn};
        const float nr = lam.r - 1.0f, ni = lam.i, den = 1.0f / (lr * lr + lim * lim);
        coef2 = cf{2.0f * (nr * lr + ni * lim) * den, 2.0f * (ni * lr - nr * lim) * den};
        const cf l2 = cmul(lam, lam), l4 = cmul(l2, l2);
        l8 = cmul(l4, l4); l16 = cmul(l8, l8);
        const cf l32 = cmul(l16, l16), l64 = cmul(l32, l32);
        l128 = cmul(l64, l64);
        lsig = (g & 1) ? l16 : cf{1.0f, 0.0f};
        if (g & 2) lsig = cmul(lsig, l32);
        if (wm) lsig = cmul(lsig, l64);
        asm volatile("" : "+v"(lam.r), "+v"(lam.i), "+v"(l8.r), "+v"(l8.i), "+v"(l16.r), "+v"(l16.i), "+v"(l128.r), "+v"(l128.i), "+v"(lsig.r), "+v"(lsig.i),
                          "+v"(coef2.r), "+v"(coef2.i));
    };
    f32x4 acc[4][5];
    {
        const int sw = 16 * (g ^ (lane & 7));
        const int xa0 = P1_X + (64 * wm + li) * 128 + sw;
        const int ba0 = P1_B + (16 * wn + li) * 128 + sw;
        int wa[2][2];
#pragma unroll
        for (int t = 0; t < 2; ++t)
#pragma unroll
            for (int cl = 0; cl < 2; ++cl) {
                const int par = (3 * wn + cl) & 1;
                wa[t][cl] = P1_W + 1536 * g + 64 * q + 256 * t + 32 * (par ^ (g & 1)) + 16 * ((pp >> 1) ^ t) + 8 * (pp & 1) + (cl == 0 ? 512 * wn : 1024 * wn);
            }
#pragma unroll
        for (int b = 0; b < 4; ++b)
#pragma unroll
            for (int c = 0; c < 5; ++c) acc[b][c] = f32x4{0, 0, 0, 0};
        s16x8 xa[2][4], wb[2][5];
        auto rd = [&](int slot, int ks, int buf) {
            LAS char* s = smem + slot * P1_SLOT;
            LAS char* xs = s + (xa0 ^ (64 * ks));
            LAS char* bs = s + (ba0 ^ (64 * ks));
#pragma unroll
            for (int b = 0; b < 4; ++b) xa[buf][b] = lds_row(xs + b * 2048);
            wb[buf][0] = lds_row(bs);
            wb[buf][1] = lds_row(bs + 4096);
            wb[buf][2] = lds_tr(s + wa[0][0] + 6144 * ks, s + wa[1][0] + 6144 * ks);
            wb[buf][3] = lds_tr(s + wa[0][1] + 6144 * ks, s + wa[1][1] + 6144 * ks);
            wb[buf][4] = lds_tr(s + wa[0][0] + 6144 * ks + 512, s + wa[1][0] + 6144 * ks + 512);
        };
        auto mm = [&](int buf) {
#pragma unroll
            for (int c = 0; c < 5; ++c)
#pragma unroll
                for (int b = 0; b < 4; ++b) acc[b][c] = mfma(xa[buf][b], wb[buf][c], acc[b][c]);
        };
        lam_setup();
        auto colsum = [&](int buf) {
            const s16x8 ones = {0x3f80, 0x3f80, 0x3f80, 0x3f80, 0x3f80, 0x3f80, 0x3f80, 0x3f80};
            float v5[5];
#pragma unroll
            for (int c = 0; c < 5; ++c) v5[c] = mfma(ones, wb[buf][c], f32x4{0, 0, 0, 0})[0];
            if (g == 0) {
#pragma unroll
                for (int c = 0; c < 5; ++c) ((LAS float*)(smem + L_MAIL + 64))[(wn * 5 + c) * 16 + li] = v5[c];
            }
        };
        asm volatile("s_waitcnt vmcnt(0) lgkmcnt(0)" : "+v"(epoch)::"memory");
        tag = __builtin_amdgcn_readfirstlane(epoch) + 1u;
        asm volatile("" : "+s"(tag));
        bar();
        STAMP(1);
        rd(0, 0, 0);
        wait_lgkm0();
#pragma unroll 1
        for (int seg = 0, t = 0; seg < 2; ++seg) {
            const int tend = seg ? 7 : 1;
#pragma unroll 1
            for (; t < tend; ++t) {
                SB0;
                rd(t & 3, 1, 1); mm(0); interleave<20, 12>();
                SB0; wait_lgkm0(); bar();
                rd((t + 1) & 3, 0, 0); mm(1); interleave<20, 12>();
                SB0; wait_lgkm0();
            }
            if (seg == 0 && wm == 0) { if (ci & 1) colsum(1); else colsum(0); }
        }
        rd(3, 1, 1); mm(0); interleave<20, 12>();
        SB0; wait_lgkm0(); bar();
        mm(1);
    }
    STAMP(2);

    LAS f32x4* tab1 = (LAS f32x4*)(smem + L_TAB1) + (wm * 4 + g) * 8;
    LAS f32x4* tab2 = (LAS f32x4*)(smem + L_TAB2) + (wm * 4 + g) * 8;
    auto csum = [&](int k) { return ((LAS float*)(smem + L_CS))[(wn * 5 + k) * 16 + li] + ((LAS float*)(smem + L_CS))[((2 + wn) * 5 + k) * 16 + li]; };
    u32x2 xv[2][2], dv[2], bv[2];
#pragma unroll
    for (int i = 0; i < 2; ++i) {
        const int d = d0 + 32 * (wave >> 1) + 16 * i + 4 * g;
        dv[i] = *(const u32x2*)(dskip + d); bv[i] = *(const u32x2*)(b2 + d);
#pragma unroll
        for (int j = 0; j < 2; ++j) xv[i][j] = *(const u32x2*)(x + (64 * T + 32 * j + 16 * ((lane >> 3) & 1) + 8 * (wave & 1) + (lane & 7)) * DM + d);
    }
    const int Tw = 2 * ci + wm;
    const __amdgpu_buffer_rsrc_t act = __builtin_amdgcn_make_buffer_rsrc((void*)(ws_ + WS_ACT), 0, 32 * ACT_TILE, 0x00020000);
    const unsigned act0 = Tw * ACT_TILE;
    {
        f32x4 tb[8];
#pragma unroll
        for (int j = 0; j < 8; ++j) tb[j] = tab2[j];
        constexpr float C1 = -2.30220819f;
        constexpr float C2 = C1 * 0.044715f;
#pragma unroll
        for (int c = 0; c < 3; ++c) {
            const int col = 16 * (3 * wn + c) + li;
            const float s1c = csum(2 + c);
            f32x2 y[8];
#pragma unroll
            for (int j = 0; j < 8; ++j) {
                const int b = j >> 1, r = 2 * (j & 1);
                const f32x2 a = {tb[j][0], tb[j][1]}, bb = {tb[j][2], tb[j][3]};
                const f32x2 z = pfma(a, f32x2{acc[b][2 + c][r], acc[b][2 + c][r + 1]}, pfma(bb, splat(s1c), splat(b1c[c])));
                const f32x2 u = z * pfma(z * z, splat(C2), splat(C1));
                const f32x2 d = f32x2{__builtin_amdgcn_exp2f(u[0]), __builtin_amdgcn_exp2f(u[1])} + splat(1.0f);
                y[j] = z * f32x2{__builtin_amdgcn_rcpf(d[0]), __builtin_amdgcn_rcpf(d[1])};
            }
            if (3 * wn + c < nfu) {
                const int k = f0 + col;
                const unsigned dst = act0 + (k >> 3) * 1024 + (k & 7) * 64 + 16 * g;
#pragma unroll
                for (int sgm = 0; sgm < 2; ++sgm)
                    __builtin_amdgcn_raw_buffer_store_b128(u32x4{pk_bf16(y[0][sgm], y[1][sgm]), pk_bf16(y[2][sgm], y[3][sgm]), pk_bf16(y[4][sgm], y[5][sgm]), pk_bf16(y[6][sgm], y[7][sgm])},
                                                           act, dst + 512 * sgm, 0, ST_AUX);
            }
        }
    }
    STAMP(3);

    f32x2 ure[8], uim[8];
    {
        const float sbr = csum(0), sbi = csum(1);
#pragma unroll
        for (int j = 0; j < 8; ++j) {
            const f32x4 tb = tab1[j];
            const f32x2 a = {tb[0], tb[1]}, bb = {tb[2], tb[3]};
            const int b = j >> 1, r = 2 * (j & 1);
            ure[j] = pfma(a, f32x2{acc[b][0][r], acc[b][0][r + 1]}, bb * splat(sbr));
            uim[j] = pfma(a, f32x2{acc[b][1][r], acc[b][1][r + 1]}, bb * splat(sbi));
        }
    }
    f32x2 hr = ure[0], hi = uim[0];
#pragma unroll
    for (int j = 1; j < 8; ++j) {
        const f32x2 tr = pfma(splat(lam.r), hr, ure[j]), ti = pfma(splat(lam.r), hi, uim[j]);
        const f32x2 nr = pfma(splat(-lam.i), hi, tr), ni = pfma(splat(lam.i), hr, ti);
        hr = nr; hi = ni;
    }
    const cf eA = cf{hr[0], hi[0]};
    const cf e = cfma(l8, eA, cf{hr[1], hi[1]});
    const int sig = 4 * wm + g;
    LAS f32x2* eL = (LAS f32x2*)(smem + L_EL);
    eL[sig * 32 + 16 * wn + li] = f32x2{e.r, e.i};
    wait_lgkm0();
    bar();
    cf Lp = cf{0.0f, 0.0f};
#pragma unroll
    for (int s = 0; s < 7; ++s) {
        const f32x2 v = eL[s * 32 + 16 * wn + li];
        const cf n = cfma(l16, Lp, cf{v[0], v[1]});
        if (s < sig) Lp = n;
    }
    wait_vm<0>();
    if (lane == 0) __hip_atomic_store((GAS unsigned*)(ws + WS_FH) + Tw * 32 + 2 * cj + wn, tag, __ATOMIC_RELAXED, __HIP_MEMORY_SCOPE_AGENT);
    GAS u32x4* eg = (GAS u32x4*)(ws + WS_EG);
    if (sig == 7) {
        const cf E = cfma(l16, Lp, e);
        store16_sc1(eg + ci * NP + p, u32x4{__float_as_uint(E.r), tag, __float_as_uint(E.i), tag});
    }
    bar();
    STAMP(4);

    const int wd = wave >> 1, wt = wave & 1;
    f32x4 o[2][2];
    cf Hin = cf{0.0f, 0.0f};
    {
#pragma unroll
        for (int j = 0; j < 2; ++j) {
            const f32x2 st = ((LAS f32x2*)(smem + L_TOK))[64 * (T & 1) + 32 * j + 16 * ((lane >> 3) & 1) + 8 * wt + (lane & 7)];
#pragma unroll
            for (int i = 0; i < 2; ++i) {
                const float xe[4] = {bf_lo(xv[i][j][0]), bf_hi(xv[i][j][0]), bf_lo(xv[i][j][1]), bf_hi(xv[i][j][1])};
                const float de[4] = {bf_lo(dv[i][0]), bf_hi(dv[i][0]), bf_lo(dv[i][1]), bf_hi(dv[i][1])};
                const float be[4] = {bf_lo(bv[i][0]), bf_hi(bv[i][0]), bf_lo(bv[i][1]), bf_hi(bv[i][1])};
#pragma unroll
                for (int k = 0; k < 4; ++k) {
                    const float xn = rbf(rbf(xe[k] - st[0]) * st[1]);
                    o[i][j][k] = xe[k] + rbf(xn * de[k]) + be[k];
                }
            }
        }
        asm volatile("" : "+v"(o[0][0]), "+v"(o[0][1]), "+v"(o[1][0]), "+v"(o[1][1]));
        const GAS u32x4* ep = eg + g * NP + p;
        u32x4 gr[4];
        auto fetch = [&]() {
            asm volatile("global_load_dwordx4 %0, %4, off sc1\n\tglobal_load_dwordx4 %1, %5, off sc1\n\t"
                         "global_load_dwordx4 %2, %6, off sc1\n\tglobal_load_dwordx4 %3, %7, off sc1"
                         : "=&v"(gr[0]), "=&v"(gr[1]), "=&v"(gr[2]), "=&v"(gr[3])
                         : "v"(ep), "v"(ep + 4 * NP), "v"(ep + 8 * NP), "v"(ep + 12 * NP) : "memory");
        };
        auto landed = [&]() { asm volatile("s_waitcnt vmcnt(0)" : "+v"(gr[0]), "+v"(gr[1]), "+v"(gr[2]), "+v"(gr[3])::"memory"); };
        auto complete = [&]() {
            bool ok = true;
#pragma unroll
            for (int i = 0; i < 4; ++i) ok &= (4 * i + g >= ci) | ((gr[i][1] == tag) & (gr[i][3] == tag));
            return __all(ok);
        };
        fetch();
        const cf l256 = cmul(l128, l128), l512 = cmul(l256, l256);
        landed();
#pragma unroll 1
        for (unsigned spins = 0; spins < SPIN_CAP && !complete(); ++spins) { __builtin_amdgcn_s_sleep(2); fetch(); landed(); }
        cf hp = cf{0.0f, 0.0f};
#pragma unroll
        for (int i = 0; i < 4; ++i) {
            const cf n = cfma(l512, hp, cf{__uint_as_float(gr[i][0]), __uint_as_float(gr[i][2])});
            if (4 * i + g < ci) hp = n;
        }
        const int rr = (ci - 1 - g) & 3;
        if (rr & 1) hp = cmul(hp, l128);
        if (rr & 2) hp = cmul(hp, l256);
        Hin = cf{sum_groups(hp.r), sum_groups(hp.i)};
    }
    STAMP(5);
    u32x4 hq[2][2];
    unsigned hdr, hdi;
    auto store_h = [&]() {
#pragma unroll
        for (int sgm = 0; sgm < 2; ++sgm) {
            __builtin_amdgcn_raw_buffer_store_b128(hq[0][sgm], act, hdr + 512 * sgm, 0, ST_AUX);
            __builtin_amdgcn_raw_buffer_store_b128(hq[1][sgm], act, hdi + 512 * sgm, 0, ST_AUX);
        }
    };
    {
        const cf cA = cfma(lsig, Hin, Lp);
        const cf cB = cfma(l8, cA, eA);
        f32x2 sr = {cA.r, cB.r}, si = {cA.i, cB.i};
        f32x2 ore[8], oim[8];
#pragma unroll
        for (int j = 0; j < 8; ++j) {
            const f32x2 tr = pfma(splat(lam.r), sr, ure[j]), ti = pfma(splat(lam.r), si, uim[j]);
            const f32x2 nr = pfma(splat(-lam.i), si, tr), ni = pfma(splat(lam.i), sr, ti);
            sr = nr; si = ni;
            ore[j] = pfma(splat(coef2.r), sr, splat(-coef2.i) * si);
            oim[j] = pfma(splat(-coef2.r), si, splat(-coef2.i) * sr);
        }
        const int kr = FF + p, kim = FF + NP + p;
        hdr = act0 + (kr >> 3) * 1024 + (kr & 7) * 64 + 16 * g;
        hdi = act0 + (kim >> 3) * 1024 + (kim & 7) * 64 + 16 * g;
#pragma unroll
        for (int sgm = 0; sgm < 2; ++sgm) {
            hq[0][sgm] = u32x4{pk_bf16(ore[0][sgm], ore[1][sgm]), pk_bf16(ore[2][sgm], ore[3][sgm]), pk_bf16(ore[4][sgm], ore[5][sgm]), pk_bf16(ore[6][sgm], ore[7][sgm])};
            hq[1][sgm] = u32x4{pk_bf16(oim[0][sgm], oim[1][sgm]), pk_bf16(oim[2][sgm], oim[3][sgm]), pk_bf16(oim[4][sgm], oim[5][sgm]), pk_bf16(oim[6][sgm], oim[7][sgm])};
        }
        if (!DEFER) store_h();
    }
    STAMP(6);

    {
        const int trb = (g >> 1) * 2080 + (g & 1) * 1056 + 64 * q + 16 * (pp >> 1) + 8 * (pp & 1);
        const int tra = trb + 512 * wt, trw = P2_IMG + trb + 512 * wd;
        const int ca0 = P2_IMG + (32 * wd + li) * 128 + 16 * (g ^ (lane & 7));
        s16x8 wf[2][2][2], af[2][2][2];
        int slot = 0;
        auto next = [&]() { slot = slot == P2_RING - P2_SLOT ? 0 : slot + P2_SLOT; };
        auto frag_a = [&](LAS char* sl, int buf, int ks, int i) { LAS char* a = sl + tra + 4160 * ks + 32 * i; af[buf][ks][i] = lds_tr(a, a + 256); };
        auto frag_w = [&](LAS char* sl, int buf, int ks, int i, bool g4) {
            if (g4) { LAS char* a = sl + trw + 4160 * ks + 32 * i; wf[buf][ks][i] = lds_tr(a, a + 256); }
            else wf[buf][ks][i] = lds_row(sl + ((ca0 ^ (64 * ks)) + 2048 * i));
        };
        auto half = [&](int rb, bool rd, bool g4, int k0, bool mm_) {
            LAS char* sl = smem + slot;
            const int ub = rb ^ 1;
#pragma unroll
            for (int ks = 0; ks < 2; ++ks) {
#pragma unroll
                for (int m = 0; m < 4; ++m) {
                    if (mm_ && ks >= k0) mfma_acc(o[m >> 1][m & 1], wf[ub][ks][m >> 1], af[ub][ks][m & 1]);
                    SB0;
                    if (rd) { if (m < 2) frag_a(sl, rb, ks, m); else frag_w(sl, rb, ks, m - 2, g4); }
                    SB0;
                }
            }
            if (rd) next();
        };
        const GAS unsigned* fsp = (const GAS unsigned*)(ws + WS_FS) + T * 32 + (lane & 31);
        const unsigned fsl = (unsigned)(size_t)smem + L_EL + 256 * wave;
        __builtin_amdgcn_s_setprio(CPRIO2);
        bar(); half(0, true, true, 0, false);
        STAMP(7);
        if (DEFER) store_h();
        half(1, true, true, 0, true);
#pragma unroll 1
        for (int seg = 0, j = 1; seg < 4; ++seg) {
            const int jend = seg == 0 ? 2 + DEFER : (seg == 1 ? 7 : (seg == 2 ? 8 : 10));
#pragma unroll 1
            for (; j < jend; ++j) {
                bar(); half(0, true, true, 0, true);
                half(1, true, true, 0, true);
            }
            if (seg == 0) {
                wait_vm<0>();
                if (lane == 0) __hip_atomic_store((GAS unsigned*)(ws + WS_FS) + Tw * 32 + 2 * cj + wn, tag, __ATOMIC_RELAXED, __HIP_MEMORY_SCOPE_AGENT);
            } else if (seg == 1) {
                asm volatile("s_mov_b32 m0, %0\n\ts_nop 0\n\tglobal_load_lds_dword %1, off sc1" ::"s"(fsl), "v"(fsp) : "memory");
            } else if (seg == 2) {
                unsigned fsv;
                asm volatile("s_waitcnt vmcnt(0)\n\tds_read_b32 %0, %1\n\ts_waitcnt lgkmcnt(0)" : "=&v"(fsv) : "v"(fsl + 4 * lane) : "memory");
                if (!__all(fsv == tag)) wait_flags((const GAS unsigned*)(ws + WS_FS) + T * 32, tag);
            }
            SB0;
        }
        bar(); half(0, true, true, 0, true);
        half(1, true, true, 0, true);
        bar(); half(0, true, false, 1, true);
        half(1, true, false, 0, true);
#pragma unroll 1
        for (int j = 12; j < 19; ++j) {
            bar(); half(0, true, false, 0, true);
            half(1, true, false, 0, true);
        }
        half(0, false, false, 0, true);
        asm volatile("s_nop 7\n\ts_nop 7" : "+v"(o[0][0]), "+v"(o[0][1]), "+v"(o[1][0]), "+v"(o[1][1]));
        STAMP(8);

#pragma unroll
        for (int j = 0; j < 2; ++j)
#pragma unroll
            for (int i = 0; i < 2; ++i)
                *(f32x4*)(out + (64 * T + 32 * j + 16 * ((lane >> 3) & 1) + 8 * wt + (lane & 7)) * DM + d0 + 32 * wd + 16 * i + 4 * g) = o[i][j];
        if (ci == 15 && cj == 0 && threadIdx.x == 0) __hip_atomic_store((GAS unsigned*)(ws + WS_EP), tag, __ATOMIC_RELAXED, __HIP_MEMORY_SCOPE_AGENT);
        STAMP(9);
    }
}

}

extern "C" void kernel_launch(void* const* d_in, const int* in_sizes, int n_in, void* d_out, int out_size, void* d_ws, size_t ws_size,
                              hipStream_t stream) {
    typedef const unsigned short* P;
    (void)hipFuncSetAttribute((const void*)k_fused, hipFuncAttributeMaxDynamicSharedMemorySize, LDS_BYTES);
    k_fused<<<256, 512, LDS_BYTES, stream>>>((P)d_in[0], (P)d_in[1], (P)d_in[2], (P)d_in[3], (P)d_in[4], (P)d_in[5], (P)d_in[6], (P)d_in[7],
                                             (P)d_in[8], (P)d_in[10], (P)d_in[11], (P)d_in[12], (P)d_in[13], (char*)d_ws, (float*)d_out);
}
```

```cpp
#include <hip/hip_runtime.h>
#include <hip/hip_bf16.h>
#include <stdint.h>
#include <type_traits>

#define LAS __attribute__((address_space(3)))
#define GAS __attribute__((address_space(1)))

typedef __attribute__((ext_vector_type(8))) short s16x8;
typedef __attribute__((ext_vector_type(4))) short s16x4;
typedef __attribute__((ext_vector_type(8))) __bf16 bf16x8;
typedef __attribute__((ext_vector_type(2))) __bf16 bf16x2;
typedef __attribute__((ext_vector_type(4))) float f32x4;
typedef __attribute__((ext_vector_type(2))) float f32x2;
typedef __attribute__((ext_vector_type(4))) unsigned u32x4;
typedef __attribute__((ext_vector_type(2))) unsigned u32x2;

#define ACT_AUX 0
#define ST_AUX 16
#define PREFETCH 1
#define WARM1 0
#define PRIO 0
#define WARMA 0
#define D4 4
#define D3 3
#define PREF2 0
#define DEFER 0
#define LPRIO2 0
#define CPRIO2 0
#define CODEW 2
#define CODE_LINES 200
#define LAMLATE 0
#define JIT 0
#define KROT 0
#define PREF3 0
#define PX (PREF3 ? 1 : 0)
#define ACQ 0

namespace {

constexpr int SEQ = 2048, DM = 512, NP = 512, FF = 1376;
constexpr int KACT = FF + 2 * NP;
constexpr int ACT_TILE = KACT * 128;
constexpr unsigned WS_ACT = 0;
constexpr unsigned WS_RS = 10u << 20;
constexpr unsigned WS_EG = WS_RS + (64u << 10);
constexpr unsigned WS_FH = WS_EG + (256u << 10);
constexpr unsigned WS_FS = WS_FH + (8u << 10);
constexpr unsigned WS_CS = WS_FS + (8u << 10);
constexpr unsigned WS_EP = WS_CS + (256u << 10);
constexpr size_t WS_STAMP = 200u << 20;
constexpr float LN_EPS = 1e-5f;
constexpr unsigned SPIN_CAP = 1u << 18;

constexpr int P1_SLOT = 36 * 1024, P1_X = 0, P1_B = 16384, P1_W = 24576;
constexpr int P2_IMG = 4 * 2080, P2_SLOT = 2 * P2_IMG, P2_RING = 9 * P2_SLOT, P2_NST = 38;
constexpr int L_TAB1 = P2_RING;
constexpr int L_TAB2 = L_TAB1 + 1024;
constexpr int L_EL = L_TAB2 + 1024;
constexpr int L_CS = L_EL + 2048;
constexpr int L_TOK = L_CS + 1280;
constexpr int L_DUMMY = L_TOK + 1024;
constexpr int L_MAIL = L_DUMMY + 1024;
constexpr int LDS_BYTES = L_MAIL + 64 + 704;

#define STAMP(s) do {} while (0)

#define SB0 __builtin_amdgcn_sched_barrier(0)
template <int N> __device__ __forceinline__ void wait_vm() { asm volatile("s_waitcnt vmcnt(%0)" ::"i"(N) : "memory"); }
__device__ __forceinline__ void wait_lgkm0() { __builtin_amdgcn_s_waitcnt(0xC07F); asm volatile("" ::: "memory"); }
template <int NM, int ND, int DPM = 1, int I = 0> __device__ __forceinline__ void interleave() {
    if constexpr (I * DPM < ND && I < NM) {
        __builtin_amdgcn_sched_group_barrier(0x008, 1, 0);
        __builtin_amdgcn_sched_group_barrier(0x100, (ND - I * DPM) < DPM ? (ND - I * DPM) : DPM, 0);
        interleave<NM, ND, DPM, I + 1>();
    } else if constexpr (I < NM) {
        __builtin_amdgcn_sched_group_barrier(0x008, NM - I, 0);
    }
}
__device__ __forceinline__ void bar() {
    SB0;
    asm volatile("" ::: "memory");
    __builtin_amdgcn_s_barrier();
    asm volatile("" ::: "memory");
    SB0;
}
__device__ __forceinline__ float bf_lo(unsigned u) { return __uint_as_float(u << 16); }
__device__ __forceinline__ float bf_hi(unsigned u) { return __uint_as_float(u & 0xffff0000u); }
__device__ __forceinline__ float bf2f(unsigned short h) { return __uint_as_float((unsigned)h << 16); }
__device__ __forceinline__ unsigned pk_bf16(float a, float b) { bf16x2 v = {(__bf16)a, (__bf16)b}; return __builtin_bit_cast(unsigned, v); }
__device__ __forceinline__ float rbf(float a) { return bf_lo(pk_bf16(a, a)); }
__device__ __forceinline__ f32x2 pfma(f32x2 a, f32x2 b, f32x2 c) { return __builtin_elementwise_fma(a, b, c); }
__device__ __forceinline__ f32x2 splat(float a) { return f32x2{a, a}; }
__device__ __forceinline__ f32x4 mfma(s16x8 a, s16x8 b, f32x4 c) {
    return __builtin_amdgcn_mfma_f32_16x16x32_bf16(__builtin_bit_cast(bf16x8, a), __builtin_bit_cast(bf16x8, b), c, 0, 0, 0);
}
__device__ __forceinline__ void mfma_acc(f32x4& c, s16x8 a, s16x8 b) {
    asm volatile("v_mfma_f32_16x16x32_bf16 %0, %1, %2, %0" : "+v"(c) : "v"(a), "v"(b));
}
__device__ __forceinline__ s16x4 lds_tr1(LAS char* p) { return __builtin_amdgcn_ds_read_tr16_b64_v4i16((LAS s16x4*)p); }
__device__ __forceinline__ s16x8 cat(s16x4 a, s16x4 b) { return __builtin_shufflevector(a, b, 0, 1, 2, 3, 4, 5, 6, 7); }
__device__ __forceinline__ s16x8 lds_row(LAS char* p) { return *(LAS s16x8*)p; }
__device__ __forceinline__ s16x8 lds_tr(LAS char* p0, LAS char* p1) {
    s16x4 a = __builtin_amdgcn_ds_read_tr16_b64_v4i16((LAS s16x4*)p0);
    s16x4 b = __builtin_amdgcn_ds_read_tr16_b64_v4i16((LAS s16x4*)p1);
    return __builtin_shufflevector(a, b, 0, 1, 2, 3, 4, 5, 6, 7);
}
template <int OFF> __device__ __forceinline__ void dma1(unsigned lds, unsigned voff, const GAS char* base) {
    asm volatile("s_mov_b32 m0, %0\n\ts_nop 0\n\tglobal_load_lds_dwordx4 %1, %2 offset:%3" ::"s"(lds), "v"(voff), "s"(base), "i"(OFF) : "memory");
}
template <int OFF> __device__ __forceinline__ void dma2(unsigned lds, unsigned voff0, unsigned voff1, const GAS char* base) {
    asm volatile("s_mov_b32 m0, %0\n\ts_nop 0\n\tglobal_load_lds_dwordx4 %1, %3\n\tglobal_load_lds_dwordx4 %2, %3 offset:%4"
                 ::"s"(lds), "v"(voff0), "v"(voff1), "s"(base), "i"(OFF) : "memory");
}
__device__ __forceinline__ void touch(unsigned lds, const GAS char* a) {
    asm volatile("s_mov_b32 m0, %0\n\ts_nop 0\n\tglobal_load_lds_dword %1, off" ::"s"(lds), "v"(a) : "memory");
}
__device__ __forceinline__ void lds_st32(unsigned a, float v) { asm volatile("ds_write_b32 %0, %1" ::"v"(a), "v"(v) : "memory"); }
__device__ __forceinline__ void lds_st64(unsigned a, f32x2 v) { asm volatile("ds_write_b64 %0, %1" ::"v"(a), "v"(v) : "memory"); }
template <int CTRL, int RM> __device__ __forceinline__ float dpp_add(float v) {
    return v + __uint_as_float(__builtin_amdgcn_update_dpp(0u, __float_as_uint(v), CTRL, RM, 0xf, true));
}
__device__ __forceinline__ float wave_sum(float v) {
    v = dpp_add<0x111, 0xf>(v); v = dpp_add<0x112, 0xf>(v); v = dpp_add<0x114, 0xf>(v); v = dpp_add<0x118, 0xf>(v);
    v = dpp_add<0x142, 0xa>(v); v = dpp_add<0x143, 0xc>(v);
    return __uint_as_float(__builtin_amdgcn_readlane(__float_as_uint(v), 63));
}
__device__ __forceinline__ float sum_groups(float v) {
    const auto a = __builtin_amdgcn_permlane16_swap(__float_as_uint(v), __float_as_uint(v), false, false);
    v = __uint_as_float(a[0]) + __uint_as_float(a[1]);
    const auto b = __builtin_amdgcn_permlane32_swap(__float_as_uint(v), __float_as_uint(v), false, false);
    return __uint_as_float(b[0]) + __uint_as_float(b[1]);
}
__device__ __forceinline__ u32x4 load16_sc1(const GAS void* p) {
    u32x4 v;
    asm volatile("global_load_dwordx4 %0, %1, off sc1\n\ts_waitcnt vmcnt(0)" : "=&v"(v) : "v"(p) : "memory");
    return v;
}
__device__ __forceinline__ void store16_sc1(GAS void* p, u32x4 v) {
    asm volatile("global_store_dwordx4 %0, %1, off sc1\n\ts_nop 1" ::"v"(p), "v"(v) : "memory");
}
__device__ __forceinline__ void wait_flags(const GAS unsigned* f, unsigned tag) {
#pragma unroll 1
    for (unsigned spins = 0; spins < SPIN_CAP; ++spins) {
        const unsigned v = __hip_atomic_load(f + (threadIdx.x & 31), __ATOMIC_RELAXED, __HIP_MEMORY_SCOPE_AGENT);
        if (__all(v == tag)) break;
        __builtin_amdgcn_s_sleep(1);
    }
    asm volatile("" ::: "memory");
}
struct cf { float r, i; };
__device__ __forceinline__ cf cmul(cf a, cf b) { return cf{a.r * b.r - a.i * b.i, a.r * b.i + a.i * b.r}; }
__device__ __forceinline__ cf cfma(cf a, cf b, cf c) { return cf{fmaf(a.r, b.r, fmaf(-a.i, b.i, c.r)), fmaf(a.r, b.i, fmaf(a.i, b.r, c.i))}; }
__device__ __forceinline__ float exp_f(float x) {
    const float hi = x * 1.44269504f, lo = fmaf(x, 1.44269504f, -hi) + x * 1.92596299e-8f;
    return __builtin_amdgcn_exp2f(hi) * fmaf(lo, 0.693147181f, 1.0f);
}
__device__ __forceinline__ void sincos_f(float x, float& s, float& c) {
    const float k = rintf(x * 0.636619772f);
    float r = fmaf(k, -1.57079601e+00f, x);
    r = fmaf(k, -3.13916473e-07f, r);
    r = fmaf(k, -5.39030253e-15f, r);
    const float z = r * r;
    const float sp = fmaf(r * z, fmaf(z, fmaf(z, fmaf(z, 2.75573137e-06f, -1.98412698e-04f), 8.33333333e-03f), -1.66666667e-01f), r);
    const float cp = fmaf(z, fmaf(z, fmaf(z, fmaf(z, fmaf(z, -2.75573144e-07f, 2.48015873e-05f), -1.38888889e-03f), 4.16666667e-02f), -0.5f), 1.0f);
    const int q = (int)k;
    const float a = (q & 1) ? cp : sp, b = (q & 1) ? sp : cp;
    s = (q & 2) ? -a : a;
    c = ((q + 1) & 2) ? -b : b;
}
__device__ __forceinline__ unsigned row_std(float v) {
    const float s1 = rbf(__builtin_amdgcn_sqrtf(rbf(rbf(v) + LN_EPS)));
    const float r1 = __builtin_amdgcn_rcpf(s1);
    const float s2 = __builtin_amdgcn_sqrtf(rbf(rbf(v * r1 * r1) + LN_EPS));
    return pk_bf16(s1, s2);
}

typedef const unsigned short* __restrict__ bfp;

__global__ __launch_bounds__(512) void k_fused(bfp x, bfp lam_re, bfp lam_im, bfp bre, bfp bim, bfp cre, bfp cim, bfp log_dt, bfp dskip,
                                               bfp w1, bfp b1, bfp w2, bfp b2, char* ws_, float* __restrict__ out) {
    extern __shared__ __attribute__((aligned(16))) char smem_[];
    LAS char* smem = (LAS char*)smem_;
    GAS char* ws = (GAS char*)ws_;
    const int wave = __builtin_amdgcn_readfirstlane(threadIdx.x >> 6), lane = threadIdx.x & 63;
    const int g = lane >> 4, li = lane & 15;
    const int xg8 = blockIdx.x & 7, loc = blockIdx.x >> 3, ci = 4 * (xg8 >> 1) + (loc >> 3), cj = 8 * (xg8 & 1) + (loc & 7);
    const int t0 = 128 * ci, p0 = 32 * cj, f0 = cj < 11 ? 96 * cj : 1056 + 64 * (cj - 11), nfu = cj < 11 ? 6 : 4;
    const int T = 2 * ci + (cj >> 3), d0 = 64 * (cj & 7);
    const int rot = ((ci + 15) >> 1) & 7;
    STAMP(0);
    unsigned long long entry_pc;
    asm volatile("s_getpc_b64 %0" : "=s"(entry_pc));
    unsigned epoch;
    asm volatile("global_load_dword %0, %1, off sc1" : "=v"(epoch) : "v"(ws + WS_EP) : "memory");
    const int q = li >> 2, pp = lane & 3;

    if (__builtin_expect(wave >= 4, 1)) {
        const int lw = wave - 4;
        __builtin_amdgcn_s_setprio(PRIO);
        const unsigned lds0 = (unsigned)(size_t)smem;
        unsigned xo[4], bo[2], wo[3];
#pragma unroll
        for (int i = 0; i < 4; ++i) {
            const int R = 8 * (4 * lw + i) + (lane >> 3), ii = R & 15, bb = (R >> 4) & 3;
            const int tok = t0 + (R & 64) + 16 * (ii >> 2) + 8 * (ii & 1) + 2 * bb + ((ii >> 1) & 1);
            xo[i] = tok * 1024 + 16 * ((lane & 7) ^ (lane >> 3)) + 3072 - 1024 * i;
        }
#pragma unroll
        for (int i = 0; i < 2; ++i) {
            const int R = 8 * (2 * lw + i) + (lane >> 3);
            bo[i] = (p0 + (R & 31)) * 1024 + 16 * ((lane & 7) ^ (lane >> 3)) + 3072 - 1024 * i;
        }
#pragma unroll
        for (int i = 0; i < 3; ++i) {
            const int st = 2 * (3 * lw + i) + (lane >> 5), kg = st / 3, ns = st - 3 * kg, kr = (lane & 31) >> 2;
            const int ch = 4 * ns + ((lane & 3) ^ ((2 * kg + (kr >> 2)) & 3));
            const int f = min(f0 + 8 * ch, FF - 8);
            wo[i] = (8 * kg + kr) * (FF * 2) + f * 2 + 3072 - 1024 * i;
        }
        const GAS char* xg = (const GAS char*)x - 3072;
        const GAS char* bg = (const GAS char*)(lw < 2 ? bre : bim) - 3072;
        const GAS char* wg = (const GAS char*)w1 - 3072;
        const unsigned lx = lds0 + P1_X + lw * 4096, lb = lds0 + P1_B + lw * 2048, lwb = lds0 + P1_W + lw * 3072;
        auto piece = [&](int t, auto I) {
            constexpr int i = decltype(I)::value;
            const unsigned so = (t & 3) * P1_SLOT;
            const int kt = (t + rot) & 7;
            if constexpr (i < 4) dma1<1024 * i>(lx + so, xo[i], xg + kt * 128);
            else if constexpr (i < 6) dma1<1024 * (i - 4)>(lb + so, bo[i - 4], bg + kt * 128);
            else dma1<1024 * (i - 6)>(lwb + so, wo[i - 6], wg + (size_t)kt * (64 * FF * 2));
        };
#define PC(t, i) piece(t, std::integral_constant<int, i>{})
        auto issue = [&](int t) { PC(t, 0); PC(t, 1); PC(t, 2); PC(t, 3); PC(t, 4); PC(t, 5); PC(t, 6); PC(t, 7); PC(t, 8); };
        const GAS char* tbase;
        unsigned tstep;
        if (lw == 0) { tbase = (const GAS char*)x + (t0 + 16 * (cj & 7) + li) * 1024; tstep = 128; }
        else if (lw == 1) { const int r = 16 * (ci & 3) + li; tbase = (const GAS char*)(r < 32 ? bre : bim) + (p0 + (r & 31)) * 1024; tstep = 128; }
        else { tbase = (const GAS char*)w1 + (16 * (ci & 3) + li) * (FF * 2) + min(f0 * 2 + (lw == 2 ? 0 : 128), FF * 2 - 4); tstep = 64 * FF * 2; }
        auto warm = [&](int t) { if (WARM1) touch(lds0 + L_DUMMY + 256 * lw, tbase + ((t + rot) & 7) * tstep); };
        STAMP(6);
        if (CODEW == 1) {
            touch(lds0 + L_DUMMY + 256 * lw, (const GAS char*)(entry_pc & ~255ull) + min(((loc & 3) * 4 + lw) * 13 + min(lane, 12), CODE_LINES - 1) * 128);
        }
        issue(0); issue(1);
        warm(2 + (g & 1));
        if (CODEW == 2) {
            touch(lds0 + L_DUMMY + 256 * lw, (const GAS char*)(entry_pc & ~255ull) + min(((loc & 3) * 4 + lw) * 13 + min(lane, 12), CODE_LINES - 1) * 128);
        }
        STAMP(7);

        unsigned w2o[2], co[2], ao[2];
#pragma unroll
        for (int i = 0; i < 2; ++i) {
            const int kg = 2 * lw + i;
            w2o[i] = (8 * kg + ((lane & 31) >> 2)) * (DM * 2) + (d0 + 32 * (lane >> 5) + 8 * (lane & 3)) * 2 + 3072 - 1056 * i;
            co[i] = (d0 + 8 * kg + (lane >> 3)) * (NP * 2) + 16 * ((lane & 7) ^ (lane >> 3)) + 3072 - 1024 * i;
            ao[i] = kg * 1024 + lane * 16 + 3072 - 1056 * i;
        }
        const GAS char* ag = ws + WS_ACT + (size_t)T * ACT_TILE;
        const GAS char* w2g = (const GAS char*)w2 - 3072;
        const unsigned l2 = lds0 + lw * 2080, l2c = lds0 + P2_IMG + lw * 2048;
        auto dmaA = [&](const GAS char* src, unsigned slot) { dma2<1056>(l2 + slot, ao[0], ao[1], src - 3072); };
        auto dmaW = [&](const GAS char* src, unsigned slot) { dma2<1056>(l2 + slot + P2_IMG, w2o[0], w2o[1], src); };
        auto dmaC = [&](const GAS char* src, unsigned slot) { dma2<1024>(l2c + slot, co[0], co[1], src); };
        const int u4 = KROT ? 2 * (((cj & 7) + 3 * (ci & 3)) % 10) : 0, u3 = KROT ? 2 * (((cj & 7) + 2 * (ci & 3)) & 7) : 0;
        auto wrap20 = [](int u) { return u >= 20 ? u - 20 : u; };
        auto preW = [&](int s) { dmaW(w2g + wrap20(u4 + s) * 65536, s * P2_SLOT); };

        const GAS char* l2a[7];
#pragma unroll
        for (int i = 0; i < 7; ++i) {
            const int n = 4 * min((4 * i + lw) * 17 + min(lane, 16), 455) + (ci & 3), m = n - 800;
            l2a[i] = n < 800 ? (const GAS char*)w2 + (576 + n) * 1024 + d0 * 2
                             : (const GAS char*)(m < 512 ? cre : cim) + (d0 + (m & 63)) * 1024 + ((m >> 6) & 7) * 128;
        }
        auto warm2 = [&](int i) {
            if (PREF3) asm volatile("s_mov_b64 exec, 0x1ffff\n\ts_mov_b32 m0, %0\n\ts_nop 0\n\tglobal_load_lds_dword %1, off\n\ts_mov_b64 exec, -1"
                                    ::"s"(lds0 + L_DUMMY + 256 * lw), "v"(l2a[i]) : "memory");
        };
        const int n1 = 75 * blockIdx.x + 19 * lw + min(lane, 18);
        const GAS char* l1a = n1 < 11008 ? (const GAS char*)w2 + n1 * 128
                                         : (n1 < 15104 ? (const GAS char*)cre + (n1 - 11008) * 128 : (const GAS char*)cim + (min(n1, 19199) - 15104) * 128);
        const GAS u32x4* rsp = (const GAS u32x4*)(ws + WS_RS) + t0 + 64 * (lw & 1) + lane;
        const GAS u32x4* csp = (const GAS u32x4*)(ws + WS_CS) + ((cj * 16 + 8 * (lw >> 1) + 2 * g) * 2 + (lw & 1)) * 32 + li;
        asm volatile("" : "+v"(l2a[0]), "+v"(l2a[1]), "+v"(l2a[2]), "+v"(l2a[3]), "+v"(l2a[4]), "+v"(l2a[5]), "+v"(l2a[6]), "+v"(l1a), "+v"(rsp), "+v"(csp));
        asm volatile("s_waitcnt vmcnt(%1)" : "+v"(epoch) : "i"(9 + WARM1 + (CODEW == 2)) : "memory");
        unsigned tag = __builtin_amdgcn_readfirstlane(epoch) + 1u;
        asm volatile("" : "+s"(tag));
        bar();
        STAMP(1);
        issue(2); warm(4);
        if (PREFETCH) touch(lds0 + L_DUMMY + 256 * lw, l1a);
        wait_vm<9 + (CODEW != 0) + PREFETCH>(); bar();
        issue(3); warm(5);
        {
            f32x2 v;
            asm volatile("ds_read_b64 %0, %1\n\ts_waitcnt lgkmcnt(0)" : "=&v"(v) : "v"(lds0 + L_MAIL + 16 * lw + 8 * (lane & 1)) : "memory");
            if (lane < 2) store16_sc1((GAS u32x4*)(ws + WS_RS) + t0 + 8 * cj + 2 * lw + lane, u32x4{__float_as_uint(v[0]), tag, __float_as_uint(v[1]), tag});
        }
        warm2(0);
        wait_vm<10 + PREFETCH + PX>(); bar();
        issue(4); warm(6);
        {
            float a, b, c;
            const unsigned ma = lds0 + L_MAIL + 64 + 4 * (((lw >> 1) * 5 + 3 * (lw & 1)) * 16 + li);
            asm volatile("ds_read_b32 %0, %3\n\tds_read_b32 %1, %3 offset:64\n\tds_read_b32 %2, %3 offset:128\n\ts_waitcnt lgkmcnt(0)"
                         : "=&v"(a), "=&v"(b), "=&v"(c) : "v"(ma) : "memory");
            if (g == 0) store16_sc1((GAS u32x4*)(ws + WS_CS) + ((cj * 16 + ci) * 2 + (lw >> 1)) * 32 + 16 * (lw & 1) + li,
                                    u32x4{__float_as_uint(a), __float_as_uint(b), __float_as_uint(c), tag});
        }
        warm2(1);
        wait_vm<11 + 2 * PX>(); bar();
        issue(5); warm2(2); wait_vm<10 + 2 * PX>(); bar();
        issue(6); warm2(3); wait_vm<9 + 2 * PX>(); bar();
        issue(7);
        u32x4 rs, cs[4];
        auto gather = [&]() {
            asm volatile("global_load_dwordx4 %0, %5, off sc1\n\t"
                         "global_load_dwordx4 %1, %6, off sc1\n\tglobal_load_dwordx4 %2, %6, off offset:256 sc1\n\t"
                         "global_load_dwordx4 %3, %6, off offset:1024 sc1\n\tglobal_load_dwordx4 %4, %6, off offset:1280 sc1"
                         : "=&v"(rs), "=&v"(cs[0]), "=&v"(cs[1]), "=&v"(cs[2]), "=&v"(cs[3]) : "v"(rsp), "v"(csp) : "memory");
        };
        gather(); warm2(4);
        wait_vm<14 + 2 * PX>(); bar();
        preW(0); preW(1); preW(2); preW(3);
        warm2(5);
        asm volatile("s_waitcnt vmcnt(%5)" : "+v"(rs), "+v"(cs[0]), "+v"(cs[1]), "+v"(cs[2]), "+v"(cs[3]) : "i"(8 + 2 * PX) : "memory");
        bar();
        preW(4); preW(5); warm2(6);
        auto fresh = [&]() {
            return __all(rs[1] == tag && rs[3] == tag && cs[0][3] == tag && cs[1][3] == tag && cs[2][3] == tag && cs[3][3] == tag);
        };
#pragma unroll 1
        for (unsigned spins = 0; spins < SPIN_CAP && !fresh(); ++spins) {
            gather();
            asm volatile("s_waitcnt vmcnt(0)" : "+v"(rs), "+v"(cs[0]), "+v"(cs[1]), "+v"(cs[2]), "+v"(cs[3])::"memory");
        }
        if (lw < 2) {
            const float m = __uint_as_float(rs[0]), mb = rbf(m);
            const float r1 = __builtin_amdgcn_rcpf(bf_lo(rs[2])), r12 = r1 * __builtin_amdgcn_rcpf(bf_hi(rs[2]));
            const int tau = 64 * lw + lane;
            const unsigned ta = lds0 + L_TAB1 + 4 * (((tau >> 4) * 8 + (tau & 7)) * 4 + ((tau >> 3) & 1));
            lds_st32(ta, r1); lds_st32(ta + 8, -r1 * mb);
            lds_st32(ta + 1024, r12); lds_st32(ta + 1032, -r12 * m);
            lds_st64(lds0 + L_TOK + 8 * tau, f32x2{mb, r1});
        }
        {
            float v5[5];
#pragma unroll
            for (int k = 0; k < 3; ++k) v5[k] = __uint_as_float(cs[0][k]) + __uint_as_float(cs[2][k]);
#pragma unroll
            for (int k = 0; k < 2; ++k) v5[3 + k] = __uint_as_float(cs[1][k]) + __uint_as_float(cs[3][k]);
#pragma unroll
            for (int k = 0; k < 5; ++k) v5[k] = sum_groups(v5[k]);
            if (g == 0) {
#pragma unroll
                for (int k = 0; k < 5; ++k) lds_st32(lds0 + L_CS + 4 * ((((lw >> 1) * 2 + (lw & 1)) * 5 + k) * 16 + li), v5[k]);
            }
        }
        asm volatile("s_waitcnt lgkmcnt(0)" ::: "memory");
        bar();
        preW(6); preW(7); preW(8);
        STAMP(2);
        bar();
        bar();
        __builtin_amdgcn_s_setprio(LPRIO2);
        wait_flags((const GAS unsigned*)(ws + WS_FH) + T * 32, tag);
        STAMP(3);
        auto preA = [&](int st) { dmaA(ag + wrap20(u4 + st) * 8192, st * P2_SLOT); };
        preA(0); preA(1); preA(2); preA(3);
        wait_vm<4>(); bar();
        STAMP(4);
        preA(4); preA(5); preA(6); preA(7);
        wait_vm<8>(); bar();
        int u = wrap20(u4 + 9);
        unsigned slot = 0;
        auto nxt = [&]() { slot = slot == P2_RING - P2_SLOT ? 0 : slot + P2_SLOT; };
        auto stW = [&]() { dmaA(ag + u * 8192, slot); dmaW(w2g + u * 65536, slot); u = u == 19 ? 0 : u + 1; nxt(); };
        auto stC = [&]() {
            dmaA(ag + (FF * 128 + u * 8192), slot);
            dmaC((const GAS char*)(u < 8 ? cre : cim) - 3072 + (u & 7) * 128, slot);
            u = (u + 1) & 15; nxt();
        };
        preA(8); stW();
        wait_vm<10>(); bar(); stW(); stW();
        wait_vm<14>(); bar(); stW(); stW();
#pragma unroll 1
        for (int j = 4; j < 7; ++j) { wait_vm<16>(); bar(); stW(); stW(); }
        wait_vm<16>(); bar();
        dmaA(ag + 20 * 8192, slot); dmaW(w2g + 20 * 65536, slot); nxt();
        dmaA(ag + 21 * 8192 - 4096, slot); dmaW(w2g + 21 * 65536 - 32768, slot); nxt();
        u = u3;
        wait_vm<16>(); bar();
        stC(); stC();
#pragma unroll 1
        for (int j = 9; j < 16; ++j) { wait_vm<16>(); bar(); stC(); stC(); }
        wait_vm<16>(); bar();
        wait_vm<8>(); bar();
        wait_vm<0>(); bar();
        STAMP(5);
        return;
    }

    const int wm = wave >> 1, wn = wave & 1;
    const int p = p0 + 16 * wn + li;
    cf lam = {0.0f, 0.0f}, l8 = lam, l16 = lam, l128 = lam, lsig = lam, coef2 = lam;
    float b1c[3], p_lr, p_lim, p_ldt;
    unsigned tag;
    {
        u32x4 xr[2];
        unsigned short pr[6];
#pragma unroll
        for (int i = 0; i < 2; ++i) xr[i] = *(const u32x4*)(x + (t0 + 8 * cj + 2 * wave + i) * DM + lane * 8);
        pr[0] = lam_re[p]; pr[1] = lam_im[p]; pr[2] = log_dt[p];
#pragma unroll
        for (int c = 0; c < 3; ++c) pr[3 + c] = b1[min(f0 + 16 * (3 * wn + c) + li, FF - 1)];
        SB0;
        p_lr = bf2f(pr[0]); p_lim = bf2f(pr[1]); p_ldt = bf2f(pr[2]);
#pragma unroll
        for (int c = 0; c < 3; ++c) b1c[c] = bf2f(pr[3 + c]);
        float mean[2], var[2];
#pragma unroll
        for (int i = 0; i < 2; ++i) {
            float e[8], s = 0.0f;
#pragma unroll
            for (int j = 0; j < 4; ++j) { e[2 * j] = bf_lo(xr[i][j]); e[2 * j + 1] = bf_hi(xr[i][j]); s += e[2 * j] + e[2 * j + 1]; }
            mean[i] = wave_sum(s) * (1.0f / DM);
            float qq = 0.0f;
#pragma unroll
            for (int j = 0; j < 8; ++j) qq = fmaf(e[j] - mean[i], e[j] - mean[i], qq);
            var[i] = wave_sum(qq) * (1.0f / DM);
        }
        if (lane < 2) ((LAS f32x2*)(smem + L_MAIL))[2 * wave + lane] = f32x2{lane ? mean[1] : mean[0], __uint_as_float(row_std(lane ? var[1] : var[0]))};
        asm volatile("" : "+v"(b1c[0]), "+v"(b1c[1]), "+v"(b1c[2]), "+v"(p_lr), "+v"(p_lim), "+v"(p_ldt));
    }
    auto lam_setup = [&]() {
        const float lr = p_lr, lim = p_lim, dt = rbf(exp_f(p_ldt));
        const float mag = exp_f(dt * lr);
        float sn, cs;
        sincos_f(dt * lim, sn, cs);
        lam = cf{mag * cs, mag * sn};
        const float nr = lam.r - 1.0f, ni = lam.i, den = 1.0f / (lr * lr + lim * lim);
        coef2 = cf{2.0f * (nr * lr + ni * lim) * den, 2.0f * (ni * lr - nr * lim) * den};
        const cf l2 = cmul(lam, lam), l4 = cmul(l2, l2);
        l8 = cmul(l4, l4); l16 = cmul(l8, l8);
        const cf l32 = cmul(l16, l16), l64 = cmul(l32, l32);
        l128 = cmul(l64, l64);
        lsig = (g & 1) ? l16 : cf{1.0f, 0.0f};
        if (g & 2) lsig = cmul(lsig, l32);
        if (wm) lsig = cmul(lsig, l64);
        asm volatile("" : "+v"(lam.r), "+v"(lam.i), "+v"(l8.r), "+v"(l8.i), "+v"(l16.r), "+v"(l16.i), "+v"(l128.r), "+v"(l128.i), "+v"(lsig.r), "+v"(lsig.i),
                          "+v"(coef2.r), "+v"(coef2.i));
    };
    f32x4 acc[4][5];
    {
        const int sw = 16 * (g ^ (lane & 7));
        const int xa0 = P1_X + (64 * wm + li) * 128 + sw;
        const int ba0 = P1_B + (16 * wn + li) * 128 + sw;
        int wa[2][2];
#pragma unroll
        for (int t = 0; t < 2; ++t)
#pragma unroll
            for (int cl = 0; cl < 2; ++cl) {
                const int par = (3 * wn + cl) & 1;
                wa[t][cl] = P1_W + 1536 * g + 64 * q + 256 * t + 32 * (par ^ (g & 1)) + 16 * ((pp >> 1) ^ t) + 8 * (pp & 1) + (cl == 0 ? 512 * wn : 1024 * wn);
            }
#pragma unroll
        for (int b = 0; b < 4; ++b)
#pragma unroll
            for (int c = 0; c < 5; ++c) acc[b][c] = f32x4{0, 0, 0, 0};
        s16x8 xa[2][4], wb[2][5];
        auto rd = [&](int slot, int ks, int buf) {
            LAS char* s = smem + slot * P1_SLOT;
            LAS char* xs = s + (xa0 ^ (64 * ks));
            LAS char* bs = s + (ba0 ^ (64 * ks));
#pragma unroll
            for (int b = 0; b < 4; ++b) xa[buf][b] = lds_row(xs + b * 2048);
            wb[buf][0] = lds_row(bs);
            wb[buf][1] = lds_row(bs + 4096);
            wb[buf][2] = lds_tr(s + wa[0][0] + 6144 * ks, s + wa[1][0] + 6144 * ks);
            wb[buf][3] = lds_tr(s + wa[0][1] + 6144 * ks, s + wa[1][1] + 6144 * ks);
            wb[buf][4] = lds_tr(s + wa[0][0] + 6144 * ks + 512, s + wa[1][0] + 6144 * ks + 512);
        };
        auto mm = [&](int buf) {
#pragma unroll
            for (int c = 0; c < 5; ++c)
#pragma unroll
                for (int b = 0; b < 4; ++b) acc[b][c] = mfma(xa[buf][b], wb[buf][c], acc[b][c]);
        };
        lam_setup();
        auto colsum = [&](int buf) {
            const s16x8 ones = {0x3f80, 0x3f80, 0x3f80, 0x3f80, 0x3f80, 0x3f80, 0x3f80, 0x3f80};
            float v5[5];
#pragma unroll
            for (int c = 0; c < 5; ++c) v5[c] = mfma(ones, wb[buf][c], f32x4{0, 0, 0, 0})[0];
            if (g == 0) {
#pragma unroll
                for (int c = 0; c < 5; ++c) ((LAS float*)(smem + L_MAIL + 64))[(wn * 5 + c) * 16 + li] = v5[c];
            }
        };
        asm volatile("s_waitcnt vmcnt(0) lgkmcnt(0)" : "+v"(epoch)::"memory");
        tag = __builtin_amdgcn_readfirstlane(epoch) + 1u;
        asm volatile("" : "+s"(tag));
        bar();
        STAMP(1);
        rd(0, 0, 0);
        wait_lgkm0();
#pragma unroll 1
        for (int seg = 0, t = 0; seg < 2; ++seg) {
            const int tend = seg ? 7 : 1;
#pragma unroll 1
            for (; t < tend; ++t) {
                SB0;
                rd(t & 3, 1, 1); mm(0); interleave<20, 12>();
                SB0; wait_lgkm0(); bar();
                rd((t + 1) & 3, 0, 0); mm(1); interleave<20, 12>();
                SB0; wait_lgkm0();
            }
            if (seg == 0 && wm == 0) { if (ci & 1) colsum(1); else colsum(0); }
        }
        rd(3, 1, 1); mm(0); interleave<20, 12>();
        SB0; wait_lgkm0(); bar();
        mm(1);
    }
    STAMP(2);

    LAS f32x4* tab1 = (LAS f32x4*)(smem + L_TAB1) + (wm * 4 + g) * 8;
    LAS f32x4* tab2 = (LAS f32x4*)(smem + L_TAB2) + (wm * 4 + g) * 8;
    auto csum = [&](int k) { return ((LAS float*)(smem + L_CS))[(wn * 5 + k) * 16 + li] + ((LAS float*)(smem + L_CS))[((2 + wn) * 5 + k) * 16 + li]; };
    u32x2 xv[2][2], dv[2], bv[2];
#pragma unroll
    for (int i = 0; i < 2; ++i) {
        const int d = d0 + 32 * (wave >> 1) + 16 * i + 4 * g;
        dv[i] = *(const u32x2*)(dskip + d); bv[i] = *(const u32x2*)(b2 + d);
#pragma unroll
        for (int j = 0; j < 2; ++j) xv[i][j] = *(const u32x2*)(x + (64 * T + 32 * j + 16 * ((lane >> 3) & 1) + 8 * (wave & 1) + (lane & 7)) * DM + d);
    }
    const int Tw = 2 * ci + wm;
    const __amdgpu_buffer_rsrc_t act = __builtin_amdgcn_make_buffer_rsrc((void*)(ws_ + WS_ACT), 0, 32 * ACT_TILE, 0x00020000);
    const unsigned act0 = Tw * ACT_TILE;
    {
        f32x4 tb[8];
#pragma unroll
        for (int j = 0; j < 8; ++j) tb[j] = tab2[j];
        constexpr float C1 = -2.30220819f;
        constexpr float C2 = C1 * 0.044715f;
#pragma unroll
        for (int c = 0; c < 3; ++c) {
            const int col = 16 * (3 * wn + c) + li;
            const float s1c = csum(2 + c);
            f32x2 y[8];
#pragma unroll
            for (int j = 0; j < 8; ++j) {
                const int b = j >> 1, r = 2 * (j & 1);
                const f32x2 a = {tb[j][0], tb[j][1]}, bb = {tb[j][2], tb[j][3]};
                const f32x2 z = pfma(a, f32x2{acc[b][2 + c][r], acc[b][2 + c][r + 1]}, pfma(bb, splat(s1c), splat(b1c[c])));
                const f32x2 u = z * pfma(z * z, splat(C2), splat(C1));
                const f32x2 d = f32x2{__builtin_amdgcn_exp2f(u[0]), __builtin_amdgcn_exp2f(u[1])} + splat(1.0f);
                y[j] = z * f32x2{__builtin_amdgcn_rcpf(d[0]), __builtin_amdgcn_rcpf(d[1])};
            }
            if (3 * wn + c < nfu) {
                const int k = f0 + col;
                const unsigned dst = act0 + (k >> 3) * 1024 + (k & 7) * 64 + 16 * g;
#pragma unroll
                for (int sgm = 0; sgm < 2; ++sgm)
                    __builtin_amdgcn_raw_buffer_store_b128(u32x4{pk_bf16(y[0][sgm], y[1][sgm]), pk_bf16(y[2][sgm], y[3][sgm]), pk_bf16(y[4][sgm], y[5][sgm]), pk_bf16(y[6][sgm], y[7][sgm])},
                                                           act, dst + 512 * sgm, 0, ST_AUX);
            }
        }
    }
    STAMP(3);

    f32x2 ure[8], uim[8];
    {
        const float sbr = csum(0), sbi = csum(1);
#pragma unroll
        for (int j = 0; j < 8; ++j) {
            const f32x4 tb = tab1[j];
            const f32x2 a = {tb[0], tb[1]}, bb = {tb[2], tb[3]};
            const int b = j >> 1, r = 2 * (j & 1);
            ure[j] = pfma(a, f32x2{acc[b][0][r], acc[b][0][r + 1]}, bb * splat(sbr));
            uim[j] = pfma(a, f32x2{acc[b][1][r], acc[b][1][r + 1]}, bb * splat(sbi));
        }
    }
    f32x2 hr = ure[0], hi = uim[0];
#pragma unroll
    for (int j = 1; j < 8; ++j) {
        const f32x2 tr = pfma(splat(lam.r), hr, ure[j]), ti = pfma(splat(lam.r), hi, uim[j]);
        const f32x2 nr = pfma(splat(-lam.i), hi, tr), ni = pfma(splat(lam.i), hr, ti);
        hr = nr; hi = ni;
    }
    const cf eA = cf{hr[0], hi[0]};
    const cf e = cfma(l8, eA, cf{hr[1], hi[1]});
    const int sig = 4 * wm + g;
    LAS f32x2* eL = (LAS f32x2*)(smem + L_EL);
    eL[sig * 32 + 16 * wn + li] = f32x2{e.r, e.i};
    wait_lgkm0();
    bar();
    cf Lp = cf{0.0f, 0.0f};
#pragma unroll
    for (int s = 0; s < 7; ++s) {
        const f32x2 v = eL[s * 32 + 16 * wn + li];
        const cf n = cfma(l16, Lp, cf{v[0], v[1]});
        if (s < sig) Lp = n;
    }
    wait_vm<0>();
    if (lane == 0) __hip_atomic_store((GAS unsigned*)(ws + WS_FH) + Tw * 32 + 2 * cj + wn, tag, __ATOMIC_RELAXED, __HIP_MEMORY_SCOPE_AGENT);
    GAS u32x4* eg = (GAS u32x4*)(ws + WS_EG);
    if (sig == 7) {
        const cf E = cfma(l16, Lp, e);
        store16_sc1(eg + ci * NP + p, u32x4{__float_as_uint(E.r), tag, __float_as_uint(E.i), tag});
    }
    bar();
    STAMP(4);

    const int wd = wave >> 1, wt = wave & 1;
    f32x4 o[2][2];
    cf Hin = cf{0.0f, 0.0f};
    {
#pragma unroll
        for (int j = 0; j < 2; ++j) {
            const f32x2 st = ((LAS f32x2*)(smem + L_TOK))[64 * (T & 1) + 32 * j + 16 * ((lane >> 3) & 1) + 8 * wt + (lane & 7)];
#pragma unroll
            for (int i = 0; i < 2; ++i) {
                const float xe[4] = {bf_lo(xv[i][j][0]), bf_hi(xv[i][j][0]), bf_lo(xv[i][j][1]), bf_hi(xv[i][j][1])};
                const float de[4] = {bf_lo(dv[i][0]), bf_hi(dv[i][0]), bf_lo(dv[i][1]), bf_hi(dv[i][1])};
                const float be[4] = {bf_lo(bv[i][0]), bf_hi(bv[i][0]), bf_lo(bv[i][1]), bf_hi(bv[i][1])};
#pragma unroll
                for (int k = 0; k < 4; ++k) {
                    const float xn = rbf(rbf(xe[k] - st[0]) * st[1]);
                    o[i][j][k] = xe[k] + rbf(xn * de[k]) + be[k];
                }
            }
        }
        asm volatile("" : "+v"(o[0][0]), "+v"(o[0][1]), "+v"(o[1][0]), "+v"(o[1][1]));
        const GAS u32x4* ep = eg + g * NP + p;
        u32x4 gr[4];
        auto fetch = [&]() {
            asm volatile("global_load_dwordx4 %0, %4, off sc1\n\tglobal_load_dwordx4 %1, %5, off sc1\n\t"
                         "global_load_dwordx4 %2, %6, off sc1\n\tglobal_load_dwordx4 %3, %7, off sc1"
                         : "=&v"(gr[0]), "=&v"(gr[1]), "=&v"(gr[2]), "=&v"(gr[3])
                         : "v"(ep), "v"(ep + 4 * NP), "v"(ep + 8 * NP), "v"(ep + 12 * NP) : "memory");
        };
        auto landed = [&]() { asm volatile("s_waitcnt vmcnt(0)" : "+v"(gr[0]), "+v"(gr[1]), "+v"(gr[2]), "+v"(gr[3])::"memory"); };
        auto complete = [&]() {
            bool ok = true;
#pragma unroll
            for (int i = 0; i < 4; ++i) ok &= (4 * i + g >= ci) | ((gr[i][1] == tag) & (gr[i][3] == tag));
            return __all(ok);
        };
        fetch();
        const cf l256 = cmul(l128, l128), l512 = cmul(l256, l256);
        landed();
#pragma unroll 1
        for (unsigned spins = 0; spins < SPIN_CAP && !complete(); ++spins) { __builtin_amdgcn_s_sleep(2); fetch(); landed(); }
        cf hp = cf{0.0f, 0.0f};
#pragma unroll
        for (int i = 0; i < 4; ++i) {
            const cf n = cfma(l512, hp, cf{__uint_as_float(gr[i][0]), __uint_as_float(gr[i][2])});
            if (4 * i + g < ci) hp = n;
        }
        const int rr = (ci - 1 - g) & 3;
        if (rr & 1) hp = cmul(hp, l128);
        if (rr & 2) hp = cmul(hp, l256);
        Hin = cf{sum_groups(hp.r), sum_groups(hp.i)};
    }
    STAMP(5);
    u32x4 hq[2][2];
    unsigned hdr, hdi;
    auto store_h = [&]() {
#pragma unroll
        for (int sgm = 0; sgm < 2; ++sgm) {
            __builtin_amdgcn_raw_buffer_store_b128(hq[0][sgm], act, hdr + 512 * sgm, 0, ST_AUX);
            __builtin_amdgcn_raw_buffer_store_b128(hq[1][sgm], act, hdi + 512 * sgm, 0, ST_AUX);
        }
    };
    {
        const cf cA = cfma(lsig, Hin, Lp);
        const cf cB = cfma(l8, cA, eA);
        f32x2 sr = {cA.r, cB.r}, si = {cA.i, cB.i};
        f32x2 ore[8], oim[8];
#pragma unroll
        for (int j = 0; j < 8; ++j) {
            const f32x2 tr = pfma(splat(lam.r), sr, ure[j]), ti = pfma(splat(lam.r), si, uim[j]);
            const f32x2 nr = pfma(splat(-lam.i), si, tr), ni = pfma(splat(lam.i), sr, ti);
            sr = nr; si = ni;
            ore[j] = pfma(splat(coef2.r), sr, splat(-coef2.i) * si);
            oim[j] = pfma(splat(-coef2.r), si, splat(-coef2.i) * sr);
        }
        const int kr = FF + p, kim = FF + NP + p;
        hdr = act0 + (kr >> 3) * 1024 + (kr & 7) * 64 + 16 * g;
        hdi = act0 + (kim >> 3) * 1024 + (kim & 7) * 64 + 16 * g;
#pragma unroll
        for (int sgm = 0; sgm < 2; ++sgm) {
            hq[0][sgm] = u32x4{pk_bf16(ore[0][sgm], ore[1][sgm]), pk_bf16(ore[2][sgm], ore[3][sgm]), pk_bf16(ore[4][sgm], ore[5][sgm]), pk_bf16(ore[6][sgm], ore[7][sgm])};
            hq[1][sgm] = u32x4{pk_bf16(oim[0][sgm], oim[1][sgm]), pk_bf16(oim[2][sgm], oim[3][sgm]), pk_bf16(oim[4][sgm], oim[5][sgm]), pk_bf16(oim[6][sgm], oim[7][sgm])};
        }
        if (!DEFER) store_h();
    }
    STAMP(6);

    {
        const int trb = (g >> 1) * 2080 + (g & 1) * 1056 + 64 * q + 16 * (pp >> 1) + 8 * (pp & 1);
        const int tra = trb + 512 * wt, trw = P2_IMG + trb + 512 * wd;
        const int ca0 = P2_IMG + (32 * wd + li) * 128 + 16 * (g ^ (lane & 7));
        s16x8 wf[2][2][2], af[2][2][2];
        int slot = 0;
        auto next = [&]() { slot = slot == P2_RING - P2_SLOT ? 0 : slot + P2_SLOT; };
        auto frag_a = [&](LAS char* sl, int buf, int ks, int i) { LAS char* a = sl + tra + 4160 * ks + 32 * i; af[buf][ks][i] = lds_tr(a, a + 256); };
        auto frag_w = [&](LAS char* sl, int buf, int ks, int i, bool g4) {
            if (g4) { LAS char* a = sl + trw + 4160 * ks + 32 * i; wf[buf][ks][i] = lds_tr(a, a + 256); }
            else wf[buf][ks][i] = lds_row(sl + ((ca0 ^ (64 * ks)) + 2048 * i));
        };
        auto half = [&](int rb, bool rd, bool g4, int k0, bool mm_) {
            LAS char* sl = smem + slot;
            const int ub = rb ^ 1;
#pragma unroll
            for (int ks = 0; ks < 2; ++ks) {
#pragma unroll
                for (int m = 0; m < 4; ++m) {
                    if (mm_ && ks >= k0) mfma_acc(o[m >> 1][m & 1], wf[ub][ks][m >> 1], af[ub][ks][m & 1]);
                    SB0;
                    if (rd) { if (m < 2) frag_a(sl, rb, ks, m); else frag_w(sl, rb, ks, m - 2, g4); }
                    SB0;
                }
            }
            if (rd) next();
        };
        const GAS unsigned* fsp = (const GAS unsigned*)(ws + WS_FS) + T * 32 + (lane & 31);
        const unsigned fsl = (unsigned)(size_t)smem + L_EL + 256 * wave;
        __builtin_amdgcn_s_setprio(CPRIO2);
        bar(); half(0, true, true, 0, false);
        STAMP(7);
        if (DEFER) store_h();
        half(1, true, true, 0, true);
#pragma unroll 1
        for (int seg = 0, j = 1; seg < 4; ++seg) {
            const int jend = seg == 0 ? 2 + DEFER : (seg == 1 ? 7 : (seg == 2 ? 8 : 10));
#pragma unroll 1
            for (; j < jend; ++j) {
                bar(); half(0, true, true, 0, true);
                half(1, true, true, 0, true);
            }
            if (seg == 0) {
                wait_vm<0>();
                if (lane == 0) __hip_atomic_store((GAS unsigned*)(ws + WS_FS) + Tw * 32 + 2 * cj + wn, tag, __ATOMIC_RELAXED, __HIP_MEMORY_SCOPE_AGENT);
            } else if (seg == 1) {
                asm volatile("s_mov_b32 m0, %0\n\ts_nop 0\n\tglobal_load_lds_dword %1, off sc1" ::"s"(fsl), "v"(fsp) : "memory");
            } else if (seg == 2) {
                unsigned fsv;
                asm volatile("s_waitcnt vmcnt(0)\n\tds_read_b32 %0, %1\n\ts_waitcnt lgkmcnt(0)" : "=&v"(fsv) : "v"(fsl + 4 * lane) : "memory");
                if (!__all(fsv == tag)) wait_flags((const GAS unsigned*)(ws + WS_FS) + T * 32, tag);
            }
            SB0;
        }
        bar(); half(0, true, true, 0, true);
        half(1, true, true, 0, true);
        bar(); half(0, true, false, 1, true);
        half(1, true, false, 0, true);
#pragma unroll 1
        for (int j = 12; j < 19; ++j) {
            bar(); half(0, true, false, 0, true);
            half(1, true, false, 0, true);
        }
        half(0, false, false, 0, true);
        asm volatile("s_nop 7\n\ts_nop 7" : "+v"(o[0][0]), "+v"(o[0][1]), "+v"(o[1][0]), "+v"(o[1][1]));
        STAMP(8);

#pragma unroll
        for (int j = 0; j < 2; ++j)
#pragma unroll
            for (int i = 0; i < 2; ++i)
                *(f32x4*)(out + (64 * T + 32 * j + 16 * ((lane >> 3) & 1) + 8 * wt + (lane & 7)) * DM + d0 + 32 * wd + 16 * i + 4 * g) = o[i][j];
        if (ci == 15 && cj == 0 && threadIdx.x == 0) __hip_atomic_store((GAS unsigned*)(ws + WS_EP), tag, __ATOMIC_RELAXED, __HIP_MEMORY_SCOPE_AGENT);
        STAMP(9);
    }
}

}

extern "C" void kernel_launch(void* const* d_in, const int* in_sizes, int n_in, void* d_out, int out_size, void* d_ws, size_t ws_size,
                              hipStream_t stream) {
    typedef const unsigned short* P;
    (void)hipFuncSetAttribute((const void*)k_fused, hipFuncAttributeMaxDynamicSharedMemorySize, LDS_BYTES);
    k_fused<<<256, 512, LDS_BYTES, stream>>>((P)d_in[0], (P)d_in[1], (P)d_in[2], (P)d_in[3], (P)d_in[4], (P)d_in[5], (P)d_in[6], (P)d_in[7],
                                             (P)d_in[8], (P)d_in[10], (P)d_in[11], (P)d_in[12], (P)d_in[13], (char*)d_ws, (float*)d_out);
}
```

```cpp
#include <hip/hip_runtime.h>
#include <hip/hip_bf16.h>
#include <stdint.h>
#include <type_traits>

#define LAS __attribute__((address_space(3)))
#define GAS __attribute__((address_space(1)))

typedef __attribute__((ext_vector_type(8))) short s16x8;
typedef __attribute__((ext_vector_type(4))) short s16x4;
typedef __attribute__((ext_vector_type(8))) __bf16 bf16x8;
typedef __attribute__((ext_vector_type(2))) __bf16 bf16x2;
typedef __attribute__((ext_vector_type(4))) float f32x4;
typedef __attribute__((ext_vector_type(2))) float f32x2;
typedef __attribute__((ext_vector_type(4))) unsigned u32x4;
typedef __attribute__((ext_vector_type(2))) unsigned u32x2;

namespace {

constexpr int SEQ = 2048, DM = 512, NP = 512, FF = 1376;
constexpr int KACT = FF + 2 * NP;
constexpr int ACT_TILE = KACT * 128;
constexpr unsigned WS_ACT = 0;
constexpr unsigned WS_RS = 10u << 20;
constexpr unsigned WS_EG = WS_RS + (64u << 10);
constexpr unsigned WS_FH = WS_EG + (256u << 10);
constexpr unsigned WS_FS = WS_FH + (8u << 10);
constexpr unsigned WS_CS = WS_FS + (8u << 10);
constexpr unsigned WS_EP = WS_CS + (256u << 10);
constexpr float LN_EPS = 1e-5f;
constexpr unsigned SPIN_CAP = 1u << 18;
constexpr int AUX_SC1 = 16;
constexpr int CODE_LINES = 192;

constexpr int P1_SLOT = 36 * 1024, P1_X = 0, P1_B = 16384, P1_W = 24576;
constexpr int P2_IMG = 4 * 2080, P2_SLOT = 2 * P2_IMG, P2_RING = 9 * P2_SLOT;
constexpr int L_TAB1 = P2_RING;
constexpr int L_TAB2 = L_TAB1 + 1024;
constexpr int L_EL = L_TAB2 + 1024;
constexpr int L_CS = L_EL + 2048;
constexpr int L_TOK = L_CS + 1280;
constexpr int L_DUMMY = L_TOK + 1024;
constexpr int L_MAIL = L_DUMMY + 1024;
constexpr int LDS_BYTES = L_MAIL + 64 + 704;

#define SB0 __builtin_amdgcn_sched_barrier(0)
template <int N> __device__ __forceinline__ void wait_vm() { asm volatile("s_waitcnt vmcnt(%0)" ::"i"(N) : "memory"); }
__device__ __forceinline__ void wait_lgkm0() { __builtin_amdgcn_s_waitcnt(0xC07F); asm volatile("" ::: "memory"); }
template <int NM, int ND, int DPM = 1, int I = 0> __device__ __forceinline__ void interleave() {
    if constexpr (I * DPM < ND && I < NM) {
        __builtin_amdgcn_sched_group_barrier(0x008, 1, 0);
        __builtin_amdgcn_sched_group_barrier(0x100, (ND - I * DPM) < DPM ? (ND - I * DPM) : DPM, 0);
        interleave<NM, ND, DPM, I + 1>();
    } else if constexpr (I < NM) {
        __builtin_amdgcn_sched_group_barrier(0x008, NM - I, 0);
    }
}
__device__ __forceinline__ void bar() {
    SB0;
    asm volatile("" ::: "memory");
    __builtin_amdgcn_s_barrier();
    asm volatile("" ::: "memory");
    SB0;
}
__device__ __forceinline__ float bf_lo(unsigned u) { return __uint_as_float(u << 16); }
__device__ __forceinline__ float bf_hi(unsigned u) { return __uint_as_float(u & 0xffff0000u); }
__device__ __forceinline__ float bf2f(unsigned short h) { return __uint_as_float((unsigned)h << 16); }
__device__ __forceinline__ unsigned pk_bf16(float a, float b) { bf16x2 v = {(__bf16)a, (__bf16)b}; return __builtin_bit_cast(unsigned, v); }
__device__ __forceinline__ float rbf(float a) { return bf_lo(pk_bf16(a, a)); }
__device__ __forceinline__ f32x2 pfma(f32x2 a, f32x2 b, f32x2 c) { return __builtin_elementwise_fma(a, b, c); }
__device__ __forceinline__ f32x2 splat(float a) { return f32x2{a, a}; }
__device__ __forceinline__ f32x4 mfma(s16x8 a, s16x8 b, f32x4 c) {
    return __builtin_amdgcn_mfma_f32_16x16x32_bf16(__builtin_bit_cast(bf16x8, a), __builtin_bit_cast(bf16x8, b), c, 0, 0, 0);
}
__device__ __forceinline__ void mfma_acc(f32x4& c, s16x8 a, s16x8 b) {
    asm volatile("v_mfma_f32_16x16x32_bf16 %0, %1, %2, %0" : "+v"(c) : "v"(a), "v"(b));
}
__device__ __forceinline__ s16x8 lds_row(LAS char* p) { return *(LAS s16x8*)p; }
__device__ __forceinline__ s16x8 lds_tr(LAS char* p0, LAS char* p1) {
    s16x4 a = __builtin_amdgcn_ds_read_tr16_b64_v4i16((LAS s16x4*)p0);
    s16x4 b = __builtin_amdgcn_ds_read_tr16_b64_v4i16((LAS s16x4*)p1);
    return __builtin_shufflevector(a, b, 0, 1, 2, 3, 4, 5, 6, 7);
}
template <int OFF> __device__ __forceinline__ void dma1(unsigned lds, unsigned voff, const GAS char* base) {
    asm volatile("s_mov_b32 m0, %0\n\ts_nop 0\n\tglobal_load_lds_dwordx4 %1, %2 offset:%3" ::"s"(lds), "v"(voff), "s"(base), "i"(OFF) : "memory");
}
template <int OFF> __device__ __forceinline__ void dma2(unsigned lds, unsigned voff0, unsigned voff1, const GAS char* base) {
    asm volatile("s_mov_b32 m0, %0\n\ts_nop 0\n\tglobal_load_lds_dwordx4 %1, %3\n\tglobal_load_lds_dwordx4 %2, %3 offset:%4"
                 ::"s"(lds), "v"(voff0), "v"(voff1), "s"(base), "i"(OFF) : "memory");
}
template <int OFF> __device__ __forceinline__ void dma2_sc1(unsigned lds, unsigned voff0, unsigned voff1, const GAS char* base) {
    asm volatile("s_mov_b32 m0, %0\n\ts_nop 0\n\tglobal_load_lds_dwordx4 %1, %3 sc1\n\tglobal_load_lds_dwordx4 %2, %3 offset:%4 sc1"
                 ::"s"(lds), "v"(voff0), "v"(voff1), "s"(base), "i"(OFF) : "memory");
}
__device__ __forceinline__ void touch(unsigned lds, const GAS char* a) {
    asm volatile("s_mov_b32 m0, %0\n\ts_nop 0\n\tglobal_load_lds_dword %1, off" ::"s"(lds), "v"(a) : "memory");
}
__device__ __forceinline__ void lds_st32(unsigned a, float v) { asm volatile("ds_write_b32 %0, %1" ::"v"(a), "v"(v) : "memory"); }
__device__ __forceinline__ void lds_st64(unsigned a, f32x2 v) { asm volatile("ds_write_b64 %0, %1" ::"v"(a), "v"(v) : "memory"); }
template <int CTRL, int RM> __device__ __forceinline__ float dpp_add(float v) {
    return v + __uint_as_float(__builtin_amdgcn_update_dpp(0u, __float_as_uint(v), CTRL, RM, 0xf, true));
}
__device__ __forceinline__ float wave_sum(float v) {
    v = dpp_add<0x111, 0xf>(v); v = dpp_add<0x112, 0xf>(v); v = dpp_add<0x114, 0xf>(v); v = dpp_add<0x118, 0xf>(v);
    v = dpp_add<0x142, 0xa>(v); v = dpp_add<0x143, 0xc>(v);
    return __uint_as_float(__builtin_amdgcn_readlane(__float_as_uint(v), 63));
}
__device__ __forceinline__ float sum_groups(float v) {
    const auto a = __builtin_amdgcn_permlane16_swap(__float_as_uint(v), __float_as_uint(v), false, false);
    v = __uint_as_float(a[0]) + __uint_as_float(a[1]);
    const auto b = __builtin_amdgcn_permlane32_swap(__float_as_uint(v), __float_as_uint(v), false, false);
    return __uint_as_float(b[0]) + __uint_as_float(b[1]);
}
__device__ __forceinline__ void store16_sc1(GAS void* p, u32x4 v) {
    asm volatile("global_store_dwordx4 %0, %1, off sc1\n\ts_nop 1" ::"v"(p), "v"(v) : "memory");
}
__device__ __forceinline__ void wait_flags(const GAS unsigned* f, unsigned tag) {
#pragma unroll 1
    for (unsigned spins = 0; spins < SPIN_CAP; ++spins) {
        const unsigned v = __hip_atomic_load(f + (threadIdx.x & 31), __ATOMIC_RELAXED, __HIP_MEMORY_SCOPE_AGENT);
        if (__all(v == tag)) break;
        __builtin_amdgcn_s_sleep(1);
    }
    asm volatile("" ::: "memory");
}
struct cf { float r, i; };
__device__ __forceinline__ cf cmul(cf a, cf b) { return cf{a.r * b.r - a.i * b.i, a.r * b.i + a.i * b.r}; }
__device__ __forceinline__ cf cfma(cf a, cf b, cf c) { return cf{fmaf(a.r, b.r, fmaf(-a.i, b.i, c.r)), fmaf(a.r, b.i, fmaf(a.i, b.r, c.i))}; }
__device__ __forceinline__ float exp_f(float x) {
    const float hi = x * 1.44269504f, lo = fmaf(x, 1.44269504f, -hi) + x * 1.92596299e-8f;
    return __builtin_amdgcn_exp2f(hi) * fmaf(lo, 0.693147181f, 1.0f);
}
__device__ __forceinline__ void sincos_f(float x, float& s, float& c) {
    const float k = rintf(x * 0.636619772f);
    float r = fmaf(k, -1.57079601e+00f, x);
    r = fmaf(k, -3.13916473e-07f, r);
    r = fmaf(k, -5.39030253e-15f, r);
    const float z = r * r;
    const float sp = fmaf(r * z, fmaf(z, fmaf(z, fmaf(z, 2.75573137e-06f, -1.98412698e-04f), 8.33333333e-03f), -1.66666667e-01f), r);
    const float cp = fmaf(z, fmaf(z, fmaf(z, fmaf(z, fmaf(z, -2.75573144e-07f, 2.48015873e-05f), -1.38888889e-03f), 4.16666667e-02f), -0.5f), 1.0f);
    const int q = (int)k;
    const float a = (q & 1) ? cp : sp, b = (q & 1) ? sp : cp;
    s = (q & 2) ? -a : a;
    c = ((q + 1) & 2) ? -b : b;
}
__device__ __forceinline__ unsigned row_std(float v) {
    const float s1 = rbf(__builtin_amdgcn_sqrtf(rbf(rbf(v) + LN_EPS)));
    const float r1 = __builtin_amdgcn_rcpf(s1);
    const float s2 = __builtin_amdgcn_sqrtf(rbf(rbf(v * r1 * r1) + LN_EPS));
    return pk_bf16(s1, s2);
}

typedef const unsigned short* __restrict__ bfp;

__global__ __launch_bounds__(512) void k_fused(bfp x, bfp lam_re, bfp lam_im, bfp bre, bfp bim, bfp cre, bfp cim, bfp log_dt, bfp dskip,
                                               bfp w1, bfp b1, bfp w2, bfp b2, char* ws_, float* __restrict__ out) {
    extern __shared__ __attribute__((aligned(16))) char smem_[];
    LAS char* smem = (LAS char*)smem_;
    GAS char* ws = (GAS char*)ws_;
    const int wave = __builtin_amdgcn_readfirstlane(threadIdx.x >> 6), lane = threadIdx.x & 63;
    const int g = lane >> 4, li = lane & 15;
    const int xg8 = blockIdx.x & 7, loc = blockIdx.x >> 3, ci = 4 * (xg8 >> 1) + (loc >> 3), cj = 8 * (xg8 & 1) + (loc & 7);
    const int t0 = 128 * ci, p0 = 32 * cj, f0 = cj < 11 ? 96 * cj : 1056 + 64 * (cj - 11), nfu = cj < 11 ? 6 : 4;
    const int T = 2 * ci + (cj >> 3), d0 = 64 * (cj & 7);
    const int rot = ((ci + 15) >> 1) & 7;
    unsigned long long entry_pc;
    asm volatile("s_getpc_b64 %0" : "=s"(entry_pc));
    unsigned tag = *(const __attribute__((address_space(4))) unsigned*)(ws_ + WS_EP) + 1u;
    const int q = li >> 2, pp = lane & 3;

    if (__builtin_expect(wave >= 4, 1)) {
        const int lw = wave - 4;
        const unsigned lds0 = (unsigned)(size_t)smem;
        unsigned xo[4], bo[2], wo[3];
#pragma unroll
        for (int i = 0; i < 4; ++i) {
            const int R = 8 * (4 * lw + i) + (lane >> 3), ii = R & 15, bb = (R >> 4) & 3;
            const int tok = t0 + (R & 64) + 16 * (ii >> 2) + 8 * (ii & 1) + 2 * bb + ((ii >> 1) & 1);
            xo[i] = tok * 1024 + 16 * ((lane & 7) ^ (lane >> 3)) + 3072 - 1024 * i;
        }
#pragma unroll
        for (int i = 0; i < 2; ++i) {
            const int R = 8 * (2 * lw + i) + (lane >> 3);
            bo[i] = (p0 + (R & 31)) * 1024 + 16 * ((lane & 7) ^ (lane >> 3)) + 3072 - 1024 * i;
        }
#pragma unroll
        for (int i = 0; i < 3; ++i) {
            const int st = 2 * (3 * lw + i) + (lane >> 5), kg = st / 3, ns = st - 3 * kg, kr = (lane & 31) >> 2;
            const int ch = 4 * ns + ((lane & 3) ^ ((2 * kg + (kr >> 2)) & 3));
            const int f = min(f0 + 8 * ch, FF - 8);
            wo[i] = (8 * kg + kr) * (FF * 2) + f * 2 + 3072 - 1024 * i;
        }
        const GAS char* xg = (const GAS char*)x - 3072;
        const GAS char* bg = (const GAS char*)(lw < 2 ? bre : bim) - 3072;
        const GAS char* wg = (const GAS char*)w1 - 3072;
        const unsigned lx = lds0 + P1_X + lw * 4096, lb = lds0 + P1_B + lw * 2048, lwb = lds0 + P1_W + lw * 3072;
        auto piece = [&](int t, auto I) {
            constexpr int i = decltype(I)::value;
            const unsigned so = (t & 3) * P1_SLOT;
            const int kt = (t + rot) & 7;
            if constexpr (i < 4) dma1<1024 * i>(lx + so, xo[i], xg + kt * 128);
            else if constexpr (i < 6) dma1<1024 * (i - 4)>(lb + so, bo[i - 4], bg + kt * 128);
            else dma1<1024 * (i - 6)>(lwb + so, wo[i - 6], wg + (size_t)kt * (64 * FF * 2));
        };
#define PC(t, i) piece(t, std::integral_constant<int, i>{})
        auto issue = [&](int t) { PC(t, 0); PC(t, 1); PC(t, 2); PC(t, 3); PC(t, 4); PC(t, 5); PC(t, 6); PC(t, 7); PC(t, 8); };
        issue(0); issue(1);
        touch(lds0 + L_DUMMY + 256 * lw, (const GAS char*)(entry_pc & ~255ull) + min(((loc & 3) * 4 + lw) * 13 + min(lane, 12), CODE_LINES - 1) * 128);

        unsigned w2o[2], co[2], ao[2];
#pragma unroll
        for (int i = 0; i < 2; ++i) {
            const int kg = 2 * lw + i;
            w2o[i] = (8 * kg + ((lane & 31) >> 2)) * (DM * 2) + (d0 + 32 * (lane >> 5) + 8 * (lane & 3)) * 2 + 3072 - 1056 * i;
            co[i] = (d0 + 8 * kg + (lane >> 3)) * (NP * 2) + 16 * ((lane & 7) ^ (lane >> 3)) + 3072 - 1024 * i;
            ao[i] = kg * 1024 + lane * 16 + 3072 - 1056 * i;
        }
        const GAS char* ag = ws + WS_ACT + (size_t)T * ACT_TILE;
        const GAS char* w2g = (const GAS char*)w2 - 3072;
        const unsigned l2 = lds0 + lw * 2080, l2c = lds0 + P2_IMG + lw * 2048;
        auto dmaA = [&](const GAS char* src, unsigned slot) { dma2_sc1<1056>(l2 + slot, ao[0], ao[1], src - 3072); };
        auto dmaW = [&](const GAS char* src, unsigned slot) { dma2<1056>(l2 + slot + P2_IMG, w2o[0], w2o[1], src); };
        auto dmaC = [&](const GAS char* src, unsigned slot) { dma2<1024>(l2c + slot, co[0], co[1], src); };
        auto preW = [&](int s) { dmaW(w2g + s * 65536, s * P2_SLOT); };
        auto preA = [&](int s) { dmaA(ag + s * 8192, s * P2_SLOT); };

        const int n1 = 75 * blockIdx.x + 19 * lw + min(lane, 18);
        const GAS char* l1a = n1 < 11008 ? (const GAS char*)w2 + n1 * 128
                                         : (n1 < 15104 ? (const GAS char*)cre + (n1 - 11008) * 128 : (const GAS char*)cim + (min(n1, 19199) - 15104) * 128);
        const GAS u32x4* rsp = (const GAS u32x4*)(ws + WS_RS) + t0 + 64 * (lw & 1) + lane;
        const GAS u32x4* csp = (const GAS u32x4*)(ws + WS_CS) + ((cj * 16 + 8 * (lw >> 1) + 2 * g) * 2 + (lw & 1)) * 32 + li;
        asm volatile("" : "+v"(l1a), "+v"(rsp), "+v"(csp));
        asm volatile("" : "+s"(tag));
        wait_vm<10>();
        bar();
        issue(2);
        touch(lds0 + L_DUMMY + 256 * lw, l1a);
        wait_vm<11>(); bar();
        issue(3);
        {
            f32x2 v;
            asm volatile("ds_read_b64 %0, %1\n\ts_waitcnt lgkmcnt(0)" : "=&v"(v) : "v"(lds0 + L_MAIL + 16 * lw + 8 * (lane & 1)) : "memory");
            if (lane < 2) store16_sc1((GAS u32x4*)(ws + WS_RS) + t0 + 8 * cj + 2 * lw + lane, u32x4{__float_as_uint(v[0]), tag, __float_as_uint(v[1]), tag});
        }
        wait_vm<11>(); bar();
        issue(4);
        {
            float a, b, c;
            const unsigned ma = lds0 + L_MAIL + 64 + 4 * (((lw >> 1) * 5 + 3 * (lw & 1)) * 16 + li);
            asm volatile("ds_read_b32 %0, %3\n\tds_read_b32 %1, %3 offset:64\n\tds_read_b32 %2, %3 offset:128\n\ts_waitcnt lgkmcnt(0)"
                         : "=&v"(a), "=&v"(b), "=&v"(c) : "v"(ma) : "memory");
            if (g == 0) store16_sc1((GAS u32x4*)(ws + WS_CS) + ((cj * 16 + ci) * 2 + (lw >> 1)) * 32 + 16 * (lw & 1) + li,
                                    u32x4{__float_as_uint(a), __float_as_uint(b), __float_as_uint(c), tag});
        }
        wait_vm<11>(); bar();
        issue(5); wait_vm<10>(); bar();
        issue(6); wait_vm<9>(); bar();
        issue(7);
        u32x4 rs, cs[4];
        auto gather = [&]() {
            asm volatile("global_load_dwordx4 %0, %5, off sc1\n\t"
                         "global_load_dwordx4 %1, %6, off sc1\n\tglobal_load_dwordx4 %2, %6, off offset:256 sc1\n\t"
                         "global_load_dwordx4 %3, %6, off offset:1024 sc1\n\tglobal_load_dwordx4 %4, %6, off offset:1280 sc1"
                         : "=&v"(rs), "=&v"(cs[0]), "=&v"(cs[1]), "=&v"(cs[2]), "=&v"(cs[3]) : "v"(rsp), "v"(csp) : "memory");
        };
        gather();
        wait_vm<14>(); bar();
        preW(0); preW(1); preW(2); preW(3);
        asm volatile("s_waitcnt vmcnt(8)" : "+v"(rs), "+v"(cs[0]), "+v"(cs[1]), "+v"(cs[2]), "+v"(cs[3])::"memory");
        bar();
        preW(4); preW(5);
        auto fresh = [&]() {
            return __all(rs[1] == tag && rs[3] == tag && cs[0][3] == tag && cs[1][3] == tag && cs[2][3] == tag && cs[3][3] == tag);
        };
#pragma unroll 1
        for (unsigned spins = 0; spins < SPIN_CAP && !fresh(); ++spins) {
            gather();
            asm volatile("s_waitcnt vmcnt(0)" : "+v"(rs), "+v"(cs[0]), "+v"(cs[1]), "+v"(cs[2]), "+v"(cs[3])::"memory");
        }
        if (lw < 2) {
            const float m = __uint_as_float(rs[0]), mb = rbf(m);
            const float r1 = __builtin_amdgcn_rcpf(bf_lo(rs[2])), r12 = r1 * __builtin_amdgcn_rcpf(bf_hi(rs[2]));
            const int tau = 64 * lw + lane;
            const unsigned ta = lds0 + L_TAB1 + 4 * (((tau >> 4) * 8 + (tau & 7)) * 4 + ((tau >> 3) & 1));
            lds_st32(ta, r1); lds_st32(ta + 8, -r1 * mb);
            lds_st32(ta + 1024, r12); lds_st32(ta + 1032, -r12 * m);
            lds_st64(lds0 + L_TOK + 8 * tau, f32x2{mb, r1});
        }
        {
            float v5[5];
#pragma unroll
            for (int k = 0; k < 3; ++k) v5[k] = __uint_as_float(cs[0][k]) + __uint_as_float(cs[2][k]);
#pragma unroll
            for (int k = 0; k < 2; ++k) v5[3 + k] = __uint_as_float(cs[1][k]) + __uint_as_float(cs[3][k]);
#pragma unroll
            for (int k = 0; k < 5; ++k) v5[k] = sum_groups(v5[k]);
            if (g == 0) {
#pragma unroll
                for (int k = 0; k < 5; ++k) lds_st32(lds0 + L_CS + 4 * ((((lw >> 1) * 2 + (lw & 1)) * 5 + k) * 16 + li), v5[k]);
            }
        }
        asm volatile("s_waitcnt lgkmcnt(0)" ::: "memory");
        bar();
        preW(6); preW(7); preW(8);
        bar();
        bar();
        wait_flags((const GAS unsigned*)(ws + WS_FH) + T * 32, tag);
        preA(0); preA(1); preA(2); preA(3);
        wait_vm<4>(); bar();
        preA(4); preA(5); preA(6); preA(7);
        wait_vm<8>(); bar();
        const GAS char* ap = ag + 9 * 8192;
        const GAS char* wp = w2g + 9 * 65536;
        unsigned slot = 0;
        auto adv = [&]() { ap += 8192; slot = slot == P2_RING - P2_SLOT ? 0 : slot + P2_SLOT; };
        auto stW = [&]() { dmaA(ap, slot); dmaW(wp, slot); wp += 65536; adv(); };
        auto stC = [&]() { dmaA(ap, slot); dmaC(wp, slot); wp += 128; adv(); };
        preA(8); stW();
        wait_vm<10>(); bar(); stW(); stW();
        wait_vm<14>(); bar(); stW(); stW();
#pragma unroll 1
        for (int j = 4; j < 7; ++j) { wait_vm<16>(); bar(); stW(); stW(); }
        wait_vm<16>(); bar(); stW();
        ap -= 4096; wp -= 32768;
        stW();
        wp = (const GAS char*)cre - 3072;
        wait_vm<16>(); bar();
        stC(); stC();
#pragma unroll 1
        for (int j = 9; j < 12; ++j) { wait_vm<16>(); bar(); stC(); stC(); }
        wp = (const GAS char*)cim - 3072;
#pragma unroll 1
        for (int j = 12; j < 16; ++j) { wait_vm<16>(); bar(); stC(); stC(); }
        wait_vm<16>(); bar();
        wait_vm<8>(); bar();
        wait_vm<0>(); bar();
        return;
    }

    const int wm = wave >> 1, wn = wave & 1;
    const int p = p0 + 16 * wn + li;
    cf lam = {0.0f, 0.0f}, l8 = lam, l16 = lam, l128 = lam, lsig = lam, coef2 = lam;
    float b1c[3], p_lr, p_lim, p_ldt;
    {
        u32x4 xr[2];
        unsigned short pr[6];
#pragma unroll
        for (int i = 0; i < 2; ++i) xr[i] = *(const u32x4*)(x + (t0 + 8 * cj + 2 * wave + i) * DM + lane * 8);
        pr[0] = lam_re[p]; pr[1] = lam_im[p]; pr[2] = log_dt[p];
#pragma unroll
        for (int c = 0; c < 3; ++c) pr[3 + c] = b1[min(f0 + 16 * (3 * wn + c) + li, FF - 1)];
        SB0;
        p_lr = bf2f(pr[0]); p_lim = bf2f(pr[1]); p_ldt = bf2f(pr[2]);
#pragma unroll
        for (int c = 0; c < 3; ++c) b1c[c] = bf2f(pr[3 + c]);
        float mean[2], var[2];
#pragma unroll
        for (int i = 0; i < 2; ++i) {
            float e[8], s = 0.0f;
#pragma unroll
            for (int j = 0; j < 4; ++j) { e[2 * j] = bf_lo(xr[i][j]); e[2 * j + 1] = bf_hi(xr[i][j]); s += e[2 * j] + e[2 * j + 1]; }
            mean[i] = wave_sum(s) * (1.0f / DM);
            float qq = 0.0f;
#pragma unroll
            for (int j = 0; j < 8; ++j) qq = fmaf(e[j] - mean[i], e[j] - mean[i], qq);
            var[i] = wave_sum(qq) * (1.0f / DM);
        }
        if (lane < 2) ((LAS f32x2*)(smem + L_MAIL))[2 * wave + lane] = f32x2{lane ? mean[1] : mean[0], __uint_as_float(row_std(lane ? var[1] : var[0]))};
        asm volatile("" : "+v"(b1c[0]), "+v"(b1c[1]), "+v"(b1c[2]), "+v"(p_lr), "+v"(p_lim), "+v"(p_ldt));
    }
    auto lam_setup = [&]() {
        const float lr = p_lr, lim = p_lim, dt = rbf(exp_f(p_ldt));
        const float mag = exp_f(dt * lr);
        float sn, cs;
        sincos_f(dt * lim, sn, cs);
        lam = cf{mag * cs, mag * sn};
        const float nr = lam.r - 1.0f, ni = lam.i, den = 1.0f / (lr * lr + lim * lim);
        coef2 = cf{2.0f * (nr * lr + ni * lim) * den, 2.0f * (ni * lr - nr * lim) * den};
        const cf l2 = cmul(lam, lam), l4 = cmul(l2, l2);
        l8 = cmul(l4, l4); l16 = cmul(l8, l8);
        const cf l32 = cmul(l16, l16), l64 = cmul(l32, l32);
        l128 = cmul(l64, l64);
        lsig = (g & 1) ? l16 : cf{1.0f, 0.0f};
        if (g & 2) lsig = cmul(lsig, l32);
        if (wm) lsig = cmul(lsig, l64);
        asm volatile("" : "+v"(lam.r), "+v"(lam.i), "+v"(l8.r), "+v"(l8.i), "+v"(l16.r), "+v"(l16.i), "+v"(l128.r), "+v"(l128.i), "+v"(lsig.r), "+v"(lsig.i),
                          "+v"(coef2.r), "+v"(coef2.i));
    };
    f32x4 acc[4][5];
    {
        const int sw = 16 * (g ^ (lane & 7));
        const int xa0 = P1_X + (64 * wm + li) * 128 + sw;
        const int ba0 = P1_B + (16 * wn + li) * 128 + sw;
        int wa[2][2];
#pragma unroll
        for (int t = 0; t < 2; ++t)
#pragma unroll
            for (int cl = 0; cl < 2; ++cl) {
                const int par = (3 * wn + cl) & 1;
                wa[t][cl] = P1_W + 1536 * g + 64 * q + 256 * t + 32 * (par ^ (g & 1)) + 16 * ((pp >> 1) ^ t) + 8 * (pp & 1) + (cl == 0 ? 512 * wn : 1024 * wn);
            }
#pragma unroll
        for (int b = 0; b < 4; ++b)
#pragma unroll
            for (int c = 0; c < 5; ++c) acc[b][c] = f32x4{0, 0, 0, 0};
        s16x8 xa[2][4], wb[2][5];
        auto rd = [&](int slot, int ks, int buf) {
            LAS char* s = smem + slot * P1_SLOT;
            LAS char* xs = s + (xa0 ^ (64 * ks));
            LAS char* bs = s + (ba0 ^ (64 * ks));
#pragma unroll
            for (int b = 0; b < 4; ++b) xa[buf][b] = lds_row(xs + b * 2048);
            wb[buf][0] = lds_row(bs);
            wb[buf][1] = lds_row(bs + 4096);
            wb[buf][2] = lds_tr(s + wa[0][0] + 6144 * ks, s + wa[1][0] + 6144 * ks);
            wb[buf][3] = lds_tr(s + wa[0][1] + 6144 * ks, s + wa[1][1] + 6144 * ks);
            wb[buf][4] = lds_tr(s + wa[0][0] + 6144 * ks + 512, s + wa[1][0] + 6144 * ks + 512);
        };
        auto mm = [&](int buf) {
#pragma unroll
            for (int c = 0; c < 5; ++c)
#pragma unroll
                for (int b = 0; b < 4; ++b) acc[b][c] = mfma(xa[buf][b], wb[buf][c], acc[b][c]);
        };
        lam_setup();
        auto colsum = [&](int buf) {
            const s16x8 ones = {0x3f80, 0x3f80, 0x3f80, 0x3f80, 0x3f80, 0x3f80, 0x3f80, 0x3f80};
            float v5[5];
#pragma unroll
            for (int c = 0; c < 5; ++c) v5[c] = mfma(ones, wb[buf][c], f32x4{0, 0, 0, 0})[0];
            if (g == 0) {
#pragma unroll
                for (int c = 0; c < 5; ++c) ((LAS float*)(smem + L_MAIL + 64))[(wn * 5 + c) * 16 + li] = v5[c];
            }
        };
        asm volatile("" : "+s"(tag));
        wait_lgkm0();
        bar();
        rd(0, 0, 0);
        wait_lgkm0();
#pragma unroll 1
        for (int seg = 0, t = 0; seg < 2; ++seg) {
            const int tend = seg ? 7 : 1;
#pragma unroll 1
            for (; t < tend; ++t) {
                SB0;
                rd(t & 3, 1, 1); mm(0); interleave<20, 12>();
                SB0; wait_lgkm0(); bar();
                rd((t + 1) & 3, 0, 0); mm(1); interleave<20, 12>();
                SB0; wait_lgkm0();
            }
            if (seg == 0 && wm == 0) { if (ci & 1) colsum(1); else colsum(0); }
        }
        rd(3, 1, 1); mm(0); interleave<20, 12>();
        SB0; wait_lgkm0(); bar();
        mm(1);
    }

    LAS f32x4* tab1 = (LAS f32x4*)(smem + L_TAB1) + (wm * 4 + g) * 8;
    LAS f32x4* tab2 = (LAS f32x4*)(smem + L_TAB2) + (wm * 4 + g) * 8;
    auto csum = [&](int k) { return ((LAS float*)(smem + L_CS))[(wn * 5 + k) * 16 + li] + ((LAS float*)(smem + L_CS))[((2 + wn) * 5 + k) * 16 + li]; };
    u32x2 xv[2][2], dv[2], bv[2];
#pragma unroll
    for (int i = 0; i < 2; ++i) {
        const int d = d0 + 32 * (wave >> 1) + 16 * i + 4 * g;
        dv[i] = *(const u32x2*)(dskip + d); bv[i] = *(const u32x2*)(b2 + d);
#pragma unroll
        for (int j = 0; j < 2; ++j) xv[i][j] = *(const u32x2*)(x + (64 * T + 32 * j + 16 * ((lane >> 3) & 1) + 8 * (wave & 1) + (lane & 7)) * DM + d);
    }
    const int Tw = 2 * ci + wm;
    const __amdgpu_buffer_rsrc_t act = __builtin_amdgcn_make_buffer_rsrc((void*)(ws_ + WS_ACT), 0, 32 * ACT_TILE, 0x00020000);
    const unsigned act0 = Tw * ACT_TILE;
    {
        f32x4 tb[8];
#pragma unroll
        for (int j = 0; j < 8; ++j) tb[j] = tab2[j];
        constexpr float C1 = -2.30220819f;
        constexpr float C2 = C1 * 0.044715f;
#pragma unroll
        for (int c = 0; c < 3; ++c) {
            const int col = 16 * (3 * wn + c) + li;
            const float s1c = csum(2 + c);
            f32x2 y[8];
#pragma unroll
            for (int j = 0; j < 8; ++j) {
                const int b = j >> 1, r = 2 * (j & 1);
                const f32x2 a = {tb[j][0], tb[j][1]}, bb = {tb[j][2], tb[j][3]};
                const f32x2 z = pfma(a, f32x2{acc[b][2 + c][r], acc[b][2 + c][r + 1]}, pfma(bb, splat(s1c), splat(b1c[c])));
                const f32x2 u = z * pfma(z * z, splat(C2), splat(C1));
                const f32x2 d = f32x2{__builtin_amdgcn_exp2f(u[0]), __builtin_amdgcn_exp2f(u[1])} + splat(1.0f);
                y[j] = z * f32x2{__builtin_amdgcn_rcpf(d[0]), __builtin_amdgcn_rcpf(d[1])};
            }
            if (3 * wn + c < nfu) {
                const int k = f0 + col;
                const unsigned dst = act0 + (k >> 3) * 1024 + (k & 7) * 64 + 16 * g;
#pragma unroll
                for (int sgm = 0; sgm < 2; ++sgm)
                    __builtin_amdgcn_raw_buffer_store_b128(u32x4{pk_bf16(y[0][sgm], y[1][sgm]), pk_bf16(y[2][sgm], y[3][sgm]), pk_bf16(y[4][sgm], y[5][sgm]), pk_bf16(y[6][sgm], y[7][sgm])},
                                                           act, dst + 512 * sgm, 0, AUX_SC1);
            }
        }
    }

    f32x2 ure[8], uim[8];
    {
        const float sbr = csum(0), sbi = csum(1);
#pragma unroll
        for (int j = 0; j < 8; ++j) {
            const f32x4 tb = tab1[j];
            const f32x2 a = {tb[0], tb[1]}, bb = {tb[2], tb[3]};
            const int b = j >> 1, r = 2 * (j & 1);
            ure[j] = pfma(a, f32x2{acc[b][0][r], acc[b][0][r + 1]}, bb * splat(sbr));
            uim[j] = pfma(a, f32x2{acc[b][1][r], acc[b][1][r + 1]}, bb * splat(sbi));
        }
    }
    f32x2 hr = ure[0], hi = uim[0];
#pragma unroll
    for (int j = 1; j < 8; ++j) {
        const f32x2 tr = pfma(splat(lam.r), hr, ure[j]), ti = pfma(splat(lam.r), hi, uim[j]);
        const f32x2 nr = pfma(splat(-lam.i), hi, tr), ni = pfma(splat(lam.i), hr, ti);
        hr = nr; hi = ni;
    }
    const cf eA = cf{hr[0], hi[0]};
    const cf e = cfma(l8, eA, cf{hr[1], hi[1]});
    const int sig = 4 * wm + g;
    LAS f32x2* eL = (LAS f32x2*)(smem + L_EL);
    eL[sig * 32 + 16 * wn + li] = f32x2{e.r, e.i};
    wait_lgkm0();
    bar();
    cf Lp = cf{0.0f, 0.0f};
#pragma unroll
    for (int s = 0; s < 7; ++s) {
        const f32x2 v = eL[s * 32 + 16 * wn + li];
        const cf n = cfma(l16, Lp, cf{v[0], v[1]});
        if (s < sig) Lp = n;
    }
    wait_vm<0>();
    if (lane == 0) __hip_atomic_store((GAS unsigned*)(ws + WS_FH) + Tw * 32 + 2 * cj + wn, tag, __ATOMIC_RELAXED, __HIP_MEMORY_SCOPE_AGENT);
    GAS u32x4* eg = (GAS u32x4*)(ws + WS_EG);
    if (sig == 7) {
        const cf E = cfma(l16, Lp, e);
        store16_sc1(eg + ci * NP + p, u32x4{__float_as_uint(E.r), tag, __float_as_uint(E.i), tag});
    }
    bar();

    const int wd = wave >> 1, wt = wave & 1;
    f32x4 o[2][2];
    cf Hin = cf{0.0f, 0.0f};
    {
#pragma unroll
        for (int j = 0; j < 2; ++j) {
            const f32x2 st = ((LAS f32x2*)(smem + L_TOK))[64 * (T & 1) + 32 * j + 16 * ((lane >> 3) & 1) + 8 * wt + (lane & 7)];
#pragma unroll
            for (int i = 0; i < 2; ++i) {
                const float xe[4] = {bf_lo(xv[i][j][0]), bf_hi(xv[i][j][0]), bf_lo(xv[i][j][1]), bf_hi(xv[i][j][1])};
                const float de[4] = {bf_lo(dv[i][0]), bf_hi(dv[i][0]), bf_lo(dv[i][1]), bf_hi(dv[i][1])};
                const float be[4] = {bf_lo(bv[i][0]), bf_hi(bv[i][0]), bf_lo(bv[i][1]), bf_hi(bv[i][1])};
#pragma unroll
                for (int k = 0; k < 4; ++k) {
                    const float xn = rbf(rbf(xe[k] - st[0]) * st[1]);
                    o[i][j][k] = xe[k] + rbf(xn * de[k]) + be[k];
                }
            }
        }
        asm volatile("" : "+v"(o[0][0]), "+v"(o[0][1]), "+v"(o[1][0]), "+v"(o[1][1]));
        const GAS u32x4* ep = eg + g * NP + p;
        u32x4 gr[4];
        auto fetch = [&]() {
            asm volatile("global_load_dwordx4 %0, %4, off sc1\n\tglobal_load_dwordx4 %1, %5, off sc1\n\t"
                         "global_load_dwordx4 %2, %6, off sc1\n\tglobal_load_dwordx4 %3, %7, off sc1"
                         : "=&v"(gr[0]), "=&v"(gr[1]), "=&v"(gr[2]), "=&v"(gr[3])
                         : "v"(ep), "v"(ep + 4 * NP), "v"(ep + 8 * NP), "v"(ep + 12 * NP) : "memory");
        };
        auto landed = [&]() { asm volatile("s_waitcnt vmcnt(0)" : "+v"(gr[0]), "+v"(gr[1]), "+v"(gr[2]), "+v"(gr[3])::"memory"); };
        auto complete = [&]() {
            bool ok = true;
#pragma unroll
            for (int i = 0; i < 4; ++i) ok &= (4 * i + g >= ci) | ((gr[i][1] == tag) & (gr[i][3] == tag));
            return __all(ok);
        };
        fetch();
        const cf l256 = cmul(l128, l128), l512 = cmul(l256, l256);
        landed();
#pragma unroll 1
        for (unsigned spins = 0; spins < SPIN_CAP && !complete(); ++spins) { __builtin_amdgcn_s_sleep(2); fetch(); landed(); }
        cf hp = cf{0.0f, 0.0f};
#pragma unroll
        for (int i = 0; i < 4; ++i) {
            const cf n = cfma(l512, hp, cf{__uint_as_float(gr[i][0]), __uint_as_float(gr[i][2])});
            if (4 * i + g < ci) hp = n;
        }
        const int rr = (ci - 1 - g) & 3;
        if (rr & 1) hp = cmul(hp, l128);
        if (rr & 2) hp = cmul(hp, l256);
        Hin = cf{sum_groups(hp.r), sum_groups(hp.i)};
    }
    u32x4 hq[2][2];
    unsigned hdr, hdi;
    auto store_h = [&]() {
#pragma unroll
        for (int sgm = 0; sgm < 2; ++sgm) {
            __builtin_amdgcn_raw_buffer_store_b128(hq[0][sgm], act, hdr + 512 * sgm, 0, AUX_SC1);
            __builtin_amdgcn_raw_buffer_store_b128(hq[1][sgm], act, hdi + 512 * sgm, 0, AUX_SC1);
        }
    };
    {
        const cf cA = cfma(lsig, Hin, Lp);
        const cf cB = cfma(l8, cA, eA);
        f32x2 sr = {cA.r, cB.r}, si = {cA.i, cB.i};
        f32x2 ore[8], oim[8];
#pragma unroll
        for (int j = 0; j < 8; ++j) {
            const f32x2 tr = pfma(splat(lam.r), sr, ure[j]), ti = pfma(splat(lam.r), si, uim[j]);
            const f32x2 nr = pfma(splat(-lam.i), si, tr), ni = pfma(splat(lam.i), sr, ti);
            sr = nr; si = ni;
            ore[j] = pfma(splat(coef2.r), sr, splat(-coef2.i) * si);
            oim[j] = pfma(splat(-coef2.r), si, splat(-coef2.i) * sr);
        }
        const int kr = FF + p, kim = FF + NP + p;
        hdr = act0 + (kr >> 3) * 1024 + (kr & 7) * 64 + 16 * g;
        hdi = act0 + (kim >> 3) * 1024 + (kim & 7) * 64 + 16 * g;
#pragma unroll
        for (int sgm = 0; sgm < 2; ++sgm) {
            hq[0][sgm] = u32x4{pk_bf16(ore[0][sgm], ore[1][sgm]), pk_bf16(ore[2][sgm], ore[3][sgm]), pk_bf16(ore[4][sgm], ore[5][sgm]), pk_bf16(ore[6][sgm], ore[7][sgm])};
            hq[1][sgm] = u32x4{pk_bf16(oim[0][sgm], oim[1][sgm]), pk_bf16(oim[2][sgm], oim[3][sgm]), pk_bf16(oim[4][sgm], oim[5][sgm]), pk_bf16(oim[6][sgm], oim[7][sgm])};
        }
        store_h();
    }

    {
        const int trb = (g >> 1) * 2080 + (g & 1) * 1056 + 64 * q + 16 * (pp >> 1) + 8 * (pp & 1);
        const int tra = trb + 512 * wt, trw = P2_IMG + trb + 512 * wd;
        const int ca0 = P2_IMG + (32 * wd + li) * 128 + 16 * (g ^ (lane & 7));
        s16x8 wf[2][2][2], af[2][2][2];
        int slot = 0;
        auto next = [&]() { slot = slot == P2_RING - P2_SLOT ? 0 : slot + P2_SLOT; };
        auto frag_a = [&](LAS char* sl, int buf, int ks, int i) { LAS char* a = sl + tra + 4160 * ks + 32 * i; af[buf][ks][i] = lds_tr(a, a + 256); };
        auto frag_w = [&](LAS char* sl, int buf, int ks, int i, bool g4) {
            if (g4) { LAS char* a = sl + trw + 4160 * ks + 32 * i; wf[buf][ks][i] = lds_tr(a, a + 256); }
            else wf[buf][ks][i] = lds_row(sl + ((ca0 ^ (64 * ks)) + 2048 * i));
        };
        auto half = [&](int rb, bool rd, bool g4, int k0, bool mm_) {
            LAS char* sl = smem + slot;
            const int ub = rb ^ 1;
#pragma unroll
            for (int ks = 0; ks < 2; ++ks) {
#pragma unroll
                for (int m = 0; m < 4; ++m) {
                    if (mm_ && ks >= k0) mfma_acc(o[m >> 1][m & 1], wf[ub][ks][m >> 1], af[ub][ks][m & 1]);
                    SB0;
                    if (rd) { if (m < 2) frag_a(sl, rb, ks, m); else frag_w(sl, rb, ks, m - 2, g4); }
                    SB0;
                }
            }
            if (rd) next();
        };
        const GAS unsigned* fsp = (const GAS unsigned*)(ws + WS_FS) + T * 32 + (lane & 31);
        const unsigned fsl = (unsigned)(size_t)smem + L_EL + 256 * wave;
        bar(); half(0, true, true, 0, false);
        half(1, true, true, 0, true);
#pragma unroll 1
        for (int seg = 0, j = 1; seg < 4; ++seg) {
            const int jend = seg == 0 ? 2 : (seg == 1 ? 7 : (seg == 2 ? 8 : 10));
#pragma unroll 1
            for (; j < jend; ++j) {
                bar(); half(0, true, true, 0, true);
                half(1, true, true, 0, true);
            }
            if (seg == 0) {
                wait_vm<0>();
                if (lane == 0) __hip_atomic_store((GAS unsigned*)(ws + WS_FS) + Tw * 32 + 2 * cj + wn, tag, __ATOMIC_RELAXED, __HIP_MEMORY_SCOPE_AGENT);
            } else if (seg == 1) {
                asm volatile("s_mov_b32 m0, %0\n\ts_nop 0\n\tglobal_load_lds_dword %1, off sc1" ::"s"(fsl), "v"(fsp) : "memory");
            } else if (seg == 2) {
                unsigned fsv;
                asm volatile("s_waitcnt vmcnt(0)\n\tds_read_b32 %0, %1\n\ts_waitcnt lgkmcnt(0)" : "=&v"(fsv) : "v"(fsl + 4 * lane) : "memory");
                if (!__all(fsv == tag)) wait_flags((const GAS unsigned*)(ws + WS_FS) + T * 32, tag);
            }
            SB0;
        }
        bar(); half(0, true, true, 0, true);
        half(1, true, true, 0, true);
        bar(); half(0, true, false, 1, true);
        half(1, true, false, 0, true);
#pragma unroll 1
        for (int j = 12; j < 19; ++j) {
            bar(); half(0, true, false, 0, true);
            half(1, true, false, 0, true);
        }
        half(0, false, false, 0, true);
        asm volatile("s_nop 7\n\ts_nop 7" : "+v"(o[0][0]), "+v"(o[0][1]), "+v"(o[1][0]), "+v"(o[1][1]));

#pragma unroll
        for (int j = 0; j < 2; ++j)
#pragma unroll
            for (int i = 0; i < 2; ++i)
                *(f32x4*)(out + (64 * T + 32 * j + 16 * ((lane >> 3) & 1) + 8 * wt + (lane & 7)) * DM + d0 + 32 * wd + 16 * i + 4 * g) = o[i][j];
        if (ci == 15 && cj == 0 && threadIdx.x == 0) __hip_atomic_store((GAS unsigned*)(ws + WS_EP), tag, __ATOMIC_RELAXED, __HIP_MEMORY_SCOPE_AGENT);
    }
}

}

extern "C" void kernel_launch(void* const* d_in, const int* in_sizes, int n_in, void* d_out, int out_size, void* d_ws, size_t ws_size,
                              hipStream_t stream) {
    typedef const unsigned short* P;
    (void)hipFuncSetAttribute((const void*)k_fused, hipFuncAttributeMaxDynamicSharedMemorySize, LDS_BYTES);
    k_fused<<<256, 512, LDS_BYTES, stream>>>((P)d_in[0], (P)d_in[1], (P)d_in[2], (P)d_in[3], (P)d_in[4], (P)d_in[5], (P)d_in[6], (P)d_in[7],
                                             (P)d_in[8], (P)d_in[10], (P)d_in[11], (P)d_in[12], (P)d_in[13], (char*)d_ws, (float*)d_out);
}
```

```cpp
#include <hip/hip_runtime.h>
#include <hip/hip_bf16.h>
#include <stdint.h>
#include <type_traits>

#define LAS __attribute__((address_space(3)))
#define GAS __attribute__((address_space(1)))

typedef __attribute__((ext_vector_type(8))) short s16x8;
typedef __attribute__((ext_vector_type(4))) short s16x4;
typedef __attribute__((ext_vector_type(8))) __bf16 bf16x8;
typedef __attribute__((ext_vector_type(2))) __bf16 bf16x2;
typedef __attribute__((ext_vector_type(4))) float f32x4;
typedef __attribute__((ext_vector_type(2))) float f32x2;
typedef __attribute__((ext_vector_type(4))) unsigned u32x4;
typedef __attribute__((ext_vector_type(2))) unsigned u32x2;

namespace {

constexpr int SEQ = 2048, DM = 512, NP = 512, FF = 1376;
constexpr int KACT = FF + 2 * NP;
constexpr int ACT_TILE = KACT * 128;
constexpr unsigned WS_ACT = 0;
constexpr unsigned WS_RS = 10u << 20;
constexpr unsigned WS_EG = WS_RS + (64u << 10);
constexpr unsigned WS_FH = WS_EG + (256u << 10);
constexpr unsigned WS_FS = WS_FH + (8u << 10);
constexpr unsigned WS_CS = WS_FS + (8u << 10);
constexpr unsigned WS_EP = WS_CS + (256u << 10);
constexpr float LN_EPS = 1e-5f;
constexpr unsigned SPIN_CAP = 1u << 18;
constexpr int AUX_SC1 = 16;
constexpr int CODE_LINES = 192;

constexpr int P1_SLOT = 36 * 1024, P1_X = 0, P1_B = 16384, P1_W = 24576;
constexpr int P2_IMG = 4 * 2080, P2_SLOT = 2 * P2_IMG, P2_RING = 9 * P2_SLOT;
constexpr int L_TAB1 = P2_RING;
constexpr int L_TAB2 = L_TAB1 + 1024;
constexpr int L_EL = L_TAB2 + 1024;
constexpr int L_CS = L_EL + 2048;
constexpr int L_TOK = L_CS + 1280;
constexpr int L_DUMMY = L_TOK + 1024;
constexpr int L_MAIL = L_DUMMY + 1024;
constexpr int LDS_BYTES = L_MAIL + 64 + 704;

#define SB0 __builtin_amdgcn_sched_barrier(0)
template <int N> __device__ __forceinline__ void wait_vm() { asm volatile("s_waitcnt vmcnt(%0)" ::"i"(N) : "memory"); }
template <int N> __device__ __forceinline__ unsigned epoch_tag(unsigned epoch) {
    unsigned e;
    asm volatile("s_waitcnt vmcnt(%2)\n\tv_readfirstlane_b32 %0, %1\n\ts_nop 1" : "=s"(e) : "v"(epoch), "i"(N) : "memory");
    return e + 1u;
}
__device__ __forceinline__ void wait_lgkm0() { __builtin_amdgcn_s_waitcnt(0xC07F); asm volatile("" ::: "memory"); }
template <int NM, int ND, int DPM = 1, int I = 0> __device__ __forceinline__ void interleave() {
    if constexpr (I * DPM < ND && I < NM) {
        __builtin_amdgcn_sched_group_barrier(0x008, 1, 0);
        __builtin_amdgcn_sched_group_barrier(0x100, (ND - I * DPM) < DPM ? (ND - I * DPM) : DPM, 0);
        interleave<NM, ND, DPM, I + 1>();
    } else if constexpr (I < NM) {
        __builtin_amdgcn_sched_group_barrier(0x008, NM - I, 0);
    }
}
__device__ __forceinline__ void bar() {
    SB0;
    asm volatile("" ::: "memory");
    __builtin_amdgcn_s_barrier();
    asm volatile("" ::: "memory");
    SB0;
}
__device__ __forceinline__ float bf_lo(unsigned u) { return __uint_as_float(u << 16); }
__device__ __forceinline__ float bf_hi(unsigned u) { return __uint_as_float(u & 0xffff0000u); }
__device__ __forceinline__ float bf2f(unsigned short h) { return __uint_as_float((unsigned)h << 16); }
__device__ __forceinline__ unsigned pk_bf16(float a, float b) { bf16x2 v = {(__bf16)a, (__bf16)b}; return __builtin_bit_cast(unsigned, v); }
__device__ __forceinline__ float rbf(float a) { return bf_lo(pk_bf16(a, a)); }
__device__ __forceinline__ f32x2 pfma(f32x2 a, f32x2 b, f32x2 c) { return __builtin_elementwise_fma(a, b, c); }
__device__ __forceinline__ f32x2 splat(float a) { return f32x2{a, a}; }
__device__ __forceinline__ f32x4 mfma(s16x8 a, s16x8 b, f32x4 c) {
    return __builtin_amdgcn_mfma_f32_16x16x32_bf16(__builtin_bit_cast(bf16x8, a), __builtin_bit_cast(bf16x8, b), c, 0, 0, 0);
}
__device__ __forceinline__ void mfma_acc(f32x4& c, s16x8 a, s16x8 b) {
    asm volatile("v_mfma_f32_16x16x32_bf16 %0, %1, %2, %0" : "+v"(c) : "v"(a), "v"(b));
}
__device__ __forceinline__ s16x8 lds_row(LAS char* p) { return *(LAS s16x8*)p; }
__device__ __forceinline__ s16x8 lds_tr(LAS char* p0, LAS char* p1) {
    s16x4 a = __builtin_amdgcn_ds_read_tr16_b64_v4i16((LAS s16x4*)p0);
    s16x4 b = __builtin_amdgcn_ds_read_tr16_b64_v4i16((LAS s16x4*)p1);
    return __builtin_shufflevector(a, b, 0, 1, 2, 3, 4, 5, 6, 7);
}
template <int OFF> __device__ __forceinline__ void dma1(unsigned lds, unsigned voff, const GAS char* base) {
    asm volatile("s_mov_b32 m0, %0\n\ts_nop 0\n\tglobal_load_lds_dwordx4 %1, %2 offset:%3" ::"s"(lds), "v"(voff), "s"(base), "i"(OFF) : "memory");
}
template <int OFF> __device__ __forceinline__ void dma2(unsigned lds, unsigned voff0, unsigned voff1, const GAS char* base) {
    asm volatile("s_mov_b32 m0, %0\n\ts_nop 0\n\tglobal_load_lds_dwordx4 %1, %3\n\tglobal_load_lds_dwordx4 %2, %3 offset:%4"
                 ::"s"(lds), "v"(voff0), "v"(voff1), "s"(base), "i"(OFF) : "memory");
}
template <int OFF> __device__ __forceinline__ void dma2_sc1(unsigned lds, unsigned voff0, unsigned voff1, const GAS char* base) {
    asm volatile("s_mov_b32 m0, %0\n\ts_nop 0\n\tglobal_load_lds_dwordx4 %1, %3 sc1\n\tglobal_load_lds_dwordx4 %2, %3 offset:%4 sc1"
                 ::"s"(lds), "v"(voff0), "v"(voff1), "s"(base), "i"(OFF) : "memory");
}
__device__ __forceinline__ void touch(unsigned lds, const GAS char* a) {
    asm volatile("s_mov_b32 m0, %0\n\ts_nop 0\n\tglobal_load_lds_dword %1, off" ::"s"(lds), "v"(a) : "memory");
}
__device__ __forceinline__ void lds_st32(unsigned a, float v) { asm volatile("ds_write_b32 %0, %1" ::"v"(a), "v"(v) : "memory"); }
__device__ __forceinline__ void lds_st64(unsigned a, f32x2 v) { asm volatile("ds_write_b64 %0, %1" ::"v"(a), "v"(v) : "memory"); }
template <int CTRL, int RM> __device__ __forceinline__ float dpp_add(float v) {
    return v + __uint_as_float(__builtin_amdgcn_update_dpp(0u, __float_as_uint(v), CTRL, RM, 0xf, true));
}
__device__ __forceinline__ float wave_sum(float v) {
    v = dpp_add<0x111, 0xf>(v); v = dpp_add<0x112, 0xf>(v); v = dpp_add<0x114, 0xf>(v); v = dpp_add<0x118, 0xf>(v);
    v = dpp_add<0x142, 0xa>(v); v = dpp_add<0x143, 0xc>(v);
    return __uint_as_float(__builtin_amdgcn_readlane(__float_as_uint(v), 63));
}
__device__ __forceinline__ float sum_groups(float v) {
    const auto a = __builtin_amdgcn_permlane16_swap(__float_as_uint(v), __float_as_uint(v), false, false);
    v = __uint_as_float(a[0]) + __uint_as_float(a[1]);
    const auto b = __builtin_amdgcn_permlane32_swap(__float_as_uint(v), __float_as_uint(v), false, false);
    return __uint_as_float(b[0]) + __uint_as_float(b[1]);
}
__device__ __forceinline__ void store16_sc1(GAS void* p, u32x4 v) {
    asm volatile("global_store_dwordx4 %0, %1, off sc1\n\ts_nop 1" ::"v"(p), "v"(v) : "memory");
}
__device__ __forceinline__ void wait_flags(const GAS unsigned* f, unsigned tag) {
#pragma unroll 1
    for (unsigned spins = 0; spins < SPIN_CAP; ++spins) {
        const unsigned v = __hip_atomic_load(f + (threadIdx.x & 31), __ATOMIC_RELAXED, __HIP_MEMORY_SCOPE_AGENT);
        if (__all(v == tag)) break;
        __builtin_amdgcn_s_sleep(1);
    }
    asm volatile("" ::: "memory");
}
struct cf { float r, i; };
__device__ __forceinline__ cf cmul(cf a, cf b) { return cf{a.r * b.r - a.i * b.i, a.r * b.i + a.i * b.r}; }
__device__ __forceinline__ cf cfma(cf a, cf b, cf c) { return cf{fmaf(a.r, b.r, fmaf(-a.i, b.i, c.r)), fmaf(a.r, b.i, fmaf(a.i, b.r, c.i))}; }
__device__ __forceinline__ float exp_f(float x) {
    const float hi = x * 1.44269504f, lo = fmaf(x, 1.44269504f, -hi) + x * 1.92596299e-8f;
    return __builtin_amdgcn_exp2f(hi) * fmaf(lo, 0.693147181f, 1.0f);
}
__device__ __forceinline__ void sincos_f(float x, float& s, float& c) {
    const float k = rintf(x * 0.636619772f);
    float r = fmaf(k, -1.57079601e+00f, x);
    r = fmaf(k, -3.13916473e-07f, r);
    r = fmaf(k, -5.39030253e-15f, r);
    const float z = r * r;
    const float sp = fmaf(r * z, fmaf(z, fmaf(z, fmaf(z, 2.75573137e-06f, -1.98412698e-04f), 8.33333333e-03f), -1.66666667e-01f), r);
    const float cp = fmaf(z, fmaf(z, fmaf(z, fmaf(z, fmaf(z, -2.75573144e-07f, 2.48015873e-05f), -1.38888889e-03f), 4.16666667e-02f), -0.5f), 1.0f);
    const int q = (int)k;
    const float a = (q & 1) ? cp : sp, b = (q & 1) ? sp : cp;
    s = (q & 2) ? -a : a;
    c = ((q + 1) & 2) ? -b : b;
}
__device__ __forceinline__ unsigned row_std(float v) {
    const float s1 = rbf(__builtin_amdgcn_sqrtf(rbf(rbf(v) + LN_EPS)));
    const float r1 = __builtin_amdgcn_rcpf(s1);
    const float s2 = __builtin_amdgcn_sqrtf(rbf(rbf(v * r1 * r1) + LN_EPS));
    return pk_bf16(s1, s2);
}

typedef const unsigned short* __restrict__ bfp;

__global__ __launch_bounds__(512) void k_fused(bfp x, bfp lam_re, bfp lam_im, bfp bre, bfp bim, bfp cre, bfp cim, bfp log_dt, bfp dskip,
                                               bfp w1, bfp b1, bfp w2, bfp b2, char* ws_, float* __restrict__ out) {
    extern __shared__ __attribute__((aligned(16))) char smem_[];
    LAS char* smem = (LAS char*)smem_;
    GAS char* ws = (GAS char*)ws_;
    const int wave = __builtin_amdgcn_readfirstlane(threadIdx.x >> 6), lane = threadIdx.x & 63;
    const int g = lane >> 4, li = lane & 15;
    const int xg8 = blockIdx.x & 7, loc = blockIdx.x >> 3, ci = 4 * (xg8 >> 1) + (loc >> 3), cj = 8 * (xg8 & 1) + (loc & 7);
    const int t0 = 128 * ci, p0 = 32 * cj, f0 = cj < 11 ? 96 * cj : 1056 + 64 * (cj - 11), nfu = cj < 11 ? 6 : 4;
    const int T = 2 * ci + (cj >> 3), d0 = 64 * (cj & 7);
    const int rot = ((ci + 15) >> 1) & 7;
    unsigned long long entry_pc;
    asm volatile("s_getpc_b64 %0" : "=s"(entry_pc));
    unsigned epoch;
    asm volatile("global_load_dword %0, %1, off sc1" : "=v"(epoch) : "v"(ws + WS_EP) : "memory");
    const int q = li >> 2, pp = lane & 3;

    if (__builtin_expect(wave >= 4, 1)) {
        const int lw = wave - 4;
        const unsigned lds0 = (unsigned)(size_t)smem;
        unsigned xo[4], bo[2], wo[3];
#pragma unroll
        for (int i = 0; i < 4; ++i) {
            const int R = 8 * (4 * lw + i) + (lane >> 3), ii = R & 15, bb = (R >> 4) & 3;
            const int tok = t0 + (R & 64) + 16 * (ii >> 2) + 8 * (ii & 1) + 2 * bb + ((ii >> 1) & 1);
            xo[i] = tok * 1024 + 16 * ((lane & 7) ^ (lane >> 3)) + 3072 - 1024 * i;
        }
#pragma unroll
        for (int i = 0; i < 2; ++i) {
            const int R = 8 * (2 * lw + i) + (lane >> 3);
            bo[i] = (p0 + (R & 31)) * 1024 + 16 * ((lane & 7) ^ (lane >> 3)) + 3072 - 1024 * i;
        }
#pragma unroll
        for (int i = 0; i < 3; ++i) {
            const int st = 2 * (3 * lw + i) + (lane >> 5), kg = st / 3, ns = st - 3 * kg, kr = (lane & 31) >> 2;
            const int ch = 4 * ns + ((lane & 3) ^ ((2 * kg + (kr >> 2)) & 3));
            const int f = min(f0 + 8 * ch, FF - 8);
            wo[i] = (8 * kg + kr) * (FF * 2) + f * 2 + 3072 - 1024 * i;
        }
        const GAS char* xg = (const GAS char*)x - 3072;
        const GAS char* bg = (const GAS char*)(lw < 2 ? bre : bim) - 3072;
        const GAS char* wg = (const GAS char*)w1 - 3072;
        const unsigned lx = lds0 + P1_X + lw * 4096, lb = lds0 + P1_B + lw * 2048, lwb = lds0 + P1_W + lw * 3072;
        auto piece = [&](int t, auto I) {
            constexpr int i = decltype(I)::value;
            const unsigned so = (t & 3) * P1_SLOT;
            const int kt = (t + rot) & 7;
            if constexpr (i < 4) dma1<1024 * i>(lx + so, xo[i], xg + kt * 128);
            else if constexpr (i < 6) dma1<1024 * (i - 4)>(lb + so, bo[i - 4], bg + kt * 128);
            else dma1<1024 * (i - 6)>(lwb + so, wo[i - 6], wg + (size_t)kt * (64 * FF * 2));
        };
#define PC(t, i) piece(t, std::integral_constant<int, i>{})
        auto issue = [&](int t) { PC(t, 0); PC(t, 1); PC(t, 2); PC(t, 3); PC(t, 4); PC(t, 5); PC(t, 6); PC(t, 7); PC(t, 8); };
        issue(0); issue(1);
        touch(lds0 + L_DUMMY + 256 * lw, (const GAS char*)(entry_pc & ~255ull) + min(((loc & 3) * 4 + lw) * 13 + min(lane, 12), CODE_LINES - 1) * 128);

        unsigned w2o[2], co[2], ao[2];
#pragma unroll
        for (int i = 0; i < 2; ++i) {
            const int kg = 2 * lw + i;
            w2o[i] = (8 * kg + ((lane & 31) >> 2)) * (DM * 2) + (d0 + 32 * (lane >> 5) + 8 * (lane & 3)) * 2 + 3072 - 1056 * i;
            co[i] = (d0 + 8 * kg + (lane >> 3)) * (NP * 2) + 16 * ((lane & 7) ^ (lane >> 3)) + 3072 - 1024 * i;
            ao[i] = kg * 1024 + lane * 16 + 3072 - 1056 * i;
        }
        const GAS char* ag = ws + WS_ACT + (size_t)T * ACT_TILE;
        const GAS char* w2g = (const GAS char*)w2 - 3072;
        const unsigned l2 = lds0 + lw * 2080, l2c = lds0 + P2_IMG + lw * 2048;
        auto dmaA = [&](const GAS char* src, unsigned slot) { dma2_sc1<1056>(l2 + slot, ao[0], ao[1], src - 3072); };
        auto dmaW = [&](const GAS char* src, unsigned slot) { dma2<1056>(l2 + slot + P2_IMG, w2o[0], w2o[1], src); };
        auto dmaC = [&](const GAS char* src, unsigned slot) { dma2<1024>(l2c + slot, co[0], co[1], src); };
        auto preW = [&](int s) { dmaW(w2g + s * 65536, s * P2_SLOT); };
        auto preA = [&](int s) { dmaA(ag + s * 8192, s * P2_SLOT); };

        const int n1 = 75 * blockIdx.x + 19 * lw + min(lane, 18);
        const GAS char* l1a = n1 < 11008 ? (const GAS char*)w2 + n1 * 128
                                         : (n1 < 15104 ? (const GAS char*)cre + (n1 - 11008) * 128 : (const GAS char*)cim + (min(n1, 19199) - 15104) * 128);
        const GAS u32x4* rsp = (const GAS u32x4*)(ws + WS_RS) + t0 + 64 * (lw & 1) + lane;
        const GAS u32x4* csp = (const GAS u32x4*)(ws + WS_CS) + ((cj * 16 + 8 * (lw >> 1) + 2 * g) * 2 + (lw & 1)) * 32 + li;
        asm volatile("" : "+v"(l1a), "+v"(rsp), "+v"(csp));
        const unsigned tag = epoch_tag<10>(epoch);
        bar();
        issue(2);
        touch(lds0 + L_DUMMY + 256 * lw, l1a);
        wait_vm<11>(); bar();
        issue(3);
        {
            f32x2 v;
            asm volatile("ds_read_b64 %0, %1\n\ts_waitcnt lgkmcnt(0)" : "=&v"(v) : "v"(lds0 + L_MAIL + 16 * lw + 8 * (lane & 1)) : "memory");
            if (lane < 2) store16_sc1((GAS u32x4*)(ws + WS_RS) + t0 + 8 * cj + 2 * lw + lane, u32x4{__float_as_uint(v[0]), tag, __float_as_uint(v[1]), tag});
        }
        wait_vm<11>(); bar();
        issue(4);
        {
            float a, b, c;
            const unsigned ma = lds0 + L_MAIL + 64 + 4 * (((lw >> 1) * 5 + 3 * (lw & 1)) * 16 + li);
            asm volatile("ds_read_b32 %0, %3\n\tds_read_b32 %1, %3 offset:64\n\tds_read_b32 %2, %3 offset:128\n\ts_waitcnt lgkmcnt(0)"
                         : "=&v"(a), "=&v"(b), "=&v"(c) : "v"(ma) : "memory");
            if (g == 0) store16_sc1((GAS u32x4*)(ws + WS_CS) + ((cj * 16 + ci) * 2 + (lw >> 1)) * 32 + 16 * (lw & 1) + li,
                                    u32x4{__float_as_uint(a), __float_as_uint(b), __float_as_uint(c), tag});
        }
        wait_vm<11>(); bar();
        issue(5); wait_vm<10>(); bar();
        issue(6); wait_vm<9>(); bar();
        issue(7);
        u32x4 rs, cs[4];
        auto gather = [&]() {
            asm volatile("global_load_dwordx4 %0, %5, off sc1\n\t"
                         "global_load_dwordx4 %1, %6, off sc1\n\tglobal_load_dwordx4 %2, %6, off offset:256 sc1\n\t"
                         "global_load_dwordx4 %3, %6, off offset:1024 sc1\n\tglobal_load_dwordx4 %4, %6, off offset:1280 sc1"
                         : "=&v"(rs), "=&v"(cs[0]), "=&v"(cs[1]), "=&v"(cs[2]), "=&v"(cs[3]) : "v"(rsp), "v"(csp) : "memory");
        };
        gather();
        wait_vm<14>(); bar();
        preW(0); preW(1); preW(2); preW(3);
        asm volatile("s_waitcnt vmcnt(8)" : "+v"(rs), "+v"(cs[0]), "+v"(cs[1]), "+v"(cs[2]), "+v"(cs[3])::"memory");
        bar();
        preW(4); preW(5);
        auto fresh = [&]() {
            return __all(rs[1] == tag && rs[3] == tag && cs[0][3] == tag && cs[1][3] == tag && cs[2][3] == tag && cs[3][3] == tag);
        };
#pragma unroll 1
        for (unsigned spins = 0; spins < SPIN_CAP && !fresh(); ++spins) {
            gather();
            asm volatile("s_waitcnt vmcnt(0)" : "+v"(rs), "+v"(cs[0]), "+v"(cs[1]), "+v"(cs[2]), "+v"(cs[3])::"memory");
        }
        if (lw < 2) {
            const float m = __uint_as_float(rs[0]), mb = rbf(m);
            const float r1 = __builtin_amdgcn_rcpf(bf_lo(rs[2])), r12 = r1 * __builtin_amdgcn_rcpf(bf_hi(rs[2]));
            const int tau = 64 * lw + lane;
            const unsigned ta = lds0 + L_TAB1 + 4 * (((tau >> 4) * 8 + (tau & 7)) * 4 + ((tau >> 3) & 1));
            lds_st32(ta, r1); lds_st32(ta + 8, -r1 * mb);
            lds_st32(ta + 1024, r12); lds_st32(ta + 1032, -r12 * m);
            lds_st64(lds0 + L_TOK + 8 * tau, f32x2{mb, r1});
        }
        {
            float v5[5];
#pragma unroll
            for (int k = 0; k < 3; ++k) v5[k] = __uint_as_float(cs[0][k]) + __uint_as_float(cs[2][k]);
#pragma unroll
            for (int k = 0; k < 2; ++k) v5[3 + k] = __uint_as_float(cs[1][k]) + __uint_as_float(cs[3][k]);
#pragma unroll
            for (int k = 0; k < 5; ++k) v5[k] = sum_groups(v5[k]);
            if (g == 0) {
#pragma unroll
                for (int k = 0; k < 5; ++k) lds_st32(lds0 + L_CS + 4 * ((((lw >> 1) * 2 + (lw & 1)) * 5 + k) * 16 + li), v5[k]);
            }
        }
        asm volatile("s_waitcnt lgkmcnt(0)" ::: "memory");
        bar();
        preW(6); preW(7); preW(8);
        bar();
        bar();
        wait_flags((const GAS unsigned*)(ws + WS_FH) + T * 32, tag);
        preA(0); preA(1); preA(2); preA(3);
        wait_vm<4>(); bar();
        preA(4); preA(5); preA(6); preA(7);
        wait_vm<8>(); bar();
        const GAS char* ap = ag + 9 * 8192;
        const GAS char* wp = w2g + 9 * 65536;
        unsigned slot = 0;
        auto adv = [&]() { ap += 8192; slot = slot == P2_RING - P2_SLOT ? 0 : slot + P2_SLOT; };
        auto stW = [&]() { dmaA(ap, slot); dmaW(wp, slot); wp += 65536; adv(); };
        auto stC = [&]() { dmaA(ap, slot); dmaC(wp, slot); wp += 128; adv(); };
        preA(8); stW();
        wait_vm<10>(); bar(); stW(); stW();
        wait_vm<14>(); bar(); stW(); stW();
#pragma unroll 1
        for (int j = 4; j < 7; ++j) { wait_vm<16>(); bar(); stW(); stW(); }
        wait_vm<16>(); bar(); stW();
        ap -= 4096; wp -= 32768;
        stW();
        wp = (const GAS char*)cre - 3072;
        wait_vm<16>(); bar();
        stC(); stC();
#pragma unroll 1
        for (int j = 9; j < 12; ++j) { wait_vm<16>(); bar(); stC(); stC(); }
        wp = (const GAS char*)cim - 3072;
#pragma unroll 1
        for (int j = 12; j < 16; ++j) { wait_vm<16>(); bar(); stC(); stC(); }
        wait_vm<16>(); bar();
        wait_vm<8>(); bar();
        wait_vm<0>(); bar();
        return;
    }

    const int wm = wave >> 1, wn = wave & 1;
    const int p = p0 + 16 * wn + li;
    cf lam = {0.0f, 0.0f}, l8 = lam, l16 = lam, l128 = lam, lsig = lam, coef2 = lam;
    float b1c[3], p_lr, p_lim, p_ldt;
    unsigned tag;
    {
        u32x4 xr[2];
        unsigned short pr[6];
#pragma unroll
        for (int i = 0; i < 2; ++i) xr[i] = *(const u32x4*)(x + (t0 + 8 * cj + 2 * wave + i) * DM + lane * 8);
        pr[0] = lam_re[p]; pr[1] = lam_im[p]; pr[2] = log_dt[p];
#pragma unroll
        for (int c = 0; c < 3; ++c) pr[3 + c] = b1[min(f0 + 16 * (3 * wn + c) + li, FF - 1)];
        SB0;
        p_lr = bf2f(pr[0]); p_lim = bf2f(pr[1]); p_ldt = bf2f(pr[2]);
#pragma unroll
        for (int c = 0; c < 3; ++c) b1c[c] = bf2f(pr[3 + c]);
        float mean[2], var[2];
#pragma unroll
        for (int i = 0; i < 2; ++i) {
            float e[8], s = 0.0f;
#pragma unroll
            for (int j = 0; j < 4; ++j) { e[2 * j] = bf_lo(xr[i][j]); e[2 * j + 1] = bf_hi(xr[i][j]); s += e[2 * j] + e[2 * j + 1]; }
            mean[i] = wave_sum(s) * (1.0f / DM);
            float qq = 0.0f;
#pragma unroll
            for (int j = 0; j < 8; ++j) qq = fmaf(e[j] - mean[i], e[j] - mean[i], qq);
            var[i] = wave_sum(qq) * (1.0f / DM);
        }
        if (lane < 2) ((LAS f32x2*)(smem + L_MAIL))[2 * wave + lane] = f32x2{lane ? mean[1] : mean[0], __uint_as_float(row_std(lane ? var[1] : var[0]))};
        asm volatile("" : "+v"(b1c[0]), "+v"(b1c[1]), "+v"(b1c[2]), "+v"(p_lr), "+v"(p_lim), "+v"(p_ldt));
    }
    auto lam_setup = [&]() {
        const float lr = p_lr, lim = p_lim, dt = rbf(exp_f(p_ldt));
        const float mag = exp_f(dt * lr);
        float sn, cs;
        sincos_f(dt * lim, sn, cs);
        lam = cf{mag * cs, mag * sn};
        const float nr = lam.r - 1.0f, ni = lam.i, den = 1.0f / (lr * lr + lim * lim);
        coef2 = cf{2.0f * (nr * lr + ni * lim) * den, 2.0f * (ni * lr - nr * lim) * den};
        const cf l2 = cmul(lam, lam), l4 = cmul(l2, l2);
        l8 = cmul(l4, l4); l16 = cmul(l8, l8);
        const cf l32 = cmul(l16, l16), l64 = cmul(l32, l32);
        l128 = cmul(l64, l64);
        lsig = (g & 1) ? l16 : cf{1.0f, 0.0f};
        if (g & 2) lsig = cmul(lsig, l32);
        if (wm) lsig = cmul(lsig, l64);
        asm volatile("" : "+v"(lam.r), "+v"(lam.i), "+v"(l8.r), "+v"(l8.i), "+v"(l16.r), "+v"(l16.i), "+v"(l128.r), "+v"(l128.i), "+v"(lsig.r), "+v"(lsig.i),
                          "+v"(coef2.r), "+v"(coef2.i));
    };
    LAS f32x4* tab1 = (LAS f32x4*)(smem + L_TAB1) + (wm * 4 + g) * 8;
    LAS f32x4* tab2 = (LAS f32x4*)(smem + L_TAB2) + (wm * 4 + g) * 8;
    auto csum = [&](int k) { return ((LAS float*)(smem + L_CS))[(wn * 5 + k) * 16 + li] + ((LAS float*)(smem + L_CS))[((2 + wn) * 5 + k) * 16 + li]; };
    f32x4 acc[4][5], tb[8];
    float s1c[3];
    {
        const int sw = 16 * (g ^ (lane & 7));
        const int xa0 = P1_X + (64 * wm + li) * 128 + sw;
        const int ba0 = P1_B + (16 * wn + li) * 128 + sw;
        int wa[2][2];
#pragma unroll
        for (int t = 0; t < 2; ++t)
#pragma unroll
            for (int cl = 0; cl < 2; ++cl) {
                const int par = (3 * wn + cl) & 1;
                wa[t][cl] = P1_W + 1536 * g + 64 * q + 256 * t + 32 * (par ^ (g & 1)) + 16 * ((pp >> 1) ^ t) + 8 * (pp & 1) + (cl == 0 ? 512 * wn : 1024 * wn);
            }
#pragma unroll
        for (int b = 0; b < 4; ++b)
#pragma unroll
            for (int c = 0; c < 5; ++c) acc[b][c] = f32x4{0, 0, 0, 0};
        s16x8 xa[2][4], wb[2][5];
        auto rd = [&](int slot, int ks, int buf) {
            LAS char* s = smem + slot * P1_SLOT;
            LAS char* xs = s + (xa0 ^ (64 * ks));
            LAS char* bs = s + (ba0 ^ (64 * ks));
#pragma unroll
            for (int b = 0; b < 4; ++b) xa[buf][b] = lds_row(xs + b * 2048);
            wb[buf][0] = lds_row(bs);
            wb[buf][1] = lds_row(bs + 4096);
            wb[buf][2] = lds_tr(s + wa[0][0] + 6144 * ks, s + wa[1][0] + 6144 * ks);
            wb[buf][3] = lds_tr(s + wa[0][1] + 6144 * ks, s + wa[1][1] + 6144 * ks);
            wb[buf][4] = lds_tr(s + wa[0][0] + 6144 * ks + 512, s + wa[1][0] + 6144 * ks + 512);
        };
        auto mm = [&](int buf) {
#pragma unroll
            for (int c = 0; c < 5; ++c)
#pragma unroll
                for (int b = 0; b < 4; ++b) acc[b][c] = mfma(xa[buf][b], wb[buf][c], acc[b][c]);
        };
        lam_setup();
        auto colsum = [&](int buf) {
            const s16x8 ones = {0x3f80, 0x3f80, 0x3f80, 0x3f80, 0x3f80, 0x3f80, 0x3f80, 0x3f80};
            float v5[5];
#pragma unroll
            for (int c = 0; c < 5; ++c) v5[c] = mfma(ones, wb[buf][c], f32x4{0, 0, 0, 0})[0];
            if (g == 0) {
#pragma unroll
                for (int c = 0; c < 5; ++c) ((LAS float*)(smem + L_MAIL + 64))[(wn * 5 + c) * 16 + li] = v5[c];
            }
        };
        tag = epoch_tag<0>(epoch);
        wait_lgkm0();
        bar();
        rd(0, 0, 0);
        wait_lgkm0();
#pragma unroll 1
        for (int seg = 0, t = 0; seg < 2; ++seg) {
            const int tend = seg ? 7 : 1;
#pragma unroll 1
            for (; t < tend; ++t) {
                SB0;
                rd(t & 3, 1, 1); mm(0); interleave<20, 12>();
                SB0; wait_lgkm0(); bar();
                rd((t + 1) & 3, 0, 0); mm(1); interleave<20, 12>();
                SB0; wait_lgkm0();
            }
            if (seg == 0 && wm == 0) { if (ci & 1) colsum(1); else colsum(0); }
        }
        rd(3, 1, 1); mm(0); interleave<20, 12>();
        SB0; wait_lgkm0(); bar();
#pragma unroll
        for (int j = 0; j < 8; ++j) tb[j] = tab2[j];
#pragma unroll
        for (int c = 0; c < 3; ++c) s1c[c] = csum(2 + c);
        mm(1);
        interleave<20, 14>();
        SB0;
    }

    u32x2 xv[2][2], dv[2], bv[2];
#pragma unroll
    for (int i = 0; i < 2; ++i) {
        const int d = d0 + 32 * (wave >> 1) + 16 * i + 4 * g;
        dv[i] = *(const u32x2*)(dskip + d); bv[i] = *(const u32x2*)(b2 + d);
#pragma unroll
        for (int j = 0; j < 2; ++j) xv[i][j] = *(const u32x2*)(x + (64 * T + 32 * j + 16 * ((lane >> 3) & 1) + 8 * (wave & 1) + (lane & 7)) * DM + d);
    }
    const int Tw = 2 * ci + wm;
    const __amdgpu_buffer_rsrc_t act = __builtin_amdgcn_make_buffer_rsrc((void*)(ws_ + WS_ACT), 0, 32 * ACT_TILE, 0x00020000);
    const unsigned act0 = Tw * ACT_TILE;
    {
        constexpr float C1 = -2.30220819f;
        constexpr float C2 = C1 * 0.044715f;
#pragma unroll
        for (int c = 0; c < 3; ++c) {
            const int col = 16 * (3 * wn + c) + li;
            f32x2 y[8];
#pragma unroll
            for (int j = 0; j < 8; ++j) {
                const int b = j >> 1, r = 2 * (j & 1);
                const f32x2 a = {tb[j][0], tb[j][1]}, bb = {tb[j][2], tb[j][3]};
                const f32x2 z = pfma(a, f32x2{acc[b][2 + c][r], acc[b][2 + c][r + 1]}, pfma(bb, splat(s1c[c]), splat(b1c[c])));
                const f32x2 u = z * pfma(z * z, splat(C2), splat(C1));
                const f32x2 d = f32x2{__builtin_amdgcn_exp2f(u[0]), __builtin_amdgcn_exp2f(u[1])} + splat(1.0f);
                y[j] = z * f32x2{__builtin_amdgcn_rcpf(d[0]), __builtin_amdgcn_rcpf(d[1])};
            }
            if (3 * wn + c < nfu) {
                const int k = f0 + col;
                const unsigned dst = act0 + (k >> 3) * 1024 + (k & 7) * 64 + 16 * g;
#pragma unroll
                for (int sgm = 0; sgm < 2; ++sgm)
                    __builtin_amdgcn_raw_buffer_store_b128(u32x4{pk_bf16(y[0][sgm], y[1][sgm]), pk_bf16(y[2][sgm], y[3][sgm]), pk_bf16(y[4][sgm], y[5][sgm]), pk_bf16(y[6][sgm], y[7][sgm])},
                                                           act, dst + 512 * sgm, 0, AUX_SC1);
            }
        }
    }

    f32x2 ure[8], uim[8];
    {
        const float sbr = csum(0), sbi = csum(1);
#pragma unroll
        for (int j = 0; j < 8; ++j) {
            const f32x4 tb = tab1[j];
            const f32x2 a = {tb[0], tb[1]}, bb = {tb[2], tb[3]};
            const int b = j >> 1, r = 2 * (j & 1);
            ure[j] = pfma(a, f32x2{acc[b][0][r], acc[b][0][r + 1]}, bb * splat(sbr));
            uim[j] = pfma(a, f32x2{acc[b][1][r], acc[b][1][r + 1]}, bb * splat(sbi));
        }
    }
    f32x2 hr = ure[0], hi = uim[0];
#pragma unroll
    for (int j = 1; j < 8; ++j) {
        const f32x2 tr = pfma(splat(lam.r), hr, ure[j]), ti = pfma(splat(lam.r), hi, uim[j]);
        const f32x2 nr = pfma(splat(-lam.i), hi, tr), ni = pfma(splat(lam.i), hr, ti);
        hr = nr; hi = ni;
    }
    const cf eA = cf{hr[0], hi[0]};
    const cf e = cfma(l8, eA, cf{hr[1], hi[1]});
    const int sig = 4 * wm + g;
    LAS f32x2* eL = (LAS f32x2*)(smem + L_EL);
    eL[sig * 32 + 16 * wn + li] = f32x2{e.r, e.i};
    wait_lgkm0();
    bar();
    cf Lp = cf{0.0f, 0.0f};
#pragma unroll
    for (int s = 0; s < 7; ++s) {
        const f32x2 v = eL[s * 32 + 16 * wn + li];
        const cf n = cfma(l16, Lp, cf{v[0], v[1]});
        if (s < sig) Lp = n;
    }
    wait_vm<0>();
    if (lane == 0) __hip_atomic_store((GAS unsigned*)(ws + WS_FH) + Tw * 32 + 2 * cj + wn, tag, __ATOMIC_RELAXED, __HIP_MEMORY_SCOPE_AGENT);
    GAS u32x4* eg = (GAS u32x4*)(ws + WS_EG);
    if (sig == 7) {
        const cf E = cfma(l16, Lp, e);
        store16_sc1(eg + ci * NP + p, u32x4{__float_as_uint(E.r), tag, __float_as_uint(E.i), tag});
    }
    bar();

    const int wd = wave >> 1, wt = wave & 1;
    f32x4 o[2][2];
    cf Hin = cf{0.0f, 0.0f};
    {
#pragma unroll
        for (int j = 0; j < 2; ++j) {
            const f32x2 st = ((LAS f32x2*)(smem + L_TOK))[64 * (T & 1) + 32 * j + 16 * ((lane >> 3) & 1) + 8 * wt + (lane & 7)];
#pragma unroll
            for (int i = 0; i < 2; ++i) {
                const float xe[4] = {bf_lo(xv[i][j][0]), bf_hi(xv[i][j][0]), bf_lo(xv[i][j][1]), bf_hi(xv[i][j][1])};
                const float de[4] = {bf_lo(dv[i][0]), bf_hi(dv[i][0]), bf_lo(dv[i][1]), bf_hi(dv[i][1])};
                const float be[4] = {bf_lo(bv[i][0]), bf_hi(bv[i][0]), bf_lo(bv[i][1]), bf_hi(bv[i][1])};
#pragma unroll
                for (int k = 0; k < 4; ++k) {
                    const float xn = rbf(rbf(xe[k] - st[0]) * st[1]);
                    o[i][j][k] = xe[k] + rbf(xn * de[k]) + be[k];
                }
            }
        }
        asm volatile("" : "+v"(o[0][0]), "+v"(o[0][1]), "+v"(o[1][0]), "+v"(o[1][1]));
        const GAS u32x4* ep = eg + g * NP + p;
        u32x4 gr[4];
        auto fetch = [&]() {
            asm volatile("global_load_dwordx4 %0, %4, off sc1\n\tglobal_load_dwordx4 %1, %5, off sc1\n\t"
                         "global_load_dwordx4 %2, %6, off sc1\n\tglobal_load_dwordx4 %3, %7, off sc1"
                         : "=&v"(gr[0]), "=&v"(gr[1]), "=&v"(gr[2]), "=&v"(gr[3])
                         : "v"(ep), "v"(ep + 4 * NP), "v"(ep + 8 * NP), "v"(ep + 12 * NP) : "memory");
        };
        auto landed = [&]() { asm volatile("s_waitcnt vmcnt(0)" : "+v"(gr[0]), "+v"(gr[1]), "+v"(gr[2]), "+v"(gr[3])::"memory"); };
        auto complete = [&]() {
            bool ok = true;
#pragma unroll
            for (int i = 0; i < 4; ++i) ok &= (4 * i + g >= ci) | ((gr[i][1] == tag) & (gr[i][3] == tag));
            return __all(ok);
        };
        fetch();
        const cf l256 = cmul(l128, l128), l512 = cmul(l256, l256);
        landed();
#pragma unroll 1
        for (unsigned spins = 0; spins < SPIN_CAP && !complete(); ++spins) { __builtin_amdgcn_s_sleep(2); fetch(); landed(); }
        cf hp = cf{0.0f, 0.0f};
#pragma unroll
        for (int i = 0; i < 4; ++i) {
            const cf n = cfma(l512, hp, cf{__uint_as_float(gr[i][0]), __uint_as_float(gr[i][2])});
            if (4 * i + g < ci) hp = n;
        }
        const int rr = (ci - 1 - g) & 3;
        if (rr & 1) hp = cmul(hp, l128);
        if (rr & 2) hp = cmul(hp, l256);
        Hin = cf{sum_groups(hp.r), sum_groups(hp.i)};
    }
    u32x4 hq[2][2];
    unsigned hdr, hdi;
    auto store_h = [&]() {
#pragma unroll
        for (int sgm = 0; sgm < 2; ++sgm) {
            __builtin_amdgcn_raw_buffer_store_b128(hq[0][sgm], act, hdr + 512 * sgm, 0, AUX_SC1);
            __builtin_amdgcn_raw_buffer_store_b128(hq[1][sgm], act, hdi + 512 * sgm, 0, AUX_SC1);
        }
    };
    {
        const cf cA = cfma(lsig, Hin, Lp);
        const cf cB = cfma(l8, cA, eA);
        f32x2 sr = {cA.r, cB.r}, si = {cA.i, cB.i};
        f32x2 ore[8], oim[8];
#pragma unroll
        for (int j = 0; j < 8; ++j) {
            const f32x2 tr = pfma(splat(lam.r), sr, ure[j]), ti = pfma(splat(lam.r), si, uim[j]);
            const f32x2 nr = pfma(splat(-lam.i), si, tr), ni = pfma(splat(lam.i), sr, ti);
            sr = nr; si = ni;
            ore[j] = pfma(splat(coef2.r), sr, splat(-coef2.i) * si);
            oim[j] = pfma(splat(-coef2.r), si, splat(-coef2.i) * sr);
        }
        const int kr = FF + p, kim = FF + NP + p;
        hdr = act0 + (kr >> 3) * 1024 + (kr & 7) * 64 + 16 * g;
        hdi = act0 + (kim >> 3) * 1024 + (kim & 7) * 64 + 16 * g;
#pragma unroll
        for (int sgm = 0; sgm < 2; ++sgm) {
            hq[0][sgm] = u32x4{pk_bf16(ore[0][sgm], ore[1][sgm]), pk_bf16(ore[2][sgm], ore[3][sgm]), pk_bf16(ore[4][sgm], ore[5][sgm]), pk_bf16(ore[6][sgm], ore[7][sgm])};
            hq[1][sgm] = u32x4{pk_bf16(oim[0][sgm], oim[1][sgm]), pk_bf16(oim[2][sgm], oim[3][sgm]), pk_bf16(oim[4][sgm], oim[5][sgm]), pk_bf16(oim[6][sgm], oim[7][sgm])};
        }
        store_h();
    }

    {
        const int trb = (g >> 1) * 2080 + (g & 1) * 1056 + 64 * q + 16 * (pp >> 1) + 8 * (pp & 1);
        const int tra = trb + 512 * wt, trw = P2_IMG + trb + 512 * wd;
        const int ca0 = P2_IMG + (32 * wd + li) * 128 + 16 * (g ^ (lane & 7));
        s16x8 wf[2][2][2], af[2][2][2];
        int slot = 0;
        auto next = [&]() { slot = slot == P2_RING - P2_SLOT ? 0 : slot + P2_SLOT; };
        auto frag_a = [&](LAS char* sl, int buf, int ks, int i) { LAS char* a = sl + tra + 4160 * ks + 32 * i; af[buf][ks][i] = lds_tr(a, a + 256); };
        auto frag_w = [&](LAS char* sl, int buf, int ks, int i, bool g4) {
            if (g4) { LAS char* a = sl + trw + 4160 * ks + 32 * i; wf[buf][ks][i] = lds_tr(a, a + 256); }
            else wf[buf][ks][i] = lds_row(sl + ((ca0 ^ (64 * ks)) + 2048 * i));
        };
        auto half = [&](int rb, bool rd, bool g4, int k0, bool mm_) {
            LAS char* sl = smem + slot;
            const int ub = rb ^ 1;
#pragma unroll
            for (int ks = 0; ks < 2; ++ks) {
#pragma unroll
                for (int m = 0; m < 4; ++m) {
                    if (mm_ && ks >= k0) mfma_acc(o[m >> 1][m & 1], wf[ub][ks][m >> 1], af[ub][ks][m & 1]);
                    SB0;
                    if (rd) { if (m < 2) frag_a(sl, rb, ks, m); else frag_w(sl, rb, ks, m - 2, g4); }
                    SB0;
                }
            }
            if (rd) next();
        };
        const GAS unsigned* fsp = (const GAS unsigned*)(ws + WS_FS) + T * 32 + (lane & 31);
        const unsigned fsl = (unsigned)(size_t)smem + L_EL + 256 * wave;
        bar(); half(0, true, true, 0, false);
        half(1, true, true, 0, true);
#pragma unroll 1
        for (int seg = 0, j = 1; seg < 4; ++seg) {
            const int jend = seg == 0 ? 2 : (seg == 1 ? 7 : (seg == 2 ? 8 : 10));
#pragma unroll 1
            for (; j < jend; ++j) {
                bar(); half(0, true, true, 0, true);
                half(1, true, true, 0, true);
            }
            if (seg == 0) {
                wait_vm<0>();
                if (lane == 0) __hip_atomic_store((GAS unsigned*)(ws + WS_FS) + Tw * 32 + 2 * cj + wn, tag, __ATOMIC_RELAXED, __HIP_MEMORY_SCOPE_AGENT);
            } else if (seg == 1) {
                asm volatile("s_mov_b32 m0, %0\n\ts_nop 0\n\tglobal_load_lds_dword %1, off sc1" ::"s"(fsl), "v"(fsp) : "memory");
            } else if (seg == 2) {
                unsigned fsv;
                asm volatile("s_waitcnt vmcnt(0)\n\tds_read_b32 %0, %1\n\ts_waitcnt lgkmcnt(0)" : "=&v"(fsv) : "v"(fsl + 4 * lane) : "memory");
                if (!__all(fsv == tag)) wait_flags((const GAS unsigned*)(ws + WS_FS) + T * 32, tag);
            }
            SB0;
        }
        bar(); half(0, true, true, 0, true);
        half(1, true, true, 0, true);
        bar(); half(0, true, false, 1, true);
        half(1, true, false, 0, true);
#pragma unroll 1
        for (int j = 12; j < 19; ++j) {
            bar(); half(0, true, false, 0, true);
            half(1, true, false, 0, true);
        }
        half(0, false, false, 0, true);
        asm volatile("s_nop 7\n\ts_nop 7" : "+v"(o[0][0]), "+v"(o[0][1]), "+v"(o[1][0]), "+v"(o[1][1]));

#pragma unroll
        for (int j = 0; j < 2; ++j)
#pragma unroll
            for (int i = 0; i < 2; ++i)
                *(f32x4*)(out + (64 * T + 32 * j + 16 * ((lane >> 3) & 1) + 8 * wt + (lane & 7)) * DM + d0 + 32 * wd + 16 * i + 4 * g) = o[i][j];
        if (ci == 15 && cj == 0 && threadIdx.x == 0) __hip_atomic_store((GAS unsigned*)(ws + WS_EP), tag, __ATOMIC_RELAXED, __HIP_MEMORY_SCOPE_AGENT);
    }
}

}

extern "C" void kernel_launch(void* const* d_in, const int* in_sizes, int n_in, void* d_out, int out_size, void* d_ws, size_t ws_size,
                              hipStream_t stream) {
    typedef const unsigned short* P;
    (void)hipFuncSetAttribute((const void*)k_fused, hipFuncAttributeMaxDynamicSharedMemorySize, LDS_BYTES);
    k_fused<<<256, 512, LDS_BYTES, stream>>>((P)d_in[0], (P)d_in[1], (P)d_in[2], (P)d_in[3], (P)d_in[4], (P)d_in[5], (P)d_in[6], (P)d_in[7],
                                             (P)d_in[8], (P)d_in[10], (P)d_in[11], (P)d_in[12], (P)d_in[13], (char*)d_ws, (float*)d_out);
}
```

```cpp
#include <hip/hip_runtime.h>
#include <hip/hip_bf16.h>
#include <stdint.h>
#include <type_traits>

#define LAS __attribute__((address_space(3)))
#define GAS __attribute__((address_space(1)))

typedef __attribute__((ext_vector_type(8))) short s16x8;
typedef __attribute__((ext_vector_type(4))) short s16x4;
typedef __attribute__((ext_vector_type(8))) __bf16 bf16x8;
typedef __attribute__((ext_vector_type(2))) __bf16 bf16x2;
typedef __attribute__((ext_vector_type(4))) float f32x4;
typedef __attribute__((ext_vector_type(2))) float f32x2;
typedef __attribute__((ext_vector_type(4))) unsigned u32x4;
typedef __attribute__((ext_vector_type(2))) unsigned u32x2;

namespace {

constexpr int SEQ = 2048, DM = 512, NP = 512, FF = 1376;
constexpr int KACT = FF + 2 * NP;
constexpr int ACT_TILE = KACT * 128;
constexpr unsigned WS_ACT = 0;
constexpr unsigned WS_RS = 10u << 20;
constexpr unsigned WS_EG = WS_RS + (64u << 10);
constexpr unsigned WS_FH = WS_EG + (256u << 10);
constexpr unsigned WS_FS = WS_FH + (8u << 10);
constexpr unsigned WS_CS = WS_FS + (8u << 10);
constexpr unsigned WS_EP = WS_CS + (256u << 10);
constexpr float LN_EPS = 1e-5f;
constexpr unsigned SPIN_CAP = 1u << 18;
constexpr int AUX_SC1 = 16;
constexpr int CODE_LINES = 192;

constexpr int P1_SLOT = 36 * 1024, P1_X = 0, P1_B = 16384, P1_W = 24576;
constexpr int P2_IMG = 4 * 2080, P2_SLOT = 2 * P2_IMG, P2_RING = 9 * P2_SLOT;
constexpr int L_TAB1 = P2_RING;
constexpr int L_TAB2 = L_TAB1 + 1024;
constexpr int L_EL = L_TAB2 + 1024;
constexpr int L_CS = L_EL + 2048;
constexpr int L_TOK = L_CS + 1280;
constexpr int L_DUMMY = L_TOK + 1024;
constexpr int L_MAIL = L_DUMMY + 1024;
constexpr int LDS_BYTES = L_MAIL + 64 + 704;

#define SB0 __builtin_amdgcn_sched_barrier(0)
template <int N> __device__ __forceinline__ void wait_vm() { asm volatile("s_waitcnt vmcnt(%0)" ::"i"(N) : "memory"); }
template <int N> __device__ __forceinline__ unsigned epoch_tag(unsigned epoch) {
    unsigned e;
    asm volatile("s_waitcnt vmcnt(%2)\n\tv_readfirstlane_b32 %0, %1\n\ts_nop 1" : "=s"(e) : "v"(epoch), "i"(N) : "memory");
    return e + 1u;
}
__device__ __forceinline__ void wait_lgkm0() { __builtin_amdgcn_s_waitcnt(0xC07F); asm volatile("" ::: "memory"); }
template <int NM, int ND, int DPM = 1, int I = 0> __device__ __forceinline__ void interleave() {
    if constexpr (I * DPM < ND && I < NM) {
        __builtin_amdgcn_sched_group_barrier(0x008, 1, 0);
        __builtin_amdgcn_sched_group_barrier(0x100, (ND - I * DPM) < DPM ? (ND - I * DPM) : DPM, 0);
        interleave<NM, ND, DPM, I + 1>();
    } else if constexpr (I < NM) {
        __builtin_amdgcn_sched_group_barrier(0x008, NM - I, 0);
    }
}
__device__ __forceinline__ void bar() {
    SB0;
    asm volatile("" ::: "memory");
    __builtin_amdgcn_s_barrier();
    asm volatile("" ::: "memory");
    SB0;
}
__device__ __forceinline__ float bf_lo(unsigned u) { return __uint_as_float(u << 16); }
__device__ __forceinline__ float bf_hi(unsigned u) { return __uint_as_float(u & 0xffff0000u); }
__device__ __forceinline__ float bf2f(unsigned short h) { return __uint_as_float((unsigned)h << 16); }
__device__ __forceinline__ unsigned pk_bf16(float a, float b) { bf16x2 v = {(__bf16)a, (__bf16)b}; return __builtin_bit_cast(unsigned, v); }
__device__ __forceinline__ float rbf(float a) { return bf_lo(pk_bf16(a, a)); }
__device__ __forceinline__ f32x2 pfma(f32x2 a, f32x2 b, f32x2 c) { return __builtin_elementwise_fma(a, b, c); }
__device__ __forceinline__ f32x2 splat(float a) { return f32x2{a, a}; }
__device__ __forceinline__ f32x4 mfma(s16x8 a, s16x8 b, f32x4 c) {
    return __builtin_amdgcn_mfma_f32_16x16x32_bf16(__builtin_bit_cast(bf16x8, a), __builtin_bit_cast(bf16x8, b), c, 0, 0, 0);
}
__device__ __forceinline__ void mfma_acc(f32x4& c, s16x8 a, s16x8 b) {
    asm volatile("v_mfma_f32_16x16x32_bf16 %0, %1, %2, %0" : "+v"(c) : "v"(a), "v"(b));
}
__device__ __forceinline__ s16x8 lds_row(LAS char* p) { return *(LAS s16x8*)p; }
__device__ __forceinline__ s16x8 lds_tr(LAS char* p0, LAS char* p1) {
    s16x4 a = __builtin_amdgcn_ds_read_tr16_b64_v4i16((LAS s16x4*)p0);
    s16x4 b = __builtin_amdgcn_ds_read_tr16_b64_v4i16((LAS s16x4*)p1);
    return __builtin_shufflevector(a, b, 0, 1, 2, 3, 4, 5, 6, 7);
}
template <int OFF> __device__ __forceinline__ void dma1(unsigned lds, unsigned voff, const GAS char* base) {
    asm volatile("s_mov_b32 m0, %0\n\ts_nop 0\n\tglobal_load_lds_dwordx4 %1, %2 offset:%3" ::"s"(lds), "v"(voff), "s"(base), "i"(OFF) : "memory");
}
template <int OFF> __device__ __forceinline__ void dma2(unsigned lds, unsigned voff0, unsigned voff1, const GAS char* base) {
    asm volatile("s_mov_b32 m0, %0\n\ts_nop 0\n\tglobal_load_lds_dwordx4 %1, %3\n\tglobal_load_lds_dwordx4 %2, %3 offset:%4"
                 ::"s"(lds), "v"(voff0), "v"(voff1), "s"(base), "i"(OFF) : "memory");
}
template <int OFF> __device__ __forceinline__ void dma2_sc1(unsigned lds, unsigned voff0, unsigned voff1, const GAS char* base) {
    asm volatile("s_mov_b32 m0, %0\n\ts_nop 0\n\tglobal_load_lds_dwordx4 %1, %3 sc1\n\tglobal_load_lds_dwordx4 %2, %3 offset:%4 sc1"
                 ::"s"(lds), "v"(voff0), "v"(voff1), "s"(base), "i"(OFF) : "memory");
}
__device__ __forceinline__ void touch(unsigned lds, const GAS char* a) {
    asm volatile("s_mov_b32 m0, %0\n\ts_nop 0\n\tglobal_load_lds_dword %1, off" ::"s"(lds), "v"(a) : "memory");
}
__device__ __forceinline__ void lds_st32(unsigned a, float v) { asm volatile("ds_write_b32 %0, %1" ::"v"(a), "v"(v) : "memory"); }
__device__ __forceinline__ void lds_st64(unsigned a, f32x2 v) { asm volatile("ds_write_b64 %0, %1" ::"v"(a), "v"(v) : "memory"); }
template <int CTRL, int RM> __device__ __forceinline__ float dpp_add(float v) {
    return v + __uint_as_float(__builtin_amdgcn_update_dpp(0u, __float_as_uint(v), CTRL, RM, 0xf, true));
}
__device__ __forceinline__ float wave_sum(float v) {
    v = dpp_add<0x111, 0xf>(v); v = dpp_add<0x112, 0xf>(v); v = dpp_add<0x114, 0xf>(v); v = dpp_add<0x118, 0xf>(v);
    v = dpp_add<0x142, 0xa>(v); v = dpp_add<0x143, 0xc>(v);
    return __uint_as_float(__builtin_amdgcn_readlane(__float_as_uint(v), 63));
}
__device__ __forceinline__ float sum_groups(float v) {
    const auto a = __builtin_amdgcn_permlane16_swap(__float_as_uint(v), __float_as_uint(v), false, false);
    v = __uint_as_float(a[0]) + __uint_as_float(a[1]);
    const auto b = __builtin_amdgcn_permlane32_swap(__float_as_uint(v), __float_as_uint(v), false, false);
    return __uint_as_float(b[0]) + __uint_as_float(b[1]);
}
__device__ __forceinline__ void store16_sc1(GAS void* p, u32x4 v) {
    asm volatile("global_store_dwordx4 %0, %1, off sc1\n\ts_nop 1" ::"v"(p), "v"(v) : "memory");
}
__device__ __forceinline__ void wait_flags(const GAS unsigned* f, unsigned tag) {
#pragma unroll 1
    for (unsigned spins = 0; spins < SPIN_CAP; ++spins) {
        const unsigned v = __hip_atomic_load(f + (threadIdx.x & 31), __ATOMIC_RELAXED, __HIP_MEMORY_SCOPE_AGENT);
        if (__all(v == tag)) break;
        __builtin_amdgcn_s_sleep(1);
    }
    asm volatile("" ::: "memory");
}
struct cf { float r, i; };
__device__ __forceinline__ cf cmul(cf a, cf b) { return cf{a.r * b.r - a.i * b.i, a.r * b.i + a.i * b.r}; }
__device__ __forceinline__ cf cfma(cf a, cf b, cf c) { return cf{fmaf(a.r, b.r, fmaf(-a.i, b.i, c.r)), fmaf(a.r, b.i, fmaf(a.i, b.r, c.i))}; }
__device__ __forceinline__ float exp_f(float x) {
    const float hi = x * 1.44269504f, lo = fmaf(x, 1.44269504f, -hi) + x * 1.92596299e-8f;
    return __builtin_amdgcn_exp2f(hi) * fmaf(lo, 0.693147181f, 1.0f);
}
__device__ __forceinline__ void sincos_f(float x, float& s, float& c) {
    const float k = rintf(x * 0.636619772f);
    float r = fmaf(k, -1.57079601e+00f, x);
    r = fmaf(k, -3.13916473e-07f, r);
    r = fmaf(k, -5.39030253e-15f, r);
    const float z = r * r;
    const float sp = fmaf(r * z, fmaf(z, fmaf(z, fmaf(z, 2.75573137e-06f, -1.98412698e-04f), 8.33333333e-03f), -1.66666667e-01f), r);
    const float cp = fmaf(z, fmaf(z, fmaf(z, fmaf(z, fmaf(z, -2.75573144e-07f, 2.48015873e-05f), -1.38888889e-03f), 4.16666667e-02f), -0.5f), 1.0f);
    const int q = (int)k;
    const float a = (q & 1) ? cp : sp, b = (q & 1) ? sp : cp;
    s = (q & 2) ? -a : a;
    c = ((q + 1) & 2) ? -b : b;
}
__device__ __forceinline__ unsigned row_std(float v) {
    const float s1 = rbf(__builtin_amdgcn_sqrtf(rbf(rbf(v) + LN_EPS)));
    const float r1 = __builtin_amdgcn_rcpf(s1);
    const float s2 = __builtin_amdgcn_sqrtf(rbf(rbf(v * r1 * r1) + LN_EPS));
    return pk_bf16(s1, s2);
}

typedef const unsigned short* __restrict__ bfp;

__global__ __launch_bounds__(512) void k_fused(bfp x, bfp lam_re, bfp lam_im, bfp bre, bfp bim, bfp cre, bfp cim, bfp log_dt, bfp dskip,
                                               bfp w1, bfp b1, bfp w2, bfp b2, char* ws_, float* __restrict__ out) {
    extern __shared__ __attribute__((aligned(16))) char smem_[];
    LAS char* smem = (LAS char*)smem_;
    GAS char* ws = (GAS char*)ws_;
    const int wave = __builtin_amdgcn_readfirstlane(threadIdx.x >> 6), lane = threadIdx.x & 63;
    const int g = lane >> 4, li = lane & 15;
    const int xg8 = blockIdx.x & 7, loc = blockIdx.x >> 3, ci = 4 * (xg8 >> 1) + (loc >> 3), cj = 8 * (xg8 & 1) + (loc & 7);
    const int t0 = 128 * ci, p0 = 32 * cj, f0 = cj < 11 ? 96 * cj : 1056 + 64 * (cj - 11), nfu = cj < 11 ? 6 : 4;
    const int T = 2 * ci + (cj >> 3), d0 = 64 * (cj & 7);
    const int rot = ((ci + 15) >> 1) & 7;
    unsigned long long entry_pc;
    asm volatile("s_getpc_b64 %0" : "=s"(entry_pc));
    unsigned epoch;
    asm volatile("global_load_dword %0, %1, off sc1" : "=v"(epoch) : "v"(ws + WS_EP) : "memory");
    const int q = li >> 2, pp = lane & 3;

    if (__builtin_expect(wave >= 4, 1)) {
        const int lw = wave - 4;
        const unsigned lds0 = (unsigned)(size_t)smem;
        unsigned xo[4], bo[2], wo[3];
#pragma unroll
        for (int i = 0; i < 4; ++i) {
            const int R = 8 * (4 * lw + i) + (lane >> 3), ii = R & 15, bb = (R >> 4) & 3;
            const int tok = t0 + (R & 64) + 16 * (ii >> 2) + 8 * (ii & 1) + 2 * bb + ((ii >> 1) & 1);
            xo[i] = tok * 1024 + 16 * ((lane & 7) ^ (lane >> 3)) + 3072 - 1024 * i;
        }
#pragma unroll
        for (int i = 0; i < 2; ++i) {
            const int R = 8 * (2 * lw + i) + (lane >> 3);
            bo[i] = (p0 + (R & 31)) * 1024 + 16 * ((lane & 7) ^ (lane >> 3)) + 3072 - 1024 * i;
        }
#pragma unroll
        for (int i = 0; i < 3; ++i) {
            const int st = 2 * (3 * lw + i) + (lane >> 5), kg = st / 3, ns = st - 3 * kg, kr = (lane & 31) >> 2;
            const int ch = 4 * ns + ((lane & 3) ^ ((2 * kg + (kr >> 2)) & 3));
            const int f = min(f0 + 8 * ch, FF - 8);
            wo[i] = (8 * kg + kr) * (FF * 2) + f * 2 + 3072 - 1024 * i;
        }
        const GAS char* xg = (const GAS char*)x - 3072;
        const GAS char* bg = (const GAS char*)(lw < 2 ? bre : bim) - 3072;
        const GAS char* wg = (const GAS char*)w1 - 3072;
        const unsigned lx = lds0 + P1_X + lw * 4096, lb = lds0 + P1_B + lw * 2048, lwb = lds0 + P1_W + lw * 3072;
        auto piece = [&](int t, auto I) {
            constexpr int i = decltype(I)::value;
            const unsigned so = (t & 3) * P1_SLOT;
            const int kt = (t + rot) & 7;
            if constexpr (i < 4) dma1<1024 * i>(lx + so, xo[i], xg + kt * 128);
            else if constexpr (i < 6) dma1<1024 * (i - 4)>(lb + so, bo[i - 4], bg + kt * 128);
            else dma1<1024 * (i - 6)>(lwb + so, wo[i - 6], wg + (size_t)kt * (64 * FF * 2));
        };
#define PC(t, i) piece(t, std::integral_constant<int, i>{})
        auto issue = [&](int t) { PC(t, 0); PC(t, 1); PC(t, 2); PC(t, 3); PC(t, 4); PC(t, 5); PC(t, 6); PC(t, 7); PC(t, 8); };
        issue(0); issue(1);
        touch(lds0 + L_DUMMY + 256 * lw, (const GAS char*)(entry_pc & ~255ull) + min(((loc & 3) * 4 + lw) * 13 + min(lane, 12), CODE_LINES - 1) * 128);

        unsigned w2o[2], co[2], ao[2];
#pragma unroll
        for (int i = 0; i < 2; ++i) {
            const int kg = 2 * lw + i;
            w2o[i] = (8 * kg + ((lane & 31) >> 2)) * (DM * 2) + (d0 + 32 * (lane >> 5) + 8 * (lane & 3)) * 2 + 3072 - 1056 * i;
            co[i] = (d0 + 8 * kg + (lane >> 3)) * (NP * 2) + 16 * ((lane & 7) ^ (lane >> 3)) + 3072 - 1024 * i;
            ao[i] = kg * 1024 + lane * 16 + 3072 - 1056 * i;
        }
        const GAS char* ag = ws + WS_ACT + (size_t)T * ACT_TILE;
        const GAS char* w2g = (const GAS char*)w2 - 3072;
        const unsigned l2 = lds0 + lw * 2080, l2c = lds0 + P2_IMG + lw * 2048;
        auto dmaA = [&](const GAS char* src, unsigned slot) { dma2_sc1<1056>(l2 + slot, ao[0], ao[1], src - 3072); };
        auto dmaW = [&](const GAS char* src, unsigned slot) { dma2<1056>(l2 + slot + P2_IMG, w2o[0], w2o[1], src); };
        auto dmaC = [&](const GAS char* src, unsigned slot) { dma2<1024>(l2c + slot, co[0], co[1], src); };
        const int ko = ((ci & 3) + cj) & 3;
        auto h_stage = [&](int s) { const int pr = s >> 1; return 2 * (pr < 8 ? (pr & ~3) | ((pr + ko) & 3) : 8 + ((pr + ko) & 1)) + (s & 1); };
        auto c_stage = [&](int s) { const int pr = s >> 1; return 2 * ((pr & 4) | ((pr + ko) & 3)) + (s & 1); };
        auto preW = [&](int s) { dmaW(w2g + h_stage(s) * 65536, s * P2_SLOT); };
        auto preA = [&](int s) { dmaA(ag + h_stage(s) * 8192, s * P2_SLOT); };

        const int n1 = 75 * blockIdx.x + 19 * lw + min(lane, 18);
        const GAS char* l1a = n1 < 11008 ? (const GAS char*)w2 + n1 * 128
                                         : (n1 < 15104 ? (const GAS char*)cre + (n1 - 11008) * 128 : (const GAS char*)cim + (min(n1, 19199) - 15104) * 128);
        const GAS u32x4* rsp = (const GAS u32x4*)(ws + WS_RS) + t0 + 64 * (lw & 1) + lane;
        const GAS u32x4* csp = (const GAS u32x4*)(ws + WS_CS) + ((cj * 16 + 8 * (lw >> 1) + 2 * g) * 2 + (lw & 1)) * 32 + li;
        asm volatile("" : "+v"(l1a), "+v"(rsp), "+v"(csp));
        const unsigned tag = epoch_tag<10>(epoch);
        bar();
        issue(2);
        touch(lds0 + L_DUMMY + 256 * lw, l1a);
        wait_vm<11>(); bar();
        issue(3);
        {
            f32x2 v;
            asm volatile("ds_read_b64 %0, %1\n\ts_waitcnt lgkmcnt(0)" : "=&v"(v) : "v"(lds0 + L_MAIL + 16 * lw + 8 * (lane & 1)) : "memory");
            if (lane < 2) store16_sc1((GAS u32x4*)(ws + WS_RS) + t0 + 8 * cj + 2 * lw + lane, u32x4{__float_as_uint(v[0]), tag, __float_as_uint(v[1]), tag});
        }
        wait_vm<11>(); bar();
        issue(4);
        {
            float a, b, c;
            const unsigned ma = lds0 + L_MAIL + 64 + 4 * (((lw >> 1) * 5 + 3 * (lw & 1)) * 16 + li);
            asm volatile("ds_read_b32 %0, %3\n\tds_read_b32 %1, %3 offset:64\n\tds_read_b32 %2, %3 offset:128\n\ts_waitcnt lgkmcnt(0)"
                         : "=&v"(a), "=&v"(b), "=&v"(c) : "v"(ma) : "memory");
            if (g == 0) store16_sc1((GAS u32x4*)(ws + WS_CS) + ((cj * 16 + ci) * 2 + (lw >> 1)) * 32 + 16 * (lw & 1) + li,
                                    u32x4{__float_as_uint(a), __float_as_uint(b), __float_as_uint(c), tag});
        }
        wait_vm<11>(); bar();
        issue(5); wait_vm<10>(); bar();
        issue(6); wait_vm<9>(); bar();
        issue(7);
        u32x4 rs, cs[4];
        auto gather = [&]() {
            asm volatile("global_load_dwordx4 %0, %5, off sc1\n\t"
                         "global_load_dwordx4 %1, %6, off sc1\n\tglobal_load_dwordx4 %2, %6, off offset:256 sc1\n\t"
                         "global_load_dwordx4 %3, %6, off offset:1024 sc1\n\tglobal_load_dwordx4 %4, %6, off offset:1280 sc1"
                         : "=&v"(rs), "=&v"(cs[0]), "=&v"(cs[1]), "=&v"(cs[2]), "=&v"(cs[3]) : "v"(rsp), "v"(csp) : "memory");
        };
        gather();
        wait_vm<14>(); bar();
        preW(0); preW(1); preW(2); preW(3);
        asm volatile("s_waitcnt vmcnt(8)" : "+v"(rs), "+v"(cs[0]), "+v"(cs[1]), "+v"(cs[2]), "+v"(cs[3])::"memory");
        bar();
        preW(4); preW(5);
        auto fresh = [&]() {
            return __all(rs[1] == tag && rs[3] == tag && cs[0][3] == tag && cs[1][3] == tag && cs[2][3] == tag && cs[3][3] == tag);
        };
#pragma unroll 1
        for (unsigned spins = 0; spins < SPIN_CAP && !fresh(); ++spins) {
            gather();
            asm volatile("s_waitcnt vmcnt(0)" : "+v"(rs), "+v"(cs[0]), "+v"(cs[1]), "+v"(cs[2]), "+v"(cs[3])::"memory");
        }
        if (lw < 2) {
            const float m = __uint_as_float(rs[0]), mb = rbf(m);
            const float r1 = __builtin_amdgcn_rcpf(bf_lo(rs[2])), r12 = r1 * __builtin_amdgcn_rcpf(bf_hi(rs[2]));
            const int tau = 64 * lw + lane;
            const unsigned ta = lds0 + L_TAB1 + 4 * (((tau >> 4) * 8 + (tau & 7)) * 4 + ((tau >> 3) & 1));
            lds_st32(ta, r1); lds_st32(ta + 8, -r1 * mb);
            lds_st32(ta + 1024, r12); lds_st32(ta + 1032, -r12 * m);
            lds_st64(lds0 + L_TOK + 8 * tau, f32x2{mb, r1});
        }
        {
            float v5[5];
#pragma unroll
            for (int k = 0; k < 3; ++k) v5[k] = __uint_as_float(cs[0][k]) + __uint_as_float(cs[2][k]);
#pragma unroll
            for (int k = 0; k < 2; ++k) v5[3 + k] = __uint_as_float(cs[1][k]) + __uint_as_float(cs[3][k]);
#pragma unroll
            for (int k = 0; k < 5; ++k) v5[k] = sum_groups(v5[k]);
            if (g == 0) {
#pragma unroll
                for (int k = 0; k < 5; ++k) lds_st32(lds0 + L_CS + 4 * ((((lw >> 1) * 2 + (lw & 1)) * 5 + k) * 16 + li), v5[k]);
            }
        }
        asm volatile("s_waitcnt lgkmcnt(0)" ::: "memory");
        bar();
        preW(6); preW(7); preW(8);
        bar();
        bar();
        wait_flags((const GAS unsigned*)(ws + WS_FH) + T * 32, tag);
        preA(0); preA(1); preA(2); preA(3);
        wait_vm<4>(); bar();
        preA(4); preA(5); preA(6); preA(7);
        wait_vm<8>(); bar();
        int pos = 9;
        unsigned slot = 0;
        auto nxt = [&]() { ++pos; slot = slot == P2_RING - P2_SLOT ? 0 : slot + P2_SLOT; };
        auto stW = [&]() { const int u = h_stage(pos); dmaA(ag + u * 8192, slot); dmaW(w2g + u * 65536, slot); nxt(); };
        auto stC = [&]() {
            const int u = c_stage(pos);
            dmaA(ag + (FF * 128 + u * 8192), slot);
            dmaC((const GAS char*)(u < 8 ? cre : cim) - 3072 + (u & 7) * 128, slot);
            nxt();
        };
        preA(8); stW();
        wait_vm<10>(); bar(); stW(); stW();
        wait_vm<14>(); bar(); stW(); stW();
#pragma unroll 1
        for (int j = 4; j < 7; ++j) { wait_vm<16>(); bar(); stW(); stW(); }
        wait_vm<16>(); bar();
        dmaA(ag + 20 * 8192, slot); dmaW(w2g + 20 * 65536, slot); nxt();
        dmaA(ag + 21 * 8192 - 4096, slot); dmaW(w2g + 21 * 65536 - 32768, slot); nxt();
        pos = 0;
        wait_vm<16>(); bar();
        stC(); stC();
#pragma unroll 1
        for (int j = 9; j < 16; ++j) { wait_vm<16>(); bar(); stC(); stC(); }
        wait_vm<16>(); bar();
        wait_vm<8>(); bar();
        wait_vm<0>(); bar();
        return;
    }

    const int wm = wave >> 1, wn = wave & 1;
    const int p = p0 + 16 * wn + li;
    cf lam = {0.0f, 0.0f}, l8 = lam, l16 = lam, l128 = lam, lsig = lam, coef2 = lam;
    float b1c[3], p_lr, p_lim, p_ldt;
    unsigned tag;
    {
        u32x4 xr[2];
        unsigned short pr[6];
#pragma unroll
        for (int i = 0; i < 2; ++i) xr[i] = *(const u32x4*)(x + (t0 + 8 * cj + 2 * wave + i) * DM + lane * 8);
        pr[0] = lam_re[p]; pr[1] = lam_im[p]; pr[2] = log_dt[p];
#pragma unroll
        for (int c = 0; c < 3; ++c) pr[3 + c] = b1[min(f0 + 16 * (3 * wn + c) + li, FF - 1)];
        SB0;
        p_lr = bf2f(pr[0]); p_lim = bf2f(pr[1]); p_ldt = bf2f(pr[2]);
#pragma unroll
        for (int c = 0; c < 3; ++c) b1c[c] = bf2f(pr[3 + c]);
        float mean[2], var[2];
#pragma unroll
        for (int i = 0; i < 2; ++i) {
            float e[8], s = 0.0f;
#pragma unroll
            for (int j = 0; j < 4; ++j) { e[2 * j] = bf_lo(xr[i][j]); e[2 * j + 1] = bf_hi(xr[i][j]); s += e[2 * j] + e[2 * j + 1]; }
            mean[i] = wave_sum(s) * (1.0f / DM);
            float qq = 0.0f;
#pragma unroll
            for (int j = 0; j < 8; ++j) qq = fmaf(e[j] - mean[i], e[j] - mean[i], qq);
            var[i] = wave_sum(qq) * (1.0f / DM);
        }
        if (lane < 2) ((LAS f32x2*)(smem + L_MAIL))[2 * wave + lane] = f32x2{lane ? mean[1] : mean[0], __uint_as_float(row_std(lane ? var[1] : var[0]))};
        asm volatile("" : "+v"(b1c[0]), "+v"(b1c[1]), "+v"(b1c[2]), "+v"(p_lr), "+v"(p_lim), "+v"(p_ldt));
    }
    auto lam_setup = [&]() {
        const float lr = p_lr, lim = p_lim, dt = rbf(exp_f(p_ldt));
        const float mag = exp_f(dt * lr);
        float sn, cs;
        sincos_f(dt * lim, sn, cs);
        lam = cf{mag * cs, mag * sn};
        const float nr = lam.r - 1.0f, ni = lam.i, den = 1.0f / (lr * lr + lim * lim);
        coef2 = cf{2.0f * (nr * lr + ni * lim) * den, 2.0f * (ni * lr - nr * lim) * den};
        const cf l2 = cmul(lam, lam), l4 = cmul(l2, l2);
        l8 = cmul(l4, l4); l16 = cmul(l8, l8);
        const cf l32 = cmul(l16, l16), l64 = cmul(l32, l32);
        l128 = cmul(l64, l64);
        lsig = (g & 1) ? l16 : cf{1.0f, 0.0f};
        if (g & 2) lsig = cmul(lsig, l32);
        if (wm) lsig = cmul(lsig, l64);
        asm volatile("" : "+v"(lam.r), "+v"(lam.i), "+v"(l8.r), "+v"(l8.i), "+v"(l16.r), "+v"(l16.i), "+v"(l128.r), "+v"(l128.i), "+v"(lsig.r), "+v"(lsig.i),
                          "+v"(coef2.r), "+v"(coef2.i));
    };
    LAS f32x4* tab1 = (LAS f32x4*)(smem + L_TAB1) + (wm * 4 + g) * 8;
    LAS f32x4* tab2 = (LAS f32x4*)(smem + L_TAB2) + (wm * 4 + g) * 8;
    auto csum = [&](int k) { return ((LAS float*)(smem + L_CS))[(wn * 5 + k) * 16 + li] + ((LAS float*)(smem + L_CS))[((2 + wn) * 5 + k) * 16 + li]; };
    f32x4 acc[4][5], tb[8];
    float s1c[3];
    {
        const int sw = 16 * (g ^ (lane & 7));
        const int xa0 = P1_X + (64 * wm + li) * 128 + sw;
        const int ba0 = P1_B + (16 * wn + li) * 128 + sw;
        int wa[2][2];
#pragma unroll
        for (int t = 0; t < 2; ++t)
#pragma unroll
            for (int cl = 0; cl < 2; ++cl) {
                const int par = (3 * wn + cl) & 1;
                wa[t][cl] = P1_W + 1536 * g + 64 * q + 256 * t + 32 * (par ^ (g & 1)) + 16 * ((pp >> 1) ^ t) + 8 * (pp & 1) + (cl == 0 ? 512 * wn : 1024 * wn);
            }
#pragma unroll
        for (int b = 0; b < 4; ++b)
#pragma unroll
            for (int c = 0; c < 5; ++c) acc[b][c] = f32x4{0, 0, 0, 0};
        s16x8 xa[2][4], wb[2][5];
        auto rd = [&](int slot, int ks, int buf) {
            LAS char* s = smem + slot * P1_SLOT;
            LAS char* xs = s + (xa0 ^ (64 * ks));
            LAS char* bs = s + (ba0 ^ (64 * ks));
#pragma unroll
            for (int b = 0; b < 4; ++b) xa[buf][b] = lds_row(xs + b * 2048);
            wb[buf][0] = lds_row(bs);
            wb[buf][1] = lds_row(bs + 4096);
            wb[buf][2] = lds_tr(s + wa[0][0] + 6144 * ks, s + wa[1][0] + 6144 * ks);
            wb[buf][3] = lds_tr(s + wa[0][1] + 6144 * ks, s + wa[1][1] + 6144 * ks);
            wb[buf][4] = lds_tr(s + wa[0][0] + 6144 * ks + 512, s + wa[1][0] + 6144 * ks + 512);
        };
        auto mm = [&](int buf) {
#pragma unroll
            for (int c = 0; c < 5; ++c)
#pragma unroll
                for (int b = 0; b < 4; ++b) acc[b][c] = mfma(xa[buf][b], wb[buf][c], acc[b][c]);
        };
        lam_setup();
        auto colsum = [&](int buf) {
            const s16x8 ones = {0x3f80, 0x3f80, 0x3f80, 0x3f80, 0x3f80, 0x3f80, 0x3f80, 0x3f80};
            float v5[5];
#pragma unroll
            for (int c = 0; c < 5; ++c) v5[c] = mfma(ones, wb[buf][c], f32x4{0, 0, 0, 0})[0];
            if (g == 0) {
#pragma unroll
                for (int c = 0; c < 5; ++c) ((LAS float*)(smem + L_MAIL + 64))[(wn * 5 + c) * 16 + li] = v5[c];
            }
        };
        tag = epoch_tag<0>(epoch);
        wait_lgkm0();
        bar();
        rd(0, 0, 0);
        wait_lgkm0();
#pragma unroll 1
        for (int seg = 0, t = 0; seg < 2; ++seg) {
            const int tend = seg ? 7 : 1;
#pragma unroll 1
            for (; t < tend; ++t) {
                SB0;
                rd(t & 3, 1, 1); mm(0); interleave<20, 12>();
                SB0; wait_lgkm0(); bar();
                rd((t + 1) & 3, 0, 0); mm(1); interleave<20, 12>();
                SB0; wait_lgkm0();
            }
            if (seg == 0 && wm == 0) { if (ci & 1) colsum(1); else colsum(0); }
        }
        rd(3, 1, 1); mm(0); interleave<20, 12>();
        SB0; wait_lgkm0(); bar();
#pragma unroll
        for (int j = 0; j < 8; ++j) tb[j] = tab2[j];
#pragma unroll
        for (int c = 0; c < 3; ++c) s1c[c] = csum(2 + c);
        mm(1);
        interleave<20, 14>();
        SB0;
    }

    u32x2 xv[2][2], dv[2], bv[2];
#pragma unroll
    for (int i = 0; i < 2; ++i) {
        const int d = d0 + 32 * (wave >> 1) + 16 * i + 4 * g;
        dv[i] = *(const u32x2*)(dskip + d); bv[i] = *(const u32x2*)(b2 + d);
#pragma unroll
        for (int j = 0; j < 2; ++j) xv[i][j] = *(const u32x2*)(x + (64 * T + 32 * j + 16 * ((lane >> 3) & 1) + 8 * (wave & 1) + (lane & 7)) * DM + d);
    }
    const int Tw = 2 * ci + wm;
    const __amdgpu_buffer_rsrc_t act = __builtin_amdgcn_make_buffer_rsrc((void*)(ws_ + WS_ACT), 0, 32 * ACT_TILE, 0x00020000);
    const unsigned act0 = Tw * ACT_TILE;
    {
        constexpr float C1 = -2.30220819f;
        constexpr float C2 = C1 * 0.044715f;
#pragma unroll
        for (int c = 0; c < 3; ++c) {
            const int col = 16 * (3 * wn + c) + li;
            f32x2 y[8];
#pragma unroll
            for (int j = 0; j < 8; ++j) {
                const int b = j >> 1, r = 2 * (j & 1);
                const f32x2 a = {tb[j][0], tb[j][1]}, bb = {tb[j][2], tb[j][3]};
                const f32x2 z = pfma(a, f32x2{acc[b][2 + c][r], acc[b][2 + c][r + 1]}, pfma(bb, splat(s1c[c]), splat(b1c[c])));
                const f32x2 u = z * pfma(z * z, splat(C2), splat(C1));
                const f32x2 d = f32x2{__builtin_amdgcn_exp2f(u[0]), __builtin_amdgcn_exp2f(u[1])} + splat(1.0f);
                y[j] = z * f32x2{__builtin_amdgcn_rcpf(d[0]), __builtin_amdgcn_rcpf(d[1])};
            }
            if (3 * wn + c < nfu) {
                const int k = f0 + col;
                const unsigned dst = act0 + (k >> 3) * 1024 + (k & 7) * 64 + 16 * g;
#pragma unroll
                for (int sgm = 0; sgm < 2; ++sgm)
                    __builtin_amdgcn_raw_buffer_store_b128(u32x4{pk_bf16(y[0][sgm], y[1][sgm]), pk_bf16(y[2][sgm], y[3][sgm]), pk_bf16(y[4][sgm], y[5][sgm]), pk_bf16(y[6][sgm], y[7][sgm])},
                                                           act, dst + 512 * sgm, 0, AUX_SC1);
            }
        }
    }

    f32x2 ure[8], uim[8];
    {
        const float sbr = csum(0), sbi = csum(1);
#pragma unroll
        for (int j = 0; j < 8; ++j) {
            const f32x4 tb = tab1[j];
            const f32x2 a = {tb[0], tb[1]}, bb = {tb[2], tb[3]};
            const int b = j >> 1, r = 2 * (j & 1);
            ure[j] = pfma(a, f32x2{acc[b][0][r], acc[b][0][r + 1]}, bb * splat(sbr));
            uim[j] = pfma(a, f32x2{acc[b][1][r], acc[b][1][r + 1]}, bb * splat(sbi));
        }
    }
    f32x2 hr = ure[0], hi = uim[0];
#pragma unroll
    for (int j = 1; j < 8; ++j) {
        const f32x2 tr = pfma(splat(lam.r), hr, ure[j]), ti = pfma(splat(lam.r), hi, uim[j]);
        const f32x2 nr = pfma(splat(-lam.i), hi, tr), ni = pfma(splat(lam.i), hr, ti);
        hr = nr; hi = ni;
    }
    const cf eA = cf{hr[0], hi[0]};
    const cf e = cfma(l8, eA, cf{hr[1], hi[1]});
    const int sig = 4 * wm + g;
    LAS f32x2* eL = (LAS f32x2*)(smem + L_EL);
    eL[sig * 32 + 16 * wn + li] = f32x2{e.r, e.i};
    wait_lgkm0();
    bar();
    cf Lp = cf{0.0f, 0.0f};
#pragma unroll
    for (int s = 0; s < 7; ++s) {
        const f32x2 v = eL[s * 32 + 16 * wn + li];
        const cf n = cfma(l16, Lp, cf{v[0], v[1]});
        if (s < sig) Lp = n;
    }
    wait_vm<0>();
    if (lane == 0) __hip_atomic_store((GAS unsigned*)(ws + WS_FH) + Tw * 32 + 2 * cj + wn, tag, __ATOMIC_RELAXED, __HIP_MEMORY_SCOPE_AGENT);
    GAS u32x4* eg = (GAS u32x4*)(ws + WS_EG);
    if (sig == 7) {
        const cf E = cfma(l16, Lp, e);
        store16_sc1(eg + ci * NP + p, u32x4{__float_as_uint(E.r), tag, __float_as_uint(E.i), tag});
    }
    bar();

    const int wd = wave >> 1, wt = wave & 1;
    f32x4 o[2][2];
    cf Hin = cf{0.0f, 0.0f};
    {
#pragma unroll
        for (int j = 0; j < 2; ++j) {
            const f32x2 st = ((LAS f32x2*)(smem + L_TOK))[64 * (T & 1) + 32 * j + 16 * ((lane >> 3) & 1) + 8 * wt + (lane & 7)];
#pragma unroll
            for (int i = 0; i < 2; ++i) {
                const float xe[4] = {bf_lo(xv[i][j][0]), bf_hi(xv[i][j][0]), bf_lo(xv[i][j][1]), bf_hi(xv[i][j][1])};
                const float de[4] = {bf_lo(dv[i][0]), bf_hi(dv[i][0]), bf_lo(dv[i][1]), bf_hi(dv[i][1])};
                const float be[4] = {bf_lo(bv[i][0]), bf_hi(bv[i][0]), bf_lo(bv[i][1]), bf_hi(bv[i][1])};
#pragma unroll
                for (int k = 0; k < 4; ++k) {
                    const float xn = rbf(rbf(xe[k] - st[0]) * st[1]);
                    o[i][j][k] = xe[k] + rbf(xn * de[k]) + be[k];
                }
            }
        }
        asm volatile("" : "+v"(o[0][0]), "+v"(o[0][1]), "+v"(o[1][0]), "+v"(o[1][1]));
        const GAS u32x4* ep = eg + g * NP + p;
        u32x4 gr[4];
        auto fetch = [&]() {
            asm volatile("global_load_dwordx4 %0, %4, off sc1\n\tglobal_load_dwordx4 %1, %5, off sc1\n\t"
                         "global_load_dwordx4 %2, %6, off sc1\n\tglobal_load_dwordx4 %3, %7, off sc1"
                         : "=&v"(gr[0]), "=&v"(gr[1]), "=&v"(gr[2]), "=&v"(gr[3])
                         : "v"(ep), "v"(ep + 4 * NP), "v"(ep + 8 * NP), "v"(ep + 12 * NP) : "memory");
        };
        auto landed = [&]() { asm volatile("s_waitcnt vmcnt(0)" : "+v"(gr[0]), "+v"(gr[1]), "+v"(gr[2]), "+v"(gr[3])::"memory"); };
        auto complete = [&]() {
            bool ok = true;
#pragma unroll
            for (int i = 0; i < 4; ++i) ok &= (4 * i + g >= ci) | ((gr[i][1] == tag) & (gr[i][3] == tag));
            return __all(ok);
        };
        fetch();
        const cf l256 = cmul(l128, l128), l512 = cmul(l256, l256);
        landed();
#pragma unroll 1
        for (unsigned spins = 0; spins < SPIN_CAP && !complete(); ++spins) { __builtin_amdgcn_s_sleep(2); fetch(); landed(); }
        cf hp = cf{0.0f, 0.0f};
#pragma unroll
        for (int i = 0; i < 4; ++i) {
            const cf n = cfma(l512, hp, cf{__uint_as_float(gr[i][0]), __uint_as_float(gr[i][2])});
            if (4 * i + g < ci) hp = n;
        }
        const int rr = (ci - 1 - g) & 3;
        if (rr & 1) hp = cmul(hp, l128);
        if (rr & 2) hp = cmul(hp, l256);
        Hin = cf{sum_groups(hp.r), sum_groups(hp.i)};
    }
    u32x4 hq[2][2];
    unsigned hdr, hdi;
    auto store_h = [&]() {
#pragma unroll
        for (int sgm = 0; sgm < 2; ++sgm) {
            __builtin_amdgcn_raw_buffer_store_b128(hq[0][sgm], act, hdr + 512 * sgm, 0, AUX_SC1);
            __builtin_amdgcn_raw_buffer_store_b128(hq[1][sgm], act, hdi + 512 * sgm, 0, AUX_SC1);
        }
    };
    {
        const cf cA = cfma(lsig, Hin, Lp);
        const cf cB = cfma(l8, cA, eA);
        f32x2 sr = {cA.r, cB.r}, si = {cA.i, cB.i};
        f32x2 ore[8], oim[8];
#pragma unroll
        for (int j = 0; j < 8; ++j) {
            const f32x2 tr = pfma(splat(lam.r), sr, ure[j]), ti = pfma(splat(lam.r), si, uim[j]);
            const f32x2 nr = pfma(splat(-lam.i), si, tr), ni = pfma(splat(lam.i), sr, ti);
            sr = nr; si = ni;
            ore[j] = pfma(splat(coef2.r), sr, splat(-coef2.i) * si);
            oim[j] = pfma(splat(-coef2.r), si, splat(-coef2.i) * sr);
        }
        const int kr = FF + p, kim = FF + NP + p;
        hdr = act0 + (kr >> 3) * 1024 + (kr & 7) * 64 + 16 * g;
        hdi = act0 + (kim >> 3) * 1024 + (kim & 7) * 64 + 16 * g;
#pragma unroll
        for (int sgm = 0; sgm < 2; ++sgm) {
            hq[0][sgm] = u32x4{pk_bf16(ore[0][sgm], ore[1][sgm]), pk_bf16(ore[2][sgm], ore[3][sgm]), pk_bf16(ore[4][sgm], ore[5][sgm]), pk_bf16(ore[6][sgm], ore[7][sgm])};
            hq[1][sgm] = u32x4{pk_bf16(oim[0][sgm], oim[1][sgm]), pk_bf16(oim[2][sgm], oim[3][sgm]), pk_bf16(oim[4][sgm], oim[5][sgm]), pk_bf16(oim[6][sgm], oim[7][sgm])};
        }
        store_h();
    }

    {
        const int trb = (g >> 1) * 2080 + (g & 1) * 1056 + 64 * q + 16 * (pp >> 1) + 8 * (pp & 1);
        const int tra = trb + 512 * wt, trw = P2_IMG + trb + 512 * wd;
        const int ca0 = P2_IMG + (32 * wd + li) * 128 + 16 * (g ^ (lane & 7));
        s16x8 wf[2][2][2], af[2][2][2];
        int slot = 0;
        auto next = [&]() { slot = slot == P2_RING - P2_SLOT ? 0 : slot + P2_SLOT; };
        auto frag_a = [&](LAS char* sl, int buf, int ks, int i) { LAS char* a = sl + tra + 4160 * ks + 32 * i; af[buf][ks][i] = lds_tr(a, a + 256); };
        auto frag_w = [&](LAS char* sl, int buf, int ks, int i, bool g4) {
            if (g4) { LAS char* a = sl + trw + 4160 * ks + 32 * i; wf[buf][ks][i] = lds_tr(a, a + 256); }
            else wf[buf][ks][i] = lds_row(sl + ((ca0 ^ (64 * ks)) + 2048 * i));
        };
        auto half = [&](int rb, bool rd, bool g4, int k0, bool mm_) {
            LAS char* sl = smem + slot;
            const int ub = rb ^ 1;
#pragma unroll
            for (int ks = 0; ks < 2; ++ks) {
#pragma unroll
                for (int m = 0; m < 4; ++m) {
                    if (mm_ && ks >= k0) mfma_acc(o[m >> 1][m & 1], wf[ub][ks][m >> 1], af[ub][ks][m & 1]);
                    SB0;
                    if (rd) { if (m < 2) frag_a(sl, rb, ks, m); else frag_w(sl, rb, ks, m - 2, g4); }
                    SB0;
                }
            }
            if (rd) next();
        };
        const GAS unsigned* fsp = (const GAS unsigned*)(ws + WS_FS) + T * 32 + (lane & 31);
        const unsigned fsl = (unsigned)(size_t)smem + L_EL + 256 * wave;
        bar(); half(0, true, true, 0, false);
        half(1, true, true, 0, true);
#pragma unroll 1
        for (int seg = 0, j = 1; seg < 4; ++seg) {
            const int jend = seg == 0 ? 2 : (seg == 1 ? 7 : (seg == 2 ? 8 : 10));
#pragma unroll 1
            for (; j < jend; ++j) {
                bar(); half(0, true, true, 0, true);
                half(1, true, true, 0, true);
            }
            if (seg == 0) {
                wait_vm<0>();
                if (lane == 0) __hip_atomic_store((GAS unsigned*)(ws + WS_FS) + Tw * 32 + 2 * cj + wn, tag, __ATOMIC_RELAXED, __HIP_MEMORY_SCOPE_AGENT);
            } else if (seg == 1) {
                asm volatile("s_mov_b32 m0, %0\n\ts_nop 0\n\tglobal_load_lds_dword %1, off sc1" ::"s"(fsl), "v"(fsp) : "memory");
            } else if (seg == 2) {
                unsigned fsv;
                asm volatile("s_waitcnt vmcnt(0)\n\tds_read_b32 %0, %1\n\ts_waitcnt lgkmcnt(0)" : "=&v"(fsv) : "v"(fsl + 4 * lane) : "memory");
                if (!__all(fsv == tag)) wait_flags((const GAS unsigned*)(ws + WS_FS) + T * 32, tag);
            }
            SB0;
        }
        bar(); half(0, true, true, 0, true);
        half(1, true, true, 0, true);
        bar(); half(0, true, false, 1, true);
        half(1, true, false, 0, true);
#pragma unroll 1
        for (int j = 12; j < 19; ++j) {
            bar(); half(0, true, false, 0, true);
            half(1, true, false, 0, true);
        }
        half(0, false, false, 0, true);
        asm volatile("s_nop 7\n\ts_nop 7" : "+v"(o[0][0]), "+v"(o[0][1]), "+v"(o[1][0]), "+v"(o[1][1]));

#pragma unroll
        for (int j = 0; j < 2; ++j)
#pragma unroll
            for (int i = 0; i < 2; ++i)
                *(f32x4*)(out + (64 * T + 32 * j + 16 * ((lane >> 3) & 1) + 8 * wt + (lane & 7)) * DM + d0 + 32 * wd + 16 * i + 4 * g) = o[i][j];
        if (ci == 15 && cj == 0 && threadIdx.x == 0) __hip_atomic_store((GAS unsigned*)(ws + WS_EP), tag, __ATOMIC_RELAXED, __HIP_MEMORY_SCOPE_AGENT);
    }
}

}

extern "C" void kernel_launch(void* const* d_in, const int* in_sizes, int n_in, void* d_out, int out_size, void* d_ws, size_t ws_size,
                              hipStream_t stream) {
    typedef const unsigned short* P;
    (void)hipFuncSetAttribute((const void*)k_fused, hipFuncAttributeMaxDynamicSharedMemorySize, LDS_BYTES);
    k_fused<<<256, 512, LDS_BYTES, stream>>>((P)d_in[0], (P)d_in[1], (P)d_in[2], (P)d_in[3], (P)d_in[4], (P)d_in[5], (P)d_in[6], (P)d_in[7],
                                             (P)d_in[8], (P)d_in[10], (P)d_in[11], (P)d_in[12], (P)d_in[13], (char*)d_ws, (float*)d_out);
}
```
